```python
import torch

def _softmax(x, axis=-1):
    return torch.softmax(x, dim=axis)
def _gelu(x):
    return torch.nn.functional.gelu(x, approximate="tanh")
def _silu(x):
    return torch.nn.functional.silu(x)
def _sigmoid(x):
    return torch.sigmoid(x)
def _layernorm(x, axis=-1, eps=1e-5):
    m = x.mean(dim=axis, keepdim=True)
    v = x.var(dim=axis, keepdim=True, unbiased=False)
    return (x - m) / torch.sqrt(v + eps)
def _scatter_add(tgt, idx, src, axis=0):
    return tgt.clone().index_add_(axis, idx, src)
def _segment_sum(src, seg, n):
    out = torch.zeros((n,) + src.shape[1:], dtype=src.dtype)
    return out.index_add_(0, seg, src)
def _pad(x, pw):
    flat = tuple(v for pair in reversed(pw) for v in pair)
    return torch.nn.functional.pad(x, flat)

kernel_name = 'gated_mix__grouped_kv_mqa__dense_activation_relu__prompt'

def setup_inputs():

    _w = (lambda a: (lambda t: t.to(torch.bfloat16) if t.is_floating_point() else t)(torch.as_tensor(a)))
    def _A_setup():
        B, S, D, H = 1, 2048, 512, 32
        Dh = D // H
        return {
            "x": _w((lambda *s: torch.randn(*s, dtype=torch.bfloat16) * 0.05)(B, S, D)),
            "wq": _w((lambda *s: torch.randn(*s, dtype=torch.bfloat16) * 0.8)(D, H * Dh)),
            "wk": _w((lambda *s: torch.randn(*s, dtype=torch.bfloat16) * 0.8)(D, Dh)),
            "wv": _w((lambda *s: torch.randn(*s, dtype=torch.bfloat16) * 0.2)(D, Dh)),
            "wo": _w((lambda *s: torch.randn(*s, dtype=torch.bfloat16) * 0.2)(H * Dh, D)),
        }
    _A_in = _A_setup()

    def _B_setup():
        B, S, D, Dff = 1, 2048, 512, 1792
        return {
            "x": _w((lambda *s: torch.randn(*s, dtype=torch.bfloat16) * 0.05)(B, S, D)),
            "w1": _w((lambda *s: torch.randn(*s, dtype=torch.bfloat16) * 0.05)(D, Dff)),
            "w2": _w((lambda *s: torch.randn(*s, dtype=torch.bfloat16) * 0.05)(Dff, D)),
        }
    _B_in = _B_setup()

    return {
        "A_x": _A_in["x"],
        "A_wq": _A_in["wq"],
        "A_wk": _A_in["wk"],
        "A_wv": _A_in["wv"],
        "A_wo": _A_in["wo"],
        "B_x": _B_in["x"],
        "B_w1": _B_in["w1"],
        "B_w2": _B_in["w2"],
        "gate_w": _w((lambda *s: torch.randn(*s, dtype=torch.bfloat16) * 0.05)(512) * 16.0),
    }

def reference(A_x, A_wq, A_wk, A_wv, A_wo, B_x, B_w1, B_w2, gate_w):
    def _A_ref(x, wq, wk, wv, wo):
        B, S, D = x.shape
        H = 32
        Dh = D // H
        q = (lambda x, *d: x.permute(*d))((x @ wq).reshape(B, S, H, Dh), 0, 2, 1, 3)
        k = x @ wk
        v = x @ wv
        scores = torch.einsum("bhqd,bkd->bhqk", q, k) * (Dh ** -0.5)
        p = _softmax(scores, axis=-1)
        o = torch.einsum("bhqk,bkd->bhqd", p, v)
        return (lambda x, *d: x.permute(*d))(o, 0, 2, 1, 3).reshape(B, S, H * Dh) @ wo

    def _B_ref(x, w1, w2):
        h = torch.einsum("bsd,df->bsf", x, w1)
        zero = torch.zeros_like(h)
        h = torch.maximum(h, zero)
        return torch.einsum("bsf,fd->bsd", h, w2)

    x = A_x
    g = _sigmoid((x * gate_w).sum(-1, keepdims=True))
    a = _A_ref(x, A_wq, A_wk, A_wv, A_wo)
    b = _B_ref(x, B_w1, B_w2)
    return g * a + (1 - g) * b
```

```cpp
#include <hip/hip_runtime.h>
#include <stdint.h>

typedef short s16x8 __attribute__((ext_vector_type(8)));
typedef short s16x4 __attribute__((ext_vector_type(4)));
typedef float f32x16 __attribute__((ext_vector_type(16)));
typedef float f32x4 __attribute__((ext_vector_type(4)));
typedef float f32x2 __attribute__((ext_vector_type(2)));
typedef unsigned u32x4 __attribute__((ext_vector_type(4)));
typedef __bf16 bf16x2 __attribute__((ext_vector_type(2)));
#define LAS __attribute__((address_space(3)))
#define GAS __attribute__((address_space(1)))
typedef GAS unsigned gu32;
typedef GAS unsigned long long gu64;
typedef LAS char* lptr;
#define DI __device__ __forceinline__
#define RLX_AGENT __ATOMIC_RELAXED, __HIP_MEMORY_SCOPE_AGENT
#define MFMA(a, b, c) __builtin_amdgcn_mfma_f32_32x32x16_bf16((a), (b), (c), 0, 0, 0)
#define SBAR() __builtin_amdgcn_sched_barrier(0)
#define PIN(x) asm volatile("" : "+v"(x))
#define CFENCE() do { asm volatile("" ::: "memory"); __builtin_amdgcn_fence(__ATOMIC_ACQUIRE, "wavefront"); } while (0)
#define BAR_L() asm volatile("s_waitcnt lgkmcnt(0)\n\ts_barrier" ::: "memory")
#define BAR_VL(N) asm volatile("s_waitcnt vmcnt(" #N ") lgkmcnt(0)\n\ts_barrier" ::: "memory")

#define ABL 0
#define STAMPS 0

constexpr int S = 2048, D = 512, DFF = 1792;
constexpr float QSCALE = 0.25f * 1.4426950408889634f;
constexpr float SAFE_RANGE = 64.f;
constexpr unsigned WS_EPOCH = 0, WS_KVFLAG = 256, WS_HFLAG = 1024;
constexpr unsigned WS_KV = 4096, KV_BYTES = 64 * 2048;
constexpr unsigned WS_H = WS_KV + KV_BYTES, H_BYTES = 64 * 112 * 1024;
constexpr unsigned WS_O = WS_H + H_BYTES, O_BYTES = 64 * 32 * 1024;
constexpr unsigned WS_STAMP = WS_O + O_BYTES;
constexpr unsigned L_STAGE = 40960;
constexpr unsigned L_WO = 0, L_W2 = 65536, L_RED = 65536;
constexpr unsigned KVT = 2080, KVSLOT = 4 * KVT, L_KV = 122880;
constexpr unsigned L_QX = 147968, L_MISC = L_QX + 8192, LDS_BYTES = L_MISC + 256;
constexpr unsigned SPIN_CAP = 1u << 16;

DI unsigned cvtpk(float a, float b) { f32x2 v = {a, b}; return __builtin_bit_cast(unsigned, __builtin_convertvector(v, bf16x2)); }
DI float bf_lo(unsigned u) { return __uint_as_float(u << 16); }
DI float bf_hi(unsigned u) { return __uint_as_float(u & 0xffff0000u); }
DI float rbf(float x) { return bf_lo(cvtpk(x, x)); }
DI s16x8 asfrag(u32x4 v) { return __builtin_bit_cast(s16x8, v); }
DI s16x8 lds_frag(lptr p) { return *(const LAS s16x8*)p; }
DI s16x4 lds_tr(lptr p) { return __builtin_amdgcn_ds_read_tr16_b64_v4i16((LAS s16x4*)p); }
DI s16x8 tr_frag(lptr p, int second) { s16x4 lo = lds_tr(p), hi = lds_tr(p + second); return __builtin_shufflevector(lo, hi, 0, 1, 2, 3, 4, 5, 6, 7); }
DI float swap_add(float v) { auto r = __builtin_amdgcn_permlane32_swap(__float_as_uint(v), __float_as_uint(v), false, false); return __uint_as_float(r[0]) + __uint_as_float(r[1]); }
DI float swap_max(float v) { auto r = __builtin_amdgcn_permlane32_swap(__float_as_uint(v), __float_as_uint(v), false, false); return fmaxf(__uint_as_float(r[0]), __uint_as_float(r[1])); }
DI void glds16(const void* sbase, unsigned voff, unsigned lds_base) {
    unsigned keep;
    asm volatile("s_mov_b32 %0, m0\n\ts_mov_b32 m0, %3\n\ts_nop 0\n\tglobal_load_lds_dwordx4 %1, %2\n\ts_mov_b32 m0, %0"
                 : "=&s"(keep) : "v"(voff), "s"(sbase), "s"(lds_base) : "memory");
}
DI __amdgpu_buffer_rsrc_t mk_rsrc(const void* p, unsigned bytes) { return __builtin_amdgcn_make_buffer_rsrc((void*)p, 0, bytes, 0x00020000); }
DI u32x4 bload(__amdgpu_buffer_rsrc_t r, unsigned voff, unsigned soff) { return __builtin_amdgcn_raw_buffer_load_b128(r, voff, soff, 0); }
DI u32x4 bload_sc1(__amdgpu_buffer_rsrc_t r, unsigned voff, unsigned soff) { return __builtin_amdgcn_raw_buffer_load_b128(r, voff, soff, 16); }
DI void bstore_sc1(u32x4 v, __amdgpu_buffer_rsrc_t r, unsigned voff) { __builtin_amdgcn_raw_buffer_store_b128(v, r, voff, 0, 16); }

#define STAMP(i) do { } while (0)

#define LOGTMO(code, val) do { } while (0)
DI unsigned wait_flags(gu64* f, int idx, unsigned epoch, char* ws, int code) {
    unsigned long long v;
    for (unsigned spins = 0;; ++spins) {
        v = __hip_atomic_load(f + idx, RLX_AGENT);
        if (__all((unsigned)(v >> 32) == epoch)) break;
        if (spins > SPIN_CAP) { if ((unsigned)(v >> 32) != epoch) LOGTMO(code, (unsigned)(v >> 32)); break; }
        __builtin_amdgcn_s_sleep(2);
    }
    CFENCE();
    return (unsigned)v;
}

template <bool HEAVY>
DI void ffn1_stage(lptr sb, unsigned xo, unsigned sw, unsigned w1o0, unsigned w1o1, unsigned wqo, f32x16& a0, f32x16& a1, f32x16& aq) {
#pragma unroll
    for (int ks = 0; ks < 4; ++ks) {
        const s16x8 xf = lds_frag(sb + xo + ((ks ^ sw) << 5));
        const s16x8 f0 = tr_frag(sb + w1o0 + ks * 4096, 1024);
        const s16x8 f1 = tr_frag(sb + w1o1 + ks * 4096, 1024);
        a0 = MFMA(f0, xf, a0);
        a1 = MFMA(f1, xf, a1);
        if (HEAVY) { const s16x8 fq = tr_frag(sb + wqo + ks * 2048, 512); aq = MFMA(fq, xf, aq); }
    }
}

__global__ void __launch_bounds__(512, 2)
fused_kernel(const uint16_t* __restrict__ x, const uint16_t* __restrict__ wq, const uint16_t* __restrict__ wk, const uint16_t* __restrict__ wv,
             const uint16_t* __restrict__ wo, const uint16_t* __restrict__ w1, const uint16_t* __restrict__ w2, const uint16_t* __restrict__ gw,
             uint16_t* out, char* ws) {
    __shared__ __attribute__((aligned(16))) char smem[LDS_BYTES];
    const lptr lds = (lptr)smem;
    const unsigned lds0 = (unsigned)(uintptr_t)smem;
    const int tid = threadIdx.x, lane = tid & 63, w = __builtin_amdgcn_readfirstlane(tid >> 6);
    const int r = lane & 31, h = lane >> 5, g1 = (lane >> 4) & 1, q4 = (lane & 15) >> 2, p4 = lane & 3;
    const int b = blockIdx.x, xcd = b & 7, bi = b >> 3, half = xcd & 1;
    const int R = 4 * (xcd >> 1) + (bi >> 3), j = bi & 7, rh64 = j & 1, hg = 4 * half + (j >> 1);
    const bool kvcu = (j == 6 + half);
    const int cb = half ? 7 + j : (j == 7 ? 6 : j);
    const int row64 = R * 128 + rh64 * 64;
    const int rt = w & 1, hq = w >> 1;
    STAMP(0);

    const __amdgpu_buffer_rsrc_t rs_kv = mk_rsrc(ws + WS_KV, KV_BYTES), rs_h = mk_rsrc(ws + WS_H, H_BYTES), rs_o = mk_rsrc(ws + WS_O, O_BYTES);
    const unsigned o_own = (unsigned)(((row64 >> 5) + rt) * 32 + hg * 4 + hq) * 1024u;
    const unsigned epoch_v = __hip_atomic_load((gu32*)(ws + WS_EPOCH), RLX_AGENT) + 1u;
    bstore_sc1((u32x4){~0u, ~0u, ~0u, ~0u}, rs_o, lane * 16 + o_own);

    f32x16 acc0 = {}, acc1 = {}, accq = {};
    const unsigned tr_in = 32 * g1 + 8 * p4;
    const unsigned wq_voff = (unsigned)((8 * w + (lane >> 3)) * 1024 + hg * 128 + (((lane & 7) ^ (((lane >> 4) & 1) << 2)) << 4));
    bool heavy;
    if (!kvcu) {
        const int rb = 2 * (w >> 2) + (w & 1), fh = (w >> 1) & 1;
        heavy = (w >> 2) == rh64;
        const unsigned xv0 = (unsigned)((R * 128 + 16 * w + (lane >> 3)) * 1024 + (((lane & 7) ^ ((lane >> 4) & 3)) << 4));
        const unsigned xv1 = (unsigned)((R * 128 + 16 * w + 8 + (lane >> 3)) * 1024 + (((lane & 7) ^ (4 + ((lane >> 4) & 3))) << 4));
        const unsigned wv0 = (unsigned)((8 * w + (lane >> 4)) * (DFF * 2) + cb * 256 + (((lane & 15) ^ (((lane >> 4) & 3) << 2)) << 4));
        auto issue = [&](int s_, unsigned slot) {
            const int s = (ABL & 16) ? (s_ + j) & 7 : s_;
            const char* xb = (const char*)x + s * 128; const char* w1b = (const char*)w1 + (size_t)s * 64 * DFF * 2; const char* wqb = (const char*)wq + s * 65536;
            const unsigned d = lds0 + slot;
            glds16(xb, xv0, d + w * 2048); glds16(xb, xv1, d + w * 2048 + 1024);
            glds16(w1b, wv0, d + 16384 + w * 2048); glds16(w1b, wv0 + 4 * DFF * 2, d + 16384 + w * 2048 + 1024);
            glds16(wqb, wq_voff, d + 32768 + w * 1024);
        };
        issue(0, 0); issue(1, L_STAGE); issue(2, 2 * L_STAGE);
        const int xrow = 32 * rb + r;
        const unsigned sw = (xrow >> 1) & 7;
        const unsigned xo = xrow * 128 + ((h ^ (sw & 1)) << 4);
        const unsigned w1o0 = 16384 + (8 * h + q4) * 256 + (((2 * fh) ^ q4) << 6) + tr_in, w1o1 = 16384 + (8 * h + q4) * 256 + (((2 * fh + 1) ^ q4) << 6) + tr_in;
        const unsigned wqo = 32768 + (8 * h + q4) * 128 + ((fh ^ (q4 >> 1)) << 6) + tr_in;
        unsigned cur = 0, fill = 2 * L_STAGE;
#pragma unroll 1
        for (int s = 0; s < 8; ++s) {
            if (s == 0) BAR_VL(10); else if (s < 7) BAR_VL(5); else BAR_VL(0);
            if (s >= 1 && s < 6) issue(s + 2, fill);
            if (heavy) ffn1_stage<true>(lds + cur, xo, sw >> 1, w1o0, w1o1, wqo, acc0, acc1, accq);
            else ffn1_stage<false>(lds + cur, xo, sw >> 1, w1o0, w1o1, wqo, acc0, acc1, accq);
            fill = cur; cur = (cur == 2 * L_STAGE) ? 0 : cur + L_STAGE;
        }
        STAMP(1);
        const unsigned hb = (unsigned)((R * 4 + rb) * 112 + cb * 8 + fh * 4) * 1024u;
#pragma unroll
        for (int t = 0; t < 2; ++t) {
            const f32x16& a = t ? acc1 : acc0;
#pragma unroll
            for (int s = 0; s < 2; ++s) {
                u32x4 v;
#pragma unroll
                for (int e = 0; e < 4; ++e) v[e] = cvtpk(fmaxf(a[8 * s + 2 * e], 0.f), fmaxf(a[8 * s + 2 * e + 1], 0.f));
                bstore_sc1(v, rs_h, lane * 16 + hb + (t * 2 + s) * 1024);
            }
        }
    } else {
        heavy = w < 4;
        const unsigned xv0 = (unsigned)((row64 + 8 * w + (lane >> 3)) * 1024 + (((lane & 7) ^ ((4 * w + (lane >> 4)) & 7)) << 4));
        const char* wkv = (const char*)((w & 2) ? wk : wv);
        const unsigned kvd = 16384 + ((w & 2) ? 2048 + 128 : 0) + (w & 1) * 1024;
        auto issue = [&](int s_, unsigned slot) {
            const int s = (ABL & 16) ? (s_ + j) & 7 : s_;
            const unsigned d = lds0 + slot;
            glds16((const char*)x + s * 128, xv0, d + w * 1024);
            glds16(wkv + s * 2048, (w & 1) * 1024 + lane * 16, d + kvd);
            glds16((const char*)wq + s * 65536, wq_voff, d + 32768 + w * 1024);
        };
        issue(0, 0); issue(1, L_STAGE); issue(2, 2 * L_STAGE);
        const int xrow = 32 * (w & 1) + r, fh = (w >> 1) & 1;
        const unsigned sw = (xrow >> 1) & 7;
        const unsigned xo = xrow * 128 + ((h ^ (sw & 1)) << 4);
        const unsigned wqo = 32768 + (8 * h + q4) * 128 + ((fh ^ (q4 >> 1)) << 6) + tr_in;
        const unsigned wo_ = 16384 + g1 * (2048 + 128) + (8 * h + q4) * 32 + 8 * p4;
        unsigned cur = 0, fill = 2 * L_STAGE;
#pragma unroll 1
        for (int s = 0; s < 8; ++s) {
            if (s == 0) BAR_VL(6); else if (s < 7) BAR_VL(3); else BAR_VL(0);
            if (s >= 1 && s < 6) issue(s + 2, fill);
            const lptr sb = lds + cur;
#pragma unroll
            for (int ks = 0; ks < 4; ++ks) {
                const s16x8 xf = lds_frag(sb + xo + ((ks ^ (sw >> 1)) << 5));
                if (heavy) { const s16x8 f = tr_frag(sb + wqo + ks * 2048, 512); accq = MFMA(f, xf, accq); }
                else { const s16x8 f = tr_frag(sb + wo_ + ks * 512, 128);
                       if (fh == 0) acc0 = MFMA(f, xf, acc0); else acc0 = MFMA(xf, f, acc0); }
            }
            fill = cur; cur = (cur == 2 * L_STAGE) ? 0 : cur + L_STAGE;
        }
        STAMP(1);
        const unsigned kvb = (unsigned)((row64 >> 5) + (w & 1)) * 2048u;
        if (w == 4 || w == 5) {
            u32x4 v; float n2 = 0.f;
#pragma unroll
            for (int e = 0; e < 4; ++e) { v[e] = cvtpk(acc0[8 + 2 * e], acc0[9 + 2 * e]); const float a = bf_lo(v[e]), c = bf_hi(v[e]); n2 += a * a + c * c; }
            bstore_sc1(v, rs_kv, lane * 16 + kvb);
            n2 = swap_add(n2);
#pragma unroll
            for (int o = 16; o >= 1; o >>= 1) n2 = fmaxf(n2, __shfl_xor(n2, o));
            if (lane == 0) *(LAS float*)(lds + L_MISC + (w & 1) * 4) = n2;
        } else if (w >= 6) {
#pragma unroll
            for (int s = 0; s < 2; ++s) {
                u32x4 v;
#pragma unroll
                for (int e = 0; e < 4; ++e) v[e] = cvtpk(acc0[8 * s + 2 * e], acc0[8 * s + 2 * e + 1]);
                bstore_sc1(v, rs_kv, r < 16 ? (unsigned)(h * 256 + r * 16) + kvb + 1024 + s * 512 : 0x7ffffff0u);
            }
        }
        BAR_VL(0);
        if (tid == 0) {
            const float n2 = fmaxf(*(LAS float*)(lds + L_MISC), *(LAS float*)(lds + L_MISC + 4));
            __hip_atomic_store((gu64*)(ws + WS_KVFLAG) + (row64 >> 6), ((unsigned long long)epoch_v << 32) | __float_as_uint(n2), RLX_AGENT);
        }
    }
    const unsigned epoch = __builtin_amdgcn_readfirstlane(epoch_v);
    if (heavy) {
        const int qrt = w & 1, qch = (w >> 1) & 1;
#pragma unroll
        for (int s = 0; s < 2; ++s) {
            u32x4 v;
#pragma unroll
            for (int e = 0; e < 4; ++e) v[e] = cvtpk(accq[8 * s + 2 * e] * QSCALE, accq[8 * s + 2 * e + 1] * QSCALE);
            *(LAS u32x4*)(lds + L_QX + ((2 * qch + s) * 2 + qrt) * 1024 + lane * 16) = v;
        }
    }
    if (tid < 24) { const unsigned o = (tid & 1) ? 0u : 0x3f803f80u; *(LAS u32x4*)(lds + L_KV + (tid >> 1) * KVT + 2048 + (tid & 1) * 16) = (u32x4){o, o, o, o}; }
    float kmax2 = __uint_as_float(wait_flags((gu64*)(ws + WS_KVFLAG), lane & 31, epoch, ws, 1));
#pragma unroll
    for (int o = 16; o >= 1; o >>= 1) kmax2 = fmaxf(kmax2, __shfl_xor(kmax2, o));
    STAMP(2);
    const unsigned kv_voff = w * 1024 + lane * 16;
    const unsigned kv_wr = L_KV + (w >> 1) * KVT + (w & 1) * 1024 + lane * 16;
    u32x4 kvst;
    {
        const u32x4 c0 = bload_sc1(rs_kv, kv_voff, 0), c1 = bload_sc1(rs_kv, kv_voff, 8192);
        kvst = bload_sc1(rs_kv, kv_voff, 2 * 8192);
        *(LAS u32x4*)(lds + kv_wr) = c0; *(LAS u32x4*)(lds + kv_wr + KVSLOT) = c1;
    }
    BAR_L();
    {
        const unsigned vo = (unsigned)((64 * w + (lane >> 3)) * 1024 + hg * 128 + (((lane & 7) ^ (((lane >> 4) & 1) << 2)) << 4));
#pragma unroll
        for (int p = 0; p < 8; ++p) glds16((const char*)wo + p * 8192, vo, lds0 + L_WO + w * 8192 + p * 1024);
    }
    const s16x8 qf = lds_frag(lds + L_QX + (hq * 2 + rt) * 1024 + lane * 16);
    float mshift;
    {
        const u32x4 qu = __builtin_bit_cast(u32x4, qf);
        float n2 = 0.f;
#pragma unroll
        for (int e = 0; e < 4; ++e) { const float a = bf_lo(qu[e]), c = bf_hi(qu[e]); n2 += a * a + c * c; }
        n2 = swap_add(n2);
        mshift = __builtin_sqrtf(n2 * kmax2) * 1.0001f;
        if (__builtin_expect(!__all(2.f * mshift <= SAFE_RANGE), 0)) {
            float m = -__builtin_inff();
#pragma unroll 1
            for (int t = 0; t < 64; ++t) {
                const f32x16 s = MFMA(asfrag(bload_sc1(rs_kv, lane * 16, t * 2048)), qf, (f32x16){});
#pragma unroll
                for (int i = 0; i < 16; ++i) m = fmaxf(m, s[i]);
            }
            mshift = swap_max(m);
        }
    }
    f32x16 cinit;
#pragma unroll
    for (int i = 0; i < 16; ++i) cinit[i] = -mshift;
    PIN(cinit);

    const __amdgpu_buffer_rsrc_t rs_w2 = mk_rsrc(w2, DFF * D * 2);
    const unsigned w2_voff = (unsigned)((448 * hq + 16 * rt + (lane >> 3)) * 1024 + hg * 128 + (lane & 7) * 16);
    const unsigned w2_wr = L_W2 + hq * 4096 + rt * 2048 + (lane >> 3) * 128 + (((lane & 7) ^ (((lane >> 4) & 1) << 2)) << 4);
    const unsigned w_rd0 = (4 * h + q4) * 128 + ((0 ^ (q4 >> 1)) << 6) + tr_in, w_rd1 = (4 * h + q4) * 128 + ((1 ^ (q4 >> 1)) << 6) + tr_in;
    const lptr w2r0 = lds + L_W2 + hq * 4096 + w_rd0, w2r1 = lds + L_W2 + hq * 4096 + w_rd1;
    const unsigned h_voff = (unsigned)(((row64 >> 5) + rt) * 112 + 28 * hq) * 1024u + lane * 16;
    const unsigned k_rd = L_KV + lane * 16;
    const unsigned v_cst = ((ABL & 8) && (r & 27) != 16) ? 2064 : 2048;
    const unsigned v_rd0 = L_KV + (r < 16 ? 1024 + h * 256 + r * 16 : v_cst), v_rd1 = L_KV + (r < 16 ? 1536 + h * 256 + r * 16 : v_cst);
    f32x16 sA, sB, oacc = {}, accb0 = {}, accb1 = {};
    u32x4 pA0, pA1, pB0 = {}, pB1 = {}, wst0 = {}, wst1 = {};
    u32x4 hA0 = {}, hA1 = {}, hB0 = {}, hB1 = {};
    s16x8 kf, vf0 = {}, vf1 = {};
    unsigned sl_cur = 0, sl_nxt = KVSLOT, sl_wr = 2 * KVSLOT;
    kf = lds_frag(lds + k_rd);
    sA = MFMA(kf, qf, cinit);
    kf = lds_frag(lds + k_rd + KVT);
    STAMP(3);

#define EX2(S_, i) do { S_[i] = __builtin_amdgcn_exp2f(S_[i]); S_[(i) + 1] = __builtin_amdgcn_exp2f(S_[(i) + 1]); } while (0)
#define PK(P_, e, S_, i) P_[e] = cvtpk(S_[i], S_[(i) + 1])
#define ITER(SC, SN, PC0, PC1, PP0, PP1, KPTR, VOFF, PRE, MEM, GEMM) do { \
    PRE; EX2(SC, 0); PIN(SC); SBAR(); \
    SN = MFMA(kf, qf, cinit); kf = lds_frag(KPTR); SBAR(); \
    EX2(SC, 2); PK(PC0, 0, SC, 0); EX2(SC, 4); PK(PC0, 1, SC, 2); PIN(SC); PIN(PC0); SBAR(); \
    oacc = MFMA(vf0, asfrag(PP0), oacc); vf0 = lds_frag(vp0 + (VOFF)); SBAR(); \
    EX2(SC, 6); PK(PC0, 2, SC, 4); EX2(SC, 8); PK(PC0, 3, SC, 6); PIN(SC); PIN(PC0); SBAR(); \
    oacc = MFMA(vf1, asfrag(PP1), oacc); vf1 = lds_frag(vp1 + (VOFF)); SBAR(); \
    EX2(SC, 10); PK(PC1, 0, SC, 8); MEM; EX2(SC, 12); PK(PC1, 1, SC, 10); PIN(SC); PIN(PC1); SBAR(); \
    GEMM; SBAR(); \
    EX2(SC, 14); PK(PC1, 2, SC, 12); PK(PC1, 3, SC, 14); PIN(PC1); SBAR(); } while (0)
#define MEM_KV(c) do { *(LAS u32x4*)(lds + kv_wr + sl_wr) = kvst; kvst = bload_sc1(rs_kv, kv_voff, ((c) + 3) * 8192); } while (0)
#define HLOAD(H, off) H = bload_sc1(rs_h, h_voff, off)
#define MEM_W(st, piece, wslot, pair) do { *(LAS u32x4*)(lds + w2_wr + (wslot) + (piece) * 1024) = st; st = bload(rs_w2, w2_voff + (piece) * 8192, (pair) * 32768); } while (0)
#define GM(acc, H) acc = MFMA(wf, asfrag(H), acc)
#define WF(p) wf = tr_frag(p, 1024)
#define LBAR() BAR_L()
#define CHUNK_HEAD() const lptr kpc = lds + k_rd + sl_cur, kpn = lds + k_rd + sl_nxt, vp0 = lds + v_rd0 + sl_cur, vp1 = lds + v_rd1 + sl_cur
#define ROT() do { const unsigned t_ = sl_cur; sl_cur = sl_nxt; sl_nxt = sl_wr; sl_wr = t_; } while (0)

#pragma unroll 1
    for (int c = 0; c < 4; ++c) {
        if (c == 3) {
            wait_flags((gu64*)(ws + WS_HFLAG) + R * 16, lane < 14 ? lane : 0, epoch, ws, 2);
            hA0 = bload_sc1(rs_h, h_voff, 0); hA1 = bload_sc1(rs_h, h_voff, 1024); hB0 = bload_sc1(rs_h, h_voff, 2048); hB1 = bload_sc1(rs_h, h_voff, 3072);
            STAMP(4);
        }
        CHUNK_HEAD();
        const int pair = __builtin_amdgcn_readfirstlane(c < 2 ? 0 : c - 2);
        const unsigned wslot = ((c + 1) & 1) * 16384;
        ITER(sA, sB, pA0, pA1, pB0, pB1, kpc + 2 * KVT, 0 * KVT, (void)0, MEM_KV(c), (void)0);
        ITER(sB, sA, pB0, pB1, pA0, pA1, kpc + 3 * KVT, 1 * KVT, (void)0, MEM_W(wst0, 0, wslot, pair), (void)0);
        ITER(sA, sB, pA0, pA1, pB0, pB1, kpn + 0 * KVT, 2 * KVT, (void)0, MEM_W(wst1, 1, wslot, pair), (void)0);
        ITER(sB, sA, pB0, pB1, pA0, pA1, kpn + 1 * KVT, 3 * KVT, (void)0, (void)0, (void)0);
        ROT();
        if (c == 0) {
            BAR_VL(0);
            if (tid == 0 && !kvcu) __hip_atomic_store((gu64*)(ws + WS_HFLAG) + R * 16 + cb, (unsigned long long)epoch << 32, RLX_AGENT);
        } else BAR_L();
    }
    s16x8 wf;
#define GCHUNK(c, RS, H0, H1) do { CHUNK_HEAD(); \
    ITER(sA, sB, pA0, pA1, pB0, pB1, kpc + 2 * KVT, 0 * KVT, WF(w2r0 + (RS)), MEM_KV(c), GM(accb0, H0)); \
    ITER(sB, sA, pB0, pB1, pA0, pA1, kpc + 3 * KVT, 1 * KVT, WF(w2r1 + (RS)), MEM_W(wst0, 0, 16384 - (RS), (c) - 2), GM(accb1, H0); HLOAD(H0, ((c) - 2) * 2048)); \
    ITER(sA, sB, pA0, pA1, pB0, pB1, kpn + 0 * KVT, 2 * KVT, WF(w2r0 + (RS) + 2048), MEM_W(wst1, 1, 16384 - (RS), (c) - 2), GM(accb0, H1)); \
    ITER(sB, sA, pB0, pB1, pA0, pA1, kpn + 1 * KVT, 3 * KVT, WF(w2r1 + (RS) + 2048), (void)0, GM(accb1, H1); HLOAD(H1, ((c) - 2) * 2048 + 1024)); \
    ROT(); LBAR(); } while (0)
    STAMP(9);
#pragma unroll 1
    for (int c = 4; c < 16; c += 2) {
        GCHUNK(c, 0, hA0, hA1);
        GCHUNK(c + 1, 16384, hB0, hB1);
    }
    STAMP(5);
    oacc = MFMA(vf0, asfrag(pB0), oacc);
    oacc = MFMA(vf1, asfrag(pB1), oacc);
    {
        const float inv = __builtin_amdgcn_rcpf(oacc[8]);
        u32x4 v;
#pragma unroll
        for (int e = 0; e < 4; ++e) { const unsigned u = cvtpk(oacc[2 * e] * inv, oacc[2 * e + 1] * inv); v[e] = u < 0xfffffffeu ? u : 0xfffffffeu; }
        bstore_sc1(v, rs_o, lane * 16 + o_own);
    }
    const int frow = row64 + 32 * rt + 8 * hq + (lane >> 3);
    u32x4 gx[8], gg[8];
#pragma unroll
    for (int i = 0; i < 8; ++i) { gx[i] = *(const u32x4*)(x + frow * 512 + (i * 8 + (lane & 7)) * 8); gg[i] = *(const u32x4*)(gw + (i * 8 + (lane & 7)) * 8); }
    *(LAS u32x4*)(lds + w2_wr + 16384) = wst0; *(LAS u32x4*)(lds + w2_wr + 16384 + 1024) = wst1;
    accb0 = MFMA(tr_frag(w2r0, 1024), asfrag(hA0), accb0); accb1 = MFMA(tr_frag(w2r1, 1024), asfrag(hA0), accb1);
    accb0 = MFMA(tr_frag(w2r0 + 2048, 1024), asfrag(hA1), accb0); accb1 = MFMA(tr_frag(w2r1 + 2048, 1024), asfrag(hA1), accb1);
    BAR_L();
    accb0 = MFMA(tr_frag(w2r0 + 16384, 1024), asfrag(hB0), accb0); accb1 = MFMA(tr_frag(w2r1 + 16384, 1024), asfrag(hB0), accb1);
    accb0 = MFMA(tr_frag(w2r0 + 16384 + 2048, 1024), asfrag(hB1), accb0); accb1 = MFMA(tr_frag(w2r1 + 16384 + 2048, 1024), asfrag(hB1), accb1);
    BAR_L();
    const lptr red_w = lds + L_RED + ((rt * 4 + hq) * 32 + r) * 256;
    const lptr red_r = lds + L_RED + (rt * 4 * 32 + 8 * hq + (lane >> 3)) * 256;
    const unsigned rc0 = ((2 * (lane & 7)) ^ ((lane >> 3) & 7)) << 4, rc1 = ((2 * (lane & 7) + 1) ^ ((lane >> 3) & 7)) << 4;
    auto put = [&](const f32x16& a0, const f32x16& a1) {
#pragma unroll
        for (int gq = 0; gq < 4; ++gq) {
            *(LAS f32x4*)(red_w + (((2 * gq + h) ^ (r & 7)) << 4)) = (f32x4){a0[4 * gq], a0[4 * gq + 1], a0[4 * gq + 2], a0[4 * gq + 3]};
            *(LAS f32x4*)(red_w + 128 + (((2 * gq + h) ^ (r & 7)) << 4)) = (f32x4){a1[4 * gq], a1[4 * gq + 1], a1[4 * gq + 2], a1[4 * gq + 3]};
        }
    };
    auto get = [&](f32x4& lo, f32x4& hi) {
        lo = *(const LAS f32x4*)(red_r + rc0); hi = *(const LAS f32x4*)(red_r + rc1);
#pragma unroll
        for (int k = 1; k < 4; ++k) { lo += *(const LAS f32x4*)(red_r + k * 8192 + rc0); hi += *(const LAS f32x4*)(red_r + k * 8192 + rc1); }
    };
    put(accb0, accb1);
    float gate;
    {
        float z = 0.f;
#pragma unroll
        for (int i = 0; i < 8; ++i)
#pragma unroll
            for (int e = 0; e < 4; ++e) { const unsigned u = cvtpk(bf_lo(gx[i][e]) * bf_lo(gg[i][e]), bf_hi(gx[i][e]) * bf_hi(gg[i][e])); z += bf_lo(u) + bf_hi(u); }
        z += __shfl_xor(z, 1); z += __shfl_xor(z, 2); z += __shfl_xor(z, 4);
        z = rbf(z);
        gate = rbf(1.f / (1.f + __expf(-z)));
    }
    BAR_L();
    f32x4 blo, bhi;
    get(blo, bhi);
    STAMP(6);
    u32x4 of[8];
    {
        const unsigned ob = (unsigned)(((row64 >> 5) + rt) * 32 + 8 * hq) * 1024u;
        for (unsigned spins = 0;; ++spins) {
            unsigned m = 0;
#pragma unroll
            for (int i = 0; i < 8; ++i) of[i] = bload_sc1(rs_o, lane * 16 + i * 1024, ob);
#pragma unroll
            for (int i = 0; i < 8; ++i) { m = max(m, max(of[i][0], of[i][1])); m = max(m, max(of[i][2], of[i][3])); }
            if (__all(m != ~0u)) break;
            if (spins > SPIN_CAP) { if (m == ~0u) { unsigned mk = 0; for (int i = 0; i < 8; ++i) mk |= (max(max(of[i][0], of[i][1]), max(of[i][2], of[i][3])) == ~0u) << i; LOGTMO(3, mk); } break; }
            CFENCE();
        }
    }
    STAMP(7);
    f32x16 acca0 = {}, acca1 = {};
    {
        const lptr wr0 = lds + L_WO + hq * 16384 + w_rd0, wr1 = lds + L_WO + hq * 16384 + w_rd1;
#pragma unroll
        for (int i = 0; i < 8; ++i) {
            acca0 = MFMA(tr_frag(wr0 + i * 2048, 1024), asfrag(of[i]), acca0);
            acca1 = MFMA(tr_frag(wr1 + i * 2048, 1024), asfrag(of[i]), acca1);
        }
    }
    BAR_L();
    put(acca0, acca1);
    BAR_L();
    f32x4 alo, ahi;
    get(alo, ahi);
    {
        const float og = rbf(1.f - gate);
        u32x4 v;
#define MIX(a_, b_) (rbf(gate * rbf(a_)) + rbf(og * rbf(b_)))
        v[0] = cvtpk(MIX(alo[0], blo[0]), MIX(alo[1], blo[1])); v[1] = cvtpk(MIX(alo[2], blo[2]), MIX(alo[3], blo[3]));
        v[2] = cvtpk(MIX(ahi[0], bhi[0]), MIX(ahi[1], bhi[1])); v[3] = cvtpk(MIX(ahi[2], bhi[2]), MIX(ahi[3], bhi[3]));
        *(u32x4*)(out + frow * 512 + hg * 64 + (lane & 7) * 8) = v;
    }
    STAMP(8);
    if (b == 0 && w == 0) {
#pragma unroll 1
        for (int i = 0; i < 4; ++i) {
            const int e = i * 64 + lane;
            gu64* f = (gu64*)(ws + WS_HFLAG) + ((e & 15) < 14 ? e : (e & ~15));
            for (unsigned spins = 0;; ++spins) {
                const unsigned long long v = __hip_atomic_load(f, RLX_AGENT);
                if (__all((unsigned)(v >> 32) == epoch) || spins > SPIN_CAP) break;
                __builtin_amdgcn_s_sleep(2);
            }
        }
        if (lane == 0) __hip_atomic_store((gu32*)(ws + WS_EPOCH), epoch, RLX_AGENT);
    }
}

extern "C" void kernel_launch(void* const* d_in, const int* in_sizes, int n_in, void* d_out, int out_size, void* d_ws, size_t ws_size,
                              hipStream_t stream) {
    fused_kernel<<<256, 512, 0, stream>>>((const uint16_t*)d_in[0], (const uint16_t*)d_in[1], (const uint16_t*)d_in[2], (const uint16_t*)d_in[3],
                                          (const uint16_t*)d_in[4], (const uint16_t*)d_in[6], (const uint16_t*)d_in[7], (const uint16_t*)d_in[8],
                                          (uint16_t*)d_out, (char*)d_ws);
}
```

```cpp
#include <hip/hip_runtime.h>
#include <stdint.h>

typedef short s16x8 __attribute__((ext_vector_type(8)));
typedef short s16x4 __attribute__((ext_vector_type(4)));
typedef short s16x2 __attribute__((ext_vector_type(2)));
typedef float f32x16 __attribute__((ext_vector_type(16)));
typedef float f32x4 __attribute__((ext_vector_type(4)));
typedef float f32x2 __attribute__((ext_vector_type(2)));
typedef unsigned u32x4 __attribute__((ext_vector_type(4)));
typedef __bf16 bf16x2 __attribute__((ext_vector_type(2)));
typedef __bf16 bf16x8 __attribute__((ext_vector_type(8)));
typedef __bf16 bf16x16 __attribute__((ext_vector_type(16)));
#define LAS __attribute__((address_space(3)))
#define GAS __attribute__((address_space(1)))
typedef GAS unsigned gu32;
typedef GAS unsigned long long gu64;
typedef LAS char* lptr;
#define DI __device__ __forceinline__
#define RLX_AGENT __ATOMIC_RELAXED, __HIP_MEMORY_SCOPE_AGENT
#define MFMA(a, b, c) __builtin_amdgcn_mfma_f32_32x32x16_bf16((a), (b), (c), 0, 0, 0)
#define SBAR() __builtin_amdgcn_sched_barrier(0)
#define PIN(x) asm volatile("" : "+v"(x))
#define CFENCE() do { asm volatile("" ::: "memory"); __builtin_amdgcn_fence(__ATOMIC_ACQUIRE, "wavefront"); } while (0)
#define BAR_L() asm volatile("s_waitcnt lgkmcnt(0)\n\ts_barrier" ::: "memory")
#define BAR_VL(N) asm volatile("s_waitcnt vmcnt(" #N ") lgkmcnt(0)\n\ts_barrier" ::: "memory")

#define ABL 0
#define STW 0
#define STAMPS 0

constexpr int D = 512, DFF = 1792;
constexpr float QSCALE = 0.25f * 1.4426950408889634f;
constexpr float SAFE_RANGE = 64.f;
constexpr unsigned WS_EPOCH = 0, WS_KVFLAG = 4096, WS_HFLAG = 8192;
constexpr unsigned WS_KV = WS_HFLAG + 32768, KV_BYTES = 64 * 2048;
constexpr unsigned WS_H = WS_KV + KV_BYTES, H_BYTES = 64 * 112 * 1024;
constexpr unsigned WS_O = WS_H + H_BYTES, O_BYTES = 64 * 32 * 1024;
constexpr unsigned WS_STAMP = WS_O + O_BYTES;
constexpr unsigned OOB = 0x7ff00000u;
constexpr unsigned L_STAGE = 40960;
constexpr unsigned L_KSTAGE = 12288, L_KWQ = 98304;
constexpr unsigned KVT = 2080;
constexpr unsigned L_WB = 64 * KVT;
constexpr unsigned L_QX = L_WB + 16384, L_GW = L_QX + 8192, L_GZ = L_GW + 2048, L_KM = L_GZ + 512, LDS_BYTES = 163840;
static_assert(L_KM + 16 <= LDS_BYTES, "lds map");
constexpr unsigned SPIN_CAP = 1u << 16;

DI unsigned cvtpk(float a, float b) { f32x2 v = {a, b}; return __builtin_bit_cast(unsigned, __builtin_convertvector(v, bf16x2)); }
DI float rbf(float x) { return __uint_as_float(cvtpk(x, x) << 16); }
DI s16x8 asfrag(u32x4 v) { return __builtin_bit_cast(s16x8, v); }
DI s16x8 lds_frag(lptr p) { return *(const LAS s16x8*)p; }
DI void lds_put(lptr p, u32x4 v) { *(LAS u32x4*)p = v; }
DI s16x4 lds_tr(lptr p) { return __builtin_amdgcn_ds_read_tr16_b64_v4i16((LAS s16x4*)p); }
DI s16x8 tr_frag2(lptr p, lptr q) { s16x4 lo = lds_tr(p), hi = lds_tr(q); return __builtin_shufflevector(lo, hi, 0, 1, 2, 3, 4, 5, 6, 7); }
DI s16x8 tr_frag(lptr p, int second) { return tr_frag2(p, p + second); }
DI float swap_add(float v) { auto r = __builtin_amdgcn_permlane32_swap(__float_as_uint(v), __float_as_uint(v), false, false); return __uint_as_float(r[0]) + __uint_as_float(r[1]); }
DI float swap_max(float v) { auto r = __builtin_amdgcn_permlane32_swap(__float_as_uint(v), __float_as_uint(v), false, false); return fmaxf(__uint_as_float(r[0]), __uint_as_float(r[1])); }
DI float dot2(unsigned a, unsigned b, float c) { return __builtin_amdgcn_fdot2_f32_bf16(__builtin_bit_cast(bf16x2, a), __builtin_bit_cast(bf16x2, b), c, false); }
DI unsigned relu_pk(unsigned u) { return __builtin_bit_cast(unsigned, __builtin_elementwise_max(__builtin_bit_cast(s16x2, u), (s16x2){0, 0})); }
template <int CTRL> DI float dpp(float v) { return __uint_as_float(__builtin_amdgcn_update_dpp(0, __float_as_uint(v), CTRL, 0xf, 0xf, true)); }
DI float row32_max(float v) {
    v = fmaxf(v, dpp<0xB1>(v)); v = fmaxf(v, dpp<0x4E>(v)); v = fmaxf(v, dpp<0x141>(v)); v = fmaxf(v, dpp<0x140>(v));
    auto r = __builtin_amdgcn_permlane16_swap(__float_as_uint(v), __float_as_uint(v), false, false);
    return fmaxf(__uint_as_float(r[0]), __uint_as_float(r[1]));
}
template <int HALF> DI u32x4 pack8(const f32x16& a, float m) {
    u32x4 v;
#pragma unroll
    for (int e = 0; e < 4; ++e) v[e] = cvtpk(a[8 * HALF + 2 * e] * m, a[8 * HALF + 2 * e + 1] * m);
    return v;
}
template <int HALF> DI u32x4 pack8_relu(const f32x16& a) {
    u32x4 v;
#pragma unroll
    for (int e = 0; e < 4; ++e) v[e] = relu_pk(cvtpk(a[8 * HALF + 2 * e], a[8 * HALF + 2 * e + 1]));
    return v;
}
template <int HALF> DI f32x16 pv_mma(s16x8 v, const f32x16& p, f32x16 c, int idx) {
    const auto ph = __builtin_shufflevector(p, p, 8 * HALF, 8 * HALF + 1, 8 * HALF + 2, 8 * HALF + 3, 8 * HALF + 4, 8 * HALF + 5, 8 * HALF + 6, 8 * HALF + 7);
    return __builtin_amdgcn_smfmac_f32_32x32x32_bf16(__builtin_bit_cast(bf16x8, v), __builtin_bit_cast(bf16x16, ph), c, idx, 0, 0);
}
DI void glds16(const void* sbase, unsigned voff, unsigned lds_base) {
    asm volatile("s_mov_b32 m0, %2\n\ts_nop 0\n\tglobal_load_lds_dwordx4 %0, %1" :: "v"(voff), "s"(sbase), "s"(lds_base) : "memory");
}
DI void glds16x2(const void* sbase, unsigned voff0, unsigned voff1, unsigned lds_base) {
    asm volatile("s_mov_b32 m0, %3\n\ts_nop 0\n\tglobal_load_lds_dwordx4 %0, %2\n\tglobal_load_lds_dwordx4 %1, %2 offset:1024" :: "v"(voff0), "v"(voff1), "s"(sbase), "s"(lds_base) : "memory");
}
DI void glds16x4(const void* sbase, unsigned v0, unsigned v1, unsigned v2, unsigned v3, unsigned lds_base) {
    asm volatile("s_mov_b32 m0, %5\n\ts_nop 0\n\tglobal_load_lds_dwordx4 %0, %4\n\tglobal_load_lds_dwordx4 %1, %4 offset:1024\n\t"
                 "global_load_lds_dwordx4 %2, %4 offset:2048\n\tglobal_load_lds_dwordx4 %3, %4 offset:3072" :: "v"(v0), "v"(v1), "v"(v2), "v"(v3), "s"(sbase), "s"(lds_base) : "memory");
}
DI void glds16x2_sc1(const void* sbase, unsigned voff, unsigned lds_base) {
    asm volatile("s_mov_b32 m0, %2\n\ts_nop 0\n\tglobal_load_lds_dwordx4 %0, %1 sc1\n\tglobal_load_lds_dwordx4 %0, %1 offset:1024 sc1" :: "v"(voff), "s"(sbase), "s"(lds_base) : "memory");
}
#define WAIT_V(N) asm volatile("s_waitcnt vmcnt(" #N ")" ::: "memory")
DI __amdgpu_buffer_rsrc_t mk_rsrc(const void* p, unsigned bytes) { return __builtin_amdgcn_make_buffer_rsrc((void*)p, 0, bytes, 0x00020000); }
DI u32x4 bload(__amdgpu_buffer_rsrc_t r, unsigned voff, unsigned soff) { return __builtin_amdgcn_raw_buffer_load_b128(r, voff, soff, 0); }
DI u32x4 bload_sc1(__amdgpu_buffer_rsrc_t r, unsigned voff, unsigned soff) { return __builtin_amdgcn_raw_buffer_load_b128(r, voff, soff, 16); }
DI void bstore_sc1(u32x4 v, __amdgpu_buffer_rsrc_t r, unsigned voff) { __builtin_amdgcn_raw_buffer_store_b128(v, r, voff, 0, 16); }

#define STAMP(i) do { } while (0)

DI unsigned long long flag_load(gu64* f) { return __hip_atomic_load(f, RLX_AGENT); }
DI unsigned wait_flags(gu64* f, unsigned long long v, unsigned epoch) {
    if (__builtin_expect(!__all((unsigned)(v >> 32) == epoch), 0)) {
        unsigned spins = 0;
        do { __builtin_amdgcn_s_sleep(2); v = flag_load(f); } while (!__all((unsigned)(v >> 32) == epoch) && ++spins < SPIN_CAP);
    }
    CFENCE();
    return (unsigned)v;
}

struct BigFrag { s16x8 x0, x1, w0, w1; };
struct SmallFrag { s16x8 x, q; u32x4 g; };

__global__ void __launch_bounds__(512, 2)
fused_kernel(const uint16_t* __restrict__ x, const uint16_t* __restrict__ wq, const uint16_t* __restrict__ wk, const uint16_t* __restrict__ wv,
             const uint16_t* __restrict__ wo, const uint16_t* __restrict__ w1, const uint16_t* __restrict__ w2, const uint16_t* __restrict__ gw,
             uint16_t* out, char* ws) {
    __shared__ __attribute__((aligned(16))) char smem[LDS_BYTES];
    const lptr lds = (lptr)smem;
    const unsigned lds0 = (unsigned)(uintptr_t)smem;
    const int tid = threadIdx.x, lane = tid & 63, w = __builtin_amdgcn_readfirstlane(tid >> 6);
    const int r = lane & 31, h = lane >> 5, g1 = (lane >> 4) & 1, q4 = (lane & 15) >> 2, p4 = lane & 3;
    const int b = blockIdx.x, bi = b >> 3;
    const int R = 2 * (b & 7) + (bi >> 4), cb = bi & 15, rh64 = cb & 1, hg = cb >> 1;
    const bool kvcu = cb >= 14, big = w < 4;
    const int row64 = R * 128 + rh64 * 64;
    const int rt = w & 1, hq = w >> 1, fh = hq & 1;
    unsigned stv = 0;
    STAMP(0);

    const unsigned epoch_v = __hip_atomic_load((gu32*)(ws + WS_EPOCH), RLX_AGENT);
    const unsigned w64_src = (unsigned)((lane >> 3) * 1024 + hg * 128 + (((lane & 7) ^ (g1 << 2)) << 4));
    const unsigned w64_src1 = w64_src + 8192 - 1024;
    const __amdgpu_buffer_rsrc_t rs_kv = mk_rsrc(ws + WS_KV, KV_BYTES), rs_h = mk_rsrc(ws + WS_H, H_BYTES), rs_o = mk_rsrc(ws + WS_O, O_BYTES);
    const unsigned o_own = (unsigned)(((row64 >> 5) + rt) * 32 + hg * 4 + hq) * 1024u + lane * 16;
    const u32x4 omark = {~0u, ~0u, ~0u, ~0u};
    gu64* const kvflag = (gu64*)(ws + WS_KVFLAG) + r * 16;
    gu64* const hflag = (gu64*)(ws + WS_HFLAG) + (R * 16 + (lane & 15)) * 16;
    const unsigned tr_in = 32 * g1 + 8 * p4;
    const unsigned wq_rd = (8 * h + q4) * 128 + ((fh ^ (q4 >> 1)) << 6) + tr_in;
    unsigned xo[4];
#pragma unroll
    for (int ks = 0; ks < 4; ++ks) xo[ks] = r * 128 + (((2 * ks + h) ^ ((r >> 1) & 7)) << 4);

    f32x16 a00 = {}, a01 = {}, a10 = {}, a11 = {};
    auto kv_wait = [&](unsigned long long fv) {
        const float km = row32_max(__uint_as_float(wait_flags(kvflag, fv, __builtin_amdgcn_readfirstlane(epoch_v) + 1u)));
        if (tid == 0) *(LAS float*)(lds + L_KM) = km;
    };
    auto kv_tile = [&](int t) { glds16x2_sc1(ws + WS_KV + t * 2048, lane * 16, lds0 + t * KVT); };
    if (!kvcu) {
        const unsigned xv = (unsigned)((R * 128 + (lane >> 3)) * 1024 + (((lane & 7) ^ ((lane >> 4) & 3)) << 4));
        const unsigned wv0 = (unsigned)((lane >> 4) * (DFF * 2) + cb * 256 + (((lane & 15) ^ (((lane >> 4) & 3) << 2)) << 4));
        const unsigned xv1 = (xv ^ 64) + 8192 - 1024, wv1 = wv0 + 4 * DFF * 2 - 1024;
#pragma unroll
        for (int s = 0; s < 2; ++s) {
            glds16x2((const char*)x + s * 128 + w * 16384, xv, xv1, lds0 + s * L_STAGE + w * 2048);
            glds16x2((const char*)w1 + (size_t)(s * 64 + w * 8) * (DFF * 2), wv0, wv1, lds0 + s * L_STAGE + 16384 + w * 2048);
            glds16((const char*)wq + s * 65536 + w * 8192, w64_src, lds0 + s * L_STAGE + 32768 + w * 1024);
            if (s == 0 && w == 7) glds16(gw, lane * 16, lds0 + L_GW);
        }
        unsigned cur = 0, nxt = L_STAGE;
        if (big) {
            const unsigned xb = rt * 8192, w0o = 16384 + (8 * h + q4) * 256 + (((2 * fh) ^ q4) << 6) + tr_in, w1o = w0o ^ 64;
            BigFrag F[4];
            auto rd = [&](BigFrag& n, lptr sb, int ks) {
                n.x0 = lds_frag(sb + xb + xo[ks]); n.x1 = lds_frag(sb + xb + 4096 + xo[ks]); n.w0 = tr_frag(sb + w0o + ks * 4096, 1024); n.w1 = tr_frag(sb + w1o + ks * 4096, 1024); };
#define AM(e, ...) do { if (ABL & 2048) asm volatile("" :: __VA_ARGS__); else e; SBAR(); } while (0)
#define AR(e, ...) do { if (ABL & 4096) asm volatile("" : __VA_ARGS__); else { e; } SBAR(); } while (0)
            auto step = [&](const BigFrag& c, BigFrag& n, lptr sb, int ks) {
                AM(a00 = MFMA(c.w0, c.x0, a00), "v"(c.w0), "v"(c.x0)); AR(n.x0 = lds_frag(sb + xb + xo[ks]); n.x1 = lds_frag(sb + xb + 4096 + xo[ks]), "+v"(n.x0), "+v"(n.x1));
                AM(a10 = MFMA(c.w0, c.x1, a10), "v"(c.x1)); AR(n.w0 = tr_frag(sb + w0o + ks * 4096, 1024), "+v"(n.w0));
                AM(a01 = MFMA(c.w1, c.x0, a01), "v"(c.w1)); AR(n.w1 = tr_frag(sb + w1o + ks * 4096, 1024), "+v"(n.w1));
                AM(a11 = MFMA(c.w1, c.x1, a11), "v"(c.w1)); };
            auto stage = [&](auto bar) {
                step(F[0], F[3], lds + cur, 3);
                bar();
                step(F[1], F[0], lds + nxt, 0); step(F[2], F[1], lds + nxt, 1); step(F[3], F[2], lds + nxt, 2);
                cur = nxt; nxt = (nxt == 2 * L_STAGE) ? 0 : nxt + L_STAGE; };
            STAMP(13);
            BAR_VL(5);
            STAMP(14);
            rd(F[0], lds, 0); rd(F[1], lds, 1); rd(F[2], lds, 2);
#pragma unroll 1
            for (int s = 0; s < 5; ++s) stage([&] { BAR_VL(0); });
            const unsigned long long fv = flag_load(kvflag);
            stage([&] { BAR_L(); });
            STAMP(15);
            kv_wait(fv);
            STAMP(16);
            stage([&] { BAR_L(); kv_tile(2 * w); kv_tile(2 * w + 1); });
            STAMP(1);
            rd(F[3], lds + cur, 3);
#pragma unroll
            for (int ks = 0; ks < 4; ++ks) { const BigFrag& c = F[ks]; a00 = MFMA(c.w0, c.x0, a00); a10 = MFMA(c.w0, c.x1, a10); a01 = MFMA(c.w1, c.x0, a01); a11 = MFMA(c.w1, c.x1, a11); }
        } else {
            const int j = w - 4;
            __builtin_amdgcn_s_setprio(2);
            const unsigned xb = (2 * rh64 + rt) * 4096, qo = 32768 + wq_rd;
            lptr gp = lds + L_GW + h * 16 + fh * 32;
            float gz0 = 0.f;
            SmallFrag F[4];
            auto rd = [&](SmallFrag& n, lptr sb, int ks) { n.x = lds_frag(sb + xb + xo[ks]); n.q = tr_frag(sb + qo + ks * 2048, 512); if (!(ks & 1)) n.g = *(const LAS u32x4*)(gp + ks * 32); SBAR(); };
            auto mma2 = [&](const SmallFrag& c0, const SmallFrag& c1) {
                if (ABL & 64) return;
                const u32x4 xu = __builtin_bit_cast(u32x4, fh ? c1.x : c0.x);
#pragma unroll
                for (int e = 0; e < 4; ++e) {
                    gz0 = fmaf(__uint_as_float(xu[e] << 16), __uint_as_float(c0.g[e] << 16), gz0);
                    gz0 = fmaf(__uint_as_float(xu[e] & 0xffff0000u), __uint_as_float(c0.g[e] & 0xffff0000u), gz0);
                }
                PIN(gz0);
                SBAR(); };
            auto mma = [&](const SmallFrag& c) { a00 = MFMA(c.q, c.x, a00); SBAR(); };
            const unsigned xv2 = xv + 2 * (8192 - 1024), xv3 = xv1 + 2 * (8192 - 1024), wv2 = wv0 + 2 * (4 * DFF * 2 - 1024), wv3 = wv1 + 2 * (4 * DFF * 2 - 1024);
            const char* xs = (const char*)x + 2 * 128 + j * 32768;
            const char* w1s = (const char*)w1 + (size_t)(2 * 64 + j * 16) * (DFF * 2);
            const char* wqs = (const char*)wq + 2 * 65536 + j * 16384;
            auto fill0 = [&](unsigned slot) { glds16x4(xs, xv, xv1, xv2, xv3, lds0 + slot + j * 4096); xs += 128; };
            auto fill1 = [&](unsigned slot) { glds16x4(w1s, wv0, wv1, wv2, wv3, lds0 + slot + 16384 + j * 4096); w1s += 64 * DFF * 2; };
            auto fill2 = [&](unsigned slot) { glds16x2(wqs, w64_src, w64_src1, lds0 + slot + 32768 + j * 2048); wqs += 65536; };
            BAR_VL(5);
            rd(F[0], lds, 0); rd(F[1], lds, 1); rd(F[2], lds, 2);
            fill0(2 * L_STAGE); fill1(2 * L_STAGE); fill2(2 * L_STAGE);
#pragma unroll 1
            for (int s = 0; s < 7; ++s) {
                const bool more = (ABL & 1024) ? false : s < 5;
                rd(F[3], lds + cur, 3); mma(F[0]); mma2(F[0], F[1]);
                gp += 128;
                if (s < 6) BAR_VL(10); else BAR_VL(0);
                if (more) fill0(cur);
                rd(F[0], lds + nxt, 0); mma(F[1]);
                rd(F[1], lds + nxt, 1); mma(F[2]);
                if (more) fill1(cur);
                mma2(F[2], F[3]);
                rd(F[2], lds + nxt, 2); mma(F[3]);
                if (more) fill2(cur);
                cur = nxt; nxt = (nxt == 2 * L_STAGE) ? 0 : nxt + L_STAGE;
            }
            rd(F[3], lds + cur, 3);
            mma(F[0]); mma(F[1]); mma2(F[0], F[1]); mma(F[2]); mma(F[3]); mma2(F[2], F[3]);
            const float gz = swap_add(gz0);
            if (h == 0) *(LAS float*)(lds + L_GZ + fh * 256 + (rt * 32 + r) * 4) = gz;
        }
    } else {
        const unsigned xv0 = (unsigned)((row64 + 8 * w + (lane >> 3)) * 1024 + (((lane & 7) ^ ((4 * w + (lane >> 4)) & 7)) << 4));
        const unsigned xb = rt * 4096;
        if (big) {
#pragma unroll 1
            for (int s = 0; s < 8; ++s) glds16((const char*)x + s * 128, xv0, lds0 + s * L_KSTAGE + w * 1024);
            const unsigned wkv_rd = 8192 + g1 * 2048 + (((8 * h + q4) * 32 + 8 * p4) ^ (g1 << 7));
#pragma unroll 1
            for (int g = 0; g < 4; ++g) {
                if (g == 0) BAR_VL(6); else if (g == 1) BAR_VL(4); else if (g == 2) BAR_VL(2); else BAR_VL(0);
                const lptr sb = lds + g * 2 * L_KSTAGE;
                s16x8 xf[8], wf[8];
#pragma unroll
                for (int k = 0; k < 8; ++k) {
                    const lptr st = sb + (k >> 2) * L_KSTAGE;
                    xf[k] = lds_frag(st + xb + xo[k & 3]); wf[k] = tr_frag2(st + wkv_rd + (k & 3) * 512, st + (wkv_rd ^ 128) + (k & 3) * 512);
                }
                if (fh == 0) { _Pragma("unroll") for (int k = 0; k < 8; ++k) a00 = MFMA(wf[k], xf[k], a00); }
                else { _Pragma("unroll") for (int k = 0; k < 8; ++k) a00 = MFMA(xf[k], wf[k], a00); }
            }
            STAMP(1);
            const unsigned kvo = (unsigned)((row64 >> 5) + rt) * 2048u;
            if (fh == 0) {
                const u32x4 v = pack8<1>(a00, 1.f);
                bstore_sc1(v, rs_kv, lane * 16 + kvo);
                float n2 = 0.f;
#pragma unroll
                for (int e = 0; e < 4; ++e) n2 = dot2(v[e], v[e], n2);
                n2 = row32_max(swap_add(n2));
                if (lane == 0) *(LAS float*)(lds + 8192 + rt * 4) = n2;
            } else {
#pragma unroll
                for (int s = 0; s < 2; ++s) {
                    const u32x4 p = s ? pack8<1>(a00, 1.f) : pack8<0>(a00, 1.f);
                    const auto t0 = __builtin_amdgcn_permlane32_swap(p[0], p[2], false, false), t1 = __builtin_amdgcn_permlane32_swap(p[1], p[3], false, false);
                    bstore_sc1((u32x4){t0[0], t1[0], t0[1], t1[1]}, rs_kv, r < 16 ? (unsigned)(h * 256 + r * 16) + kvo + 1024 + s * 512 : OOB);
                }
            }
            BAR_VL(0);
            if (tid == 0) {
                const float n2 = fmaxf(*(LAS float*)(lds + 8192), *(LAS float*)(lds + 8196));
                __hip_atomic_store((gu64*)(ws + WS_KVFLAG) + (row64 >> 6) * 16, ((unsigned long long)(epoch_v + 1u) << 32) | __float_as_uint(n2), RLX_AGENT);
            }
            BAR_L();
        } else {
            const char* wkv = (const char*)((w & 2) ? wk : wv);
            const unsigned kvv = (w & 1) * 1024 + ((lane ^ ((w & 2) << 2)) << 4);
#pragma unroll 1
            for (int s = 0; s < 8; ++s) {
                glds16((const char*)x + s * 128, xv0, lds0 + s * L_KSTAGE + w * 1024);
                glds16(wkv + s * 2048, kvv, lds0 + s * L_KSTAGE + 8192 + (w & 3) * 1024);
            }
#pragma unroll 1
            for (int g = 0; g < 4; ++g) {
                BAR_VL(12);
#pragma unroll
                for (int i = 0; i < 4; ++i) { const int p = g * 16 + (w - 4) * 4 + i; glds16((const char*)wq + p * 8192, w64_src, lds0 + L_KWQ + p * 1024); }
            }
            BAR_L();
            BAR_VL(0);
        }
        float gz = 0.f;
        if (big) {
#pragma unroll 4
            for (int i = 0; i < 16; ++i) {
                const u32x4 gx = *(const u32x4*)(x + (row64 + 16 * w + (lane >> 2)) * 512 + (i * 4 + p4) * 8), gg = *(const u32x4*)(gw + (i * 4 + p4) * 8);
#pragma unroll
                for (int e = 0; e < 4; ++e) gz = dot2(gx[e], gg[e], gz);
            }
            gz += dpp<0xB1>(gz); gz += dpp<0x4E>(gz);
        } else {
#pragma unroll 1
            for (int s = 0; s < 8; ++s) {
                s16x8 xf[4], wf[4];
#pragma unroll
                for (int k = 0; k < 4; ++k) { xf[k] = lds_frag(lds + s * L_KSTAGE + xb + xo[k]); wf[k] = tr_frag(lds + L_KWQ + s * 8192 + wq_rd + k * 2048, 512); }
#pragma unroll
                for (int k = 0; k < 4; ++k) a00 = MFMA(wf[k], xf[k], a00);
            }
        }
        BAR_L();
        if (big) {
            if (p4 < 2) *(LAS float*)(lds + L_GZ + p4 * 256 + (16 * w + (lane >> 2)) * 4) = p4 ? 0.f : gz;
            kv_wait(flag_load(kvflag));
            kv_tile(2 * w); kv_tile(2 * w + 1);
        }
    }
    STAMP(2);
    const unsigned epoch = __builtin_amdgcn_readfirstlane(epoch_v) + 1u;
    u32x4 hs[8];
    const unsigned hb = ((kvcu || !big) ? OOB : (unsigned)((R * 4 + 2 * rt) * 112 + cb * 8 + fh * 4) * 1024u) + lane * 16;
    const u32x4 ones = {0x3f803f80u, 0x3f803f80u, 0x3f803f80u, 0x3f803f80u};
    if (!big) {
        lds_put(lds + L_QX + (4 * fh + rt) * 1024 + lane * 16, pack8<0>(a00, QSCALE));
        lds_put(lds + L_QX + (4 * fh + 2 + rt) * 1024 + lane * 16, pack8<1>(a00, QSCALE));
        if (w == 4 && lane < 16) lds_put(lds + (lane >> 1) * KVT + 2048 + (lane & 1) * 16, (lane & 1) ? (u32x4){} : ones);
    }
    hs[0] = pack8_relu<0>(a00); hs[1] = pack8_relu<1>(a00); hs[2] = pack8_relu<0>(a01); hs[3] = pack8_relu<1>(a01);
    hs[4] = pack8_relu<0>(a10); hs[5] = pack8_relu<1>(a10); hs[6] = pack8_relu<0>(a11); hs[7] = pack8_relu<1>(a11);
    BAR_VL(0);
    STAMP(3);
    __builtin_amdgcn_s_setprio(3);
    lptr kp = lds + lane * 16;
    const unsigned v_cst = (r & 27) != 16 ? 2064 : 2048;
    lptr vp0 = lds + (r < 16 ? 1024 + h * 256 + r * 16 : v_cst), vp1 = lds + (r < 16 ? 1536 + h * 256 + r * 16 : v_cst);
    const s16x8 qf = lds_frag(lds + L_QX + (hq * 2 + rt) * 1024 + lane * 16);
    s16x8 kf = lds_frag(kp), vf0 = lds_frag(vp0), vf1 = {};
    const float kmax2 = *(const LAS float*)(lds + L_KM);
    const LAS float* gzp = (const LAS float*)(lds + L_GZ) + r;
    const float gz0 = gzp[0] + gzp[64], gz1 = gzp[32] + gzp[96];
    kv_tile(8 + w); kv_tile(16 + w);
    if (tid >= 16 && tid < 128) lds_put(lds + (tid >> 1) * KVT + 2048 + (tid & 1) * 16, (tid & 1) ? (u32x4){} : ones);
    float mshift;
    {
        const u32x4 qu = __builtin_bit_cast(u32x4, qf);
        float n2 = 0.f;
#pragma unroll
        for (int e = 0; e < 4; ++e) n2 = dot2(qu[e], qu[e], n2);
        mshift = __builtin_amdgcn_sqrtf(swap_add(n2) * kmax2) * 1.0001f;
        if (__builtin_expect(!__all(2.f * mshift <= SAFE_RANGE), 0)) {
            float m = -__builtin_inff();
#pragma unroll 1
            for (int t = 0; t < 64; ++t) {
                const f32x16 s = MFMA(asfrag(bload_sc1(rs_kv, lane * 16, t * 2048)), qf, (f32x16){});
#pragma unroll
                for (int i = 0; i < 16; ++i) m = fmaxf(m, s[i]);
            }
            mshift = swap_max(m);
        }
    }
    f32x16 cinit;
#pragma unroll
    for (int i = 0; i < 16; ++i) cinit[i] = -mshift;
    PIN(cinit);
    f32x16 sA = MFMA(kf, qf, cinit), sB = {}, oacc = {};
    kf = lds_frag(kp + KVT);
    float gate0 = rbf(__builtin_amdgcn_rcpf(1.f + __expf(-rbf(gz0)))), gate1 = rbf(__builtin_amdgcn_rcpf(1.f + __expf(-rbf(gz1))));
    PIN(gate0); PIN(gate1);
    int spidx = 0xDDDD; PIN(spidx);

#define S1(S_, i) S_[i] = __builtin_amdgcn_exp2f(S_[i])
#define EX2(S_, i) do { S1(S_, i); S1(S_, (i) + 1); } while (0)
#define ITER(SC, SN, T, PR, PRE, MEM, G0, G1) do { \
    __builtin_amdgcn_s_setprio(PR); \
    PRE; EX2(SC, 0); PIN(SC); SBAR(); \
    oacc = pv_mma<1>(vf1, SN, oacc, spidx); vf1 = lds_frag(vp1 + (T) * KVT); SBAR(); \
    EX2(SC, 2); S1(SC, 4); PIN(SC); SBAR(); \
    SN = MFMA(kf, qf, cinit); kf = lds_frag(kp + ((T) + 2) * KVT); SBAR(); \
    S1(SC, 5); EX2(SC, 6); PIN(SC); SBAR(); \
    G0; SBAR(); \
    EX2(SC, 8); S1(SC, 10); PIN(SC); SBAR(); \
    oacc = pv_mma<0>(vf0, SC, oacc, spidx); vf0 = lds_frag(vp0 + ((T) + 1) * KVT); SBAR(); \
    S1(SC, 11); MEM; EX2(SC, 12); PIN(SC); SBAR(); \
    G1; SBAR(); \
    EX2(SC, 14); PIN(SC); SBAR(); } while (0)
#define NOP (void)0
#define ITER2(T, M0, M1) do { ITER(sA, sB, T, (2 - (T)) & 3, NOP, M0, NOP, NOP); ITER(sB, sA, (T) + 1, (1 - (T)) & 3, NOP, M1, NOP, NOP); } while (0)
#define ADV(n) do { kp += (n) * KVT; vp0 += (n) * KVT; vp1 += (n) * KVT; PIN(kp); PIN(vp0); PIN(vp1); } while (0)
#define H_ST(i) bstore_sc1(hs[i], rs_h, hb + (((i) >> 2) * 112 + ((i) & 3)) * 1024)
    STAMP(4);
    ITER2(0, H_ST(0); H_ST(1), H_ST(2); H_ST(3));
    ITER2(2, H_ST(4); H_ST(5), H_ST(6); H_ST(7));
    ITER2(4, bstore_sc1(omark, rs_o, o_own); kv_tile(24 + w), kv_tile(32 + w));
    BAR_VL(13);
    ITER2(6, kv_tile(40 + w), kv_tile(48 + w));
    ITER2(8, kv_tile(56 + w), NOP);
    ITER2(10, NOP, NOP);
    BAR_VL(0);
    if (tid == 0) __hip_atomic_store((gu64*)(ws + WS_HFLAG) + (R * 16 + cb) * 16, (unsigned long long)epoch << 32, RLX_AGENT);
    STAMP(5);
    const char* wsrc = (const char*)w2 + w * (14 * 16384);
    const char* const wow = (const char*)wo + w * 65536;
    const unsigned w_rd0 = w * 2048 + (4 * h + q4) * 128 + ((q4 >> 1) << 6) + tr_in;
    const lptr wrA0 = lds + L_WB + w_rd0, wrA1 = lds + L_WB + (w_rd0 ^ 64), wrB0 = lds + w_rd0, wrB1 = lds + (w_rd0 ^ 64);
    const unsigned wbA = lds0 + L_WB + w * 2048, wbB = lds0 + w * 2048;
    const unsigned h_voff = (unsigned)((row64 >> 5) * 112 + 14 * w) * 1024u + lane * 16;
    u32x4 hA0, hA1, hB0, hB1;
    s16x8 wf0, wf1;
#define W_LD(src, buf) glds16x2(src, w64_src, w64_src1, buf)
#define H_LD(reg, step, tile) reg = bload_sc1(rs_h, h_voff, ((tile) * 112 + (step)) * 1024)
    unsigned long long hfv;
    ITER2(12, NOP, NOP);
    ITER2(14, W_LD(wsrc, wbA), W_LD(wsrc + 16384, wbB));
    ITER2(16, NOP, hfv = flag_load(hflag));
    ITER(sA, sB, 18, 0, NOP, NOP, NOP, NOP);
    STAMP(6);
    wait_flags(hflag, hfv, epoch);
    STAMP(7);
    ITER(sB, sA, 19, 3, NOP, H_LD(hA0, 0, 0); H_LD(hA1, 0, 1), NOP, NOP);
    ITER2(20, H_LD(hB0, 1, 0), H_LD(hB1, 1, 1));
    ADV(22);
    a00 = (f32x16){}; a01 = (f32x16){}; a10 = (f32x16){}; a11 = (f32x16){};
    STAMP(8);
#define GM(acc, wf, H) acc = MFMA(wf, asfrag(H), acc); SBAR()
#define GSTEP(SX, SY, T, R0, R1, H0, H1, LW, LH0, LH1) do { \
    ITER(SX, SY, T, 2, WAIT_V(5); wf0 = tr_frag(R0, 1024); wf1 = tr_frag(R1, 1024), NOP, NOP, GM(a00, wf0, H0)); \
    ITER(SY, SX, (T) + 1, 1, NOP, LW, GM(a01, wf1, H0); LH0, GM(a10, wf0, H1)); \
    ITER(SX, SY, (T) + 2, 0, NOP, NOP, NOP, GM(a11, wf1, H1); LH1); } while (0)
#pragma unroll 1
    for (int n = 0; n < 14; n += 2) {
        wsrc = n == 12 ? wow : wsrc + 32768;
        GSTEP(sA, sB, 0, wrA0, wrA1, hA0, hA1, W_LD(wsrc, wbA), H_LD(hA0, n + 2, 0), H_LD(hA1, n + 2, 1));
        GSTEP(sB, sA, 3, wrB0, wrB1, hB0, hB1, W_LD(wsrc + 16384, wbB), H_LD(hB0, n + 3, 0), H_LD(hB1, n + 3, 1));
        ADV(6);
    }
    STAMP(9);
    __builtin_amdgcn_s_setprio(0);
    oacc = pv_mma<1>(vf1, sB, oacc, spidx);
    {
        u32x4 v = pack8<0>(oacc, __builtin_amdgcn_rcpf(oacc[8]) * (rt ? gate1 : gate0));
#pragma unroll
        for (int e = 0; e < 4; ++e) v[e] = v[e] < 0xfffffffeu ? v[e] : 0xfffffffeu;
        bstore_sc1(v, rs_o, o_own);
        asm volatile("s_nop 1");
    }
    s16x8 wof[8];
    WAIT_V(2);
    wof[0] = tr_frag(wrA0, 1024); wof[1] = tr_frag(wrA1, 1024); wof[2] = tr_frag(wrB0, 1024); wof[3] = tr_frag(wrB1, 1024);
    asm volatile("s_waitcnt lgkmcnt(0)" : "+v"(wof[0]), "+v"(wof[1]), "+v"(wof[2]), "+v"(wof[3]) :: "memory");
    W_LD(wow + 32768, wbA); W_LD(wow + 49152, wbB);
    {
        const float og0 = rbf(1.f - gate0), og1 = rbf(1.f - gate1);
        a00 *= og0; a01 *= og0; a10 *= og1; a11 *= og1;
        PIN(a00); PIN(a01); PIN(a10); PIN(a11);
    }
    const unsigned ob = (unsigned)((row64 >> 5) * 32 + 4 * w) * 1024u;
    u32x4 of[8];
    auto o_load = [&]() {
#pragma unroll
        for (int i = 0; i < 8; ++i) of[i] = bload_sc1(rs_o, lane * 16 + (i & 3) * 1024, ob + (i >> 2) * 32768);
    };
    auto o_ready = [&]() {
        unsigned m = 0;
#pragma unroll
        for (int i = 0; i < 8; ++i) { m = max(m, max(of[i][0], of[i][1])); m = max(m, max(of[i][2], of[i][3])); }
        return __all(m != ~0u);
    };
    WAIT_V(0);
    wof[4] = tr_frag(wrA0, 1024); wof[5] = tr_frag(wrA1, 1024); wof[6] = tr_frag(wrB0, 1024); wof[7] = tr_frag(wrB1, 1024);
    BAR_L();
    STAMP(10);
    o_load();
    if (!o_ready()) {
        unsigned spins = 0;
        do { CFENCE(); o_load(); } while (!o_ready() && ++spins < SPIN_CAP);
    }
    STAMP(11);
    const lptr red_w = lds + w * 16384 + r * 256;
    auto put = [&](unsigned off, const f32x16& a) {
#pragma unroll
        for (int gq = 0; gq < 4; ++gq) *(LAS f32x4*)(red_w + off + (((2 * gq + h) ^ (r & 7)) << 4)) = (f32x4){a[4 * gq], a[4 * gq + 1], a[4 * gq + 2], a[4 * gq + 3]};
    };
#pragma unroll
    for (int i = 0; i < 4; ++i) a00 = MFMA(wof[2 * i], asfrag(of[i]), a00);
#pragma unroll
    for (int i = 0; i < 4; ++i) a01 = MFMA(wof[2 * i + 1], asfrag(of[i]), a01);
    SBAR(); put(0, a00); SBAR();
#pragma unroll
    for (int i = 0; i < 4; ++i) a10 = MFMA(wof[2 * i], asfrag(of[4 + i]), a10);
    SBAR(); put(128, a01); SBAR();
#pragma unroll
    for (int i = 0; i < 4; ++i) a11 = MFMA(wof[2 * i + 1], asfrag(of[4 + i]), a11);
    put(8192, a10); put(8192 + 128, a11);
    unsigned long long ef[4];
    gu64* const efp = (gu64*)(ws + WS_HFLAG) + lane * 16;
    if (b == 112 && w == 0) {
#pragma unroll
        for (int i = 0; i < 4; ++i) ef[i] = flag_load(efp + i * 1024);
    }
    BAR_L();
    {
        const lptr red_r = lds + (8 * w + (lane >> 3)) * 256;
        const unsigned rc0 = ((2 * (lane & 7)) ^ ((lane >> 3) & 7)) << 4, rc1 = rc0 ^ 16;
        f32x4 lo = *(const LAS f32x4*)(red_r + rc0), hi = *(const LAS f32x4*)(red_r + rc1);
#pragma unroll
        for (int k = 1; k < 8; ++k) { lo += *(const LAS f32x4*)(red_r + k * 16384 + rc0); hi += *(const LAS f32x4*)(red_r + k * 16384 + rc1); }
        const u32x4 v = {cvtpk(lo[0], lo[1]), cvtpk(lo[2], lo[3]), cvtpk(hi[0], hi[1]), cvtpk(hi[2], hi[3])};
        *(u32x4*)(out + (row64 + 8 * w + (lane >> 3)) * 512 + hg * 64 + (lane & 7) * 8) = v;
    }
    if (b == 112 && w == 0) {
#pragma unroll
        for (int i = 0; i < 4; ++i) wait_flags(efp + i * 1024, ef[i], epoch);
        if (lane == 0) __hip_atomic_store((gu32*)(ws + WS_EPOCH), epoch, RLX_AGENT);
    }
    STAMP(12);
}

extern "C" void kernel_launch(void* const* d_in, const int* in_sizes, int n_in, void* d_out, int out_size, void* d_ws, size_t ws_size,
                              hipStream_t stream) {
    fused_kernel<<<256, 512, 0, stream>>>((const uint16_t*)d_in[0], (const uint16_t*)d_in[1], (const uint16_t*)d_in[2], (const uint16_t*)d_in[3],
                                          (const uint16_t*)d_in[4], (const uint16_t*)d_in[6], (const uint16_t*)d_in[7], (const uint16_t*)d_in[8],
                                          (uint16_t*)d_out, (char*)d_ws);
}
```

```cpp
#include <hip/hip_runtime.h>
#include <stdint.h>

typedef short s16x8 __attribute__((ext_vector_type(8)));
typedef short s16x4 __attribute__((ext_vector_type(4)));
typedef short s16x2 __attribute__((ext_vector_type(2)));
typedef float f32x16 __attribute__((ext_vector_type(16)));
typedef float f32x4 __attribute__((ext_vector_type(4)));
typedef float f32x2 __attribute__((ext_vector_type(2)));
typedef unsigned u32x4 __attribute__((ext_vector_type(4)));
typedef __bf16 bf16x2 __attribute__((ext_vector_type(2)));
typedef __bf16 bf16x8 __attribute__((ext_vector_type(8)));
typedef __bf16 bf16x16 __attribute__((ext_vector_type(16)));
#define LAS __attribute__((address_space(3)))
#define GAS __attribute__((address_space(1)))
typedef GAS unsigned gu32;
typedef GAS unsigned long long gu64;
typedef LAS char* lptr;
#define DI __device__ __forceinline__
#define RLX_AGENT __ATOMIC_RELAXED, __HIP_MEMORY_SCOPE_AGENT
#define MFMA(a, b, c) __builtin_amdgcn_mfma_f32_32x32x16_bf16((a), (b), (c), 0, 0, 0)
#define SBAR() __builtin_amdgcn_sched_barrier(0)
#define PIN(x) asm volatile("" : "+v"(x))
#define CFENCE() do { asm volatile("" ::: "memory"); __builtin_amdgcn_fence(__ATOMIC_ACQUIRE, "wavefront"); } while (0)
#define BAR_L() asm volatile("s_waitcnt lgkmcnt(0)\n\ts_barrier" ::: "memory")
#define BAR_VL(N) asm volatile("s_waitcnt vmcnt(" #N ") lgkmcnt(0)\n\ts_barrier" ::: "memory")

#define ABL 0
#define STW 0
#define STAMPS 0

constexpr int D = 512, DFF = 1792;
constexpr float QSCALE = 0.25f * 1.4426950408889634f;
constexpr float SAFE_RANGE = 64.f;
constexpr unsigned WS_EPOCH = 0, WS_KVFLAG = 4096, WS_HFLAG = 8192;
constexpr unsigned WS_KV = WS_HFLAG + 32768, KV_BYTES = 64 * 2048;
constexpr unsigned WS_H = WS_KV + KV_BYTES, H_BYTES = 64 * 112 * 1024;
constexpr unsigned WS_O = WS_H + H_BYTES, O_BYTES = 64 * 32 * 1024;
constexpr unsigned WS_STAMP = WS_O + O_BYTES;
constexpr unsigned OOB = 0x7ff00000u;
constexpr unsigned L_STAGE = 40960;
constexpr unsigned L_KSTAGE = 12288, L_KWQ = 98304;
constexpr unsigned KVT = 2080;
constexpr unsigned L_WB = 64 * KVT;
constexpr unsigned L_QX = L_WB + 16384, L_GW = L_QX + 8192, L_GZ = L_GW + 2048, L_KM = L_GZ + 512, L_TOUCH = L_KM + 256, LDS_BYTES = 163840;
static_assert(L_TOUCH + 256 <= LDS_BYTES, "lds map");
constexpr unsigned SPIN_CAP = 1u << 16;

DI unsigned cvtpk(float a, float b) { f32x2 v = {a, b}; return __builtin_bit_cast(unsigned, __builtin_convertvector(v, bf16x2)); }
DI float rbf(float x) { return __uint_as_float(cvtpk(x, x) << 16); }
DI s16x8 asfrag(u32x4 v) { return __builtin_bit_cast(s16x8, v); }
DI s16x8 lds_frag(lptr p) { return *(const LAS s16x8*)p; }
DI void lds_put(lptr p, u32x4 v) { *(LAS u32x4*)p = v; }
DI s16x4 lds_tr(lptr p) { return __builtin_amdgcn_ds_read_tr16_b64_v4i16((LAS s16x4*)p); }
DI s16x8 tr_frag2(lptr p, lptr q) { s16x4 lo = lds_tr(p), hi = lds_tr(q); return __builtin_shufflevector(lo, hi, 0, 1, 2, 3, 4, 5, 6, 7); }
DI s16x8 tr_frag(lptr p, int second) { return tr_frag2(p, p + second); }
DI float swap_add(float v) { auto r = __builtin_amdgcn_permlane32_swap(__float_as_uint(v), __float_as_uint(v), false, false); return __uint_as_float(r[0]) + __uint_as_float(r[1]); }
DI float swap_max(float v) { auto r = __builtin_amdgcn_permlane32_swap(__float_as_uint(v), __float_as_uint(v), false, false); return fmaxf(__uint_as_float(r[0]), __uint_as_float(r[1])); }
DI float dot2(unsigned a, unsigned b, float c) { return __builtin_amdgcn_fdot2_f32_bf16(__builtin_bit_cast(bf16x2, a), __builtin_bit_cast(bf16x2, b), c, false); }
DI unsigned relu_pk(unsigned u) { return __builtin_bit_cast(unsigned, __builtin_elementwise_max(__builtin_bit_cast(s16x2, u), (s16x2){0, 0})); }
template <int CTRL> DI float dpp(float v) { return __uint_as_float(__builtin_amdgcn_update_dpp(0, __float_as_uint(v), CTRL, 0xf, 0xf, true)); }
DI float row32_max(float v) {
    v = fmaxf(v, dpp<0xB1>(v)); v = fmaxf(v, dpp<0x4E>(v)); v = fmaxf(v, dpp<0x141>(v)); v = fmaxf(v, dpp<0x140>(v));
    auto r = __builtin_amdgcn_permlane16_swap(__float_as_uint(v), __float_as_uint(v), false, false);
    return fmaxf(__uint_as_float(r[0]), __uint_as_float(r[1]));
}
template <int HALF> DI u32x4 pack8(const f32x16& a, float m) {
    u32x4 v;
#pragma unroll
    for (int e = 0; e < 4; ++e) v[e] = cvtpk(a[8 * HALF + 2 * e] * m, a[8 * HALF + 2 * e + 1] * m);
    return v;
}
template <int HALF> DI u32x4 pack8_relu(const f32x16& a) {
    u32x4 v;
#pragma unroll
    for (int e = 0; e < 4; ++e) v[e] = relu_pk(cvtpk(a[8 * HALF + 2 * e], a[8 * HALF + 2 * e + 1]));
    return v;
}
template <int HALF> DI f32x16 pv_mma(s16x8 v, const f32x16& p, f32x16 c, int idx) {
    const auto ph = __builtin_shufflevector(p, p, 8 * HALF, 8 * HALF + 1, 8 * HALF + 2, 8 * HALF + 3, 8 * HALF + 4, 8 * HALF + 5, 8 * HALF + 6, 8 * HALF + 7);
    return __builtin_amdgcn_smfmac_f32_32x32x32_bf16(__builtin_bit_cast(bf16x8, v), __builtin_bit_cast(bf16x16, ph), c, idx, 0, 0);
}
DI void glds16(const void* sbase, unsigned voff, unsigned lds_base) {
    asm volatile("s_mov_b32 m0, %2\n\ts_nop 0\n\tglobal_load_lds_dwordx4 %0, %1" :: "v"(voff), "s"(sbase), "s"(lds_base) : "memory");
}
DI void glds16x2(const void* sbase, unsigned voff0, unsigned voff1, unsigned lds_base) {
    asm volatile("s_mov_b32 m0, %3\n\ts_nop 0\n\tglobal_load_lds_dwordx4 %0, %2\n\tglobal_load_lds_dwordx4 %1, %2 offset:1024" :: "v"(voff0), "v"(voff1), "s"(sbase), "s"(lds_base) : "memory");
}
DI void glds16x4(const void* sbase, unsigned v0, unsigned v1, unsigned v2, unsigned v3, unsigned lds_base) {
    asm volatile("s_mov_b32 m0, %5\n\ts_nop 0\n\tglobal_load_lds_dwordx4 %0, %4\n\tglobal_load_lds_dwordx4 %1, %4 offset:1024\n\t"
                 "global_load_lds_dwordx4 %2, %4 offset:2048\n\tglobal_load_lds_dwordx4 %3, %4 offset:3072" :: "v"(v0), "v"(v1), "v"(v2), "v"(v3), "s"(sbase), "s"(lds_base) : "memory");
}
DI void glds16x2_sc1(const void* sbase, unsigned voff, unsigned lds_base) {
    asm volatile("s_mov_b32 m0, %2\n\ts_nop 0\n\tglobal_load_lds_dwordx4 %0, %1 sc1\n\tglobal_load_lds_dwordx4 %0, %1 offset:1024 sc1" :: "v"(voff), "s"(sbase), "s"(lds_base) : "memory");
}
DI void touch8k(const void* sbase, int lane, unsigned lds_scratch) {
    asm volatile("s_mov_b32 m0, %2\n\ts_nop 0\n\tglobal_load_lds_dword %0, %1" :: "v"(lane * 128), "s"(sbase), "s"(lds_scratch) : "memory");
}
#define WAIT_V(N) asm volatile("s_waitcnt vmcnt(" #N ")" ::: "memory")
DI __amdgpu_buffer_rsrc_t mk_rsrc(const void* p, unsigned bytes) { return __builtin_amdgcn_make_buffer_rsrc((void*)p, 0, bytes, 0x00020000); }
DI u32x4 bload(__amdgpu_buffer_rsrc_t r, unsigned voff, unsigned soff) { return __builtin_amdgcn_raw_buffer_load_b128(r, voff, soff, 0); }
DI u32x4 bload_sc1(__amdgpu_buffer_rsrc_t r, unsigned voff, unsigned soff) { return __builtin_amdgcn_raw_buffer_load_b128(r, voff, soff, 16); }
DI void bstore_sc1(u32x4 v, __amdgpu_buffer_rsrc_t r, unsigned voff) { __builtin_amdgcn_raw_buffer_store_b128(v, r, voff, 0, 16); }

#define STAMP(i) do { } while (0)

DI unsigned long long flag_load(gu64* f) { return __hip_atomic_load(f, RLX_AGENT); }
DI unsigned wait_flags(gu64* f, unsigned long long v, unsigned epoch) {
    if (__builtin_expect(!__all((unsigned)(v >> 32) == epoch), 0)) {
        unsigned spins = 0;
        do { __builtin_amdgcn_s_sleep(2); v = flag_load(f); } while (!__all((unsigned)(v >> 32) == epoch) && ++spins < SPIN_CAP);
    }
    CFENCE();
    return (unsigned)v;
}

struct BigFrag { s16x8 x0, x1, w0, w1; };
struct SmallFrag { s16x8 x, q; u32x4 g; };

__global__ void __launch_bounds__(512, 2)
fused_kernel(const uint16_t* __restrict__ x, const uint16_t* __restrict__ wq, const uint16_t* __restrict__ wk, const uint16_t* __restrict__ wv,
             const uint16_t* __restrict__ wo, const uint16_t* __restrict__ w1, const uint16_t* __restrict__ w2, const uint16_t* __restrict__ gw,
             uint16_t* out, char* ws) {
    __shared__ __attribute__((aligned(16))) char smem[LDS_BYTES];
    const lptr lds = (lptr)smem;
    const unsigned lds0 = (unsigned)(uintptr_t)smem;
    const int tid = threadIdx.x, lane = tid & 63, w = __builtin_amdgcn_readfirstlane(tid >> 6);
    const int r = lane & 31, h = lane >> 5, g1 = (lane >> 4) & 1, q4 = (lane & 15) >> 2, p4 = lane & 3;
    const int b = blockIdx.x, bi = b >> 3;
    const int R = 2 * (b & 7) + (bi >> 4), cb = bi & 15, rh64 = cb & 1, hg = cb >> 1;
    const bool kvcu = cb >= 14, big = w < 4;
    const int row64 = R * 128 + rh64 * 64;
    const int rt = w & 1, hq = w >> 1, fh = hq & 1;
    unsigned stv = 0;
    STAMP(0);

    const unsigned epoch_v = __hip_atomic_load((gu32*)(ws + WS_EPOCH), RLX_AGENT);
    const unsigned w64_src = (unsigned)((lane >> 3) * 1024 + hg * 128 + (((lane & 7) ^ (g1 << 2)) << 4));
    const unsigned w64_src1 = w64_src + 8192 - 1024;
    const __amdgpu_buffer_rsrc_t rs_kv = mk_rsrc(ws + WS_KV, KV_BYTES), rs_h = mk_rsrc(ws + WS_H, H_BYTES), rs_o = mk_rsrc(ws + WS_O, O_BYTES);
    const unsigned o_own = (unsigned)(((row64 >> 5) + rt) * 32 + hg * 4 + hq) * 1024u + lane * 16;
    const u32x4 omark = {~0u, ~0u, ~0u, ~0u};
    gu64* const kvflag = (gu64*)(ws + WS_KVFLAG) + r * 16;
    gu64* const hflag = (gu64*)(ws + WS_HFLAG) + (R * 16 + (lane & 15)) * 16;
    const unsigned tr_in = 32 * g1 + 8 * p4;
    const unsigned wq_rd = (8 * h + q4) * 128 + ((fh ^ (q4 >> 1)) << 6) + tr_in;
    unsigned xo[4];
#pragma unroll
    for (int ks = 0; ks < 4; ++ks) xo[ks] = r * 128 + (((2 * ks + h) ^ ((r >> 1) & 7)) << 4);

    f32x16 a00 = {}, a01 = {}, a10 = {}, a11 = {};
    auto kv_wait = [&](unsigned long long fv) {
        const float km = row32_max(__uint_as_float(wait_flags(kvflag, fv, __builtin_amdgcn_readfirstlane(epoch_v) + 1u)));
        if (tid == 0) *(LAS float*)(lds + L_KM) = km;
    };
    auto kv_tile = [&](int t) { glds16x2_sc1(ws + WS_KV + t * 2048, lane * 16, lds0 + t * KVT); };
    if (!kvcu) {
        const unsigned xv = (unsigned)((R * 128 + (lane >> 3)) * 1024 + (((lane & 7) ^ ((lane >> 4) & 3)) << 4));
        const unsigned wv0 = (unsigned)((lane >> 4) * (DFF * 2) + cb * 256 + (((lane & 15) ^ (((lane >> 4) & 3) << 2)) << 4));
        const unsigned xv1 = (xv ^ 64) + 8192 - 1024, wv1 = wv0 + 4 * DFF * 2 - 1024;
#pragma unroll
        for (int s = 0; s < 2; ++s) {
            glds16x2((const char*)x + s * 128 + w * 16384, xv, xv1, lds0 + s * L_STAGE + w * 2048);
            glds16x2((const char*)w1 + (size_t)(s * 64 + w * 8) * (DFF * 2), wv0, wv1, lds0 + s * L_STAGE + 16384 + w * 2048);
            glds16((const char*)wq + s * 65536 + w * 8192, w64_src, lds0 + s * L_STAGE + 32768 + w * 1024);
            if (s == 0 && w == 7) glds16(gw, lane * 16, lds0 + L_GW);
        }
        unsigned cur = 0, nxt = L_STAGE;
        if (big) {
            const unsigned xb = rt * 8192, w0o = 16384 + (8 * h + q4) * 256 + (((2 * fh) ^ q4) << 6) + tr_in, w1o = w0o ^ 64;
            BigFrag F[4];
            auto rd = [&](BigFrag& n, lptr sb, int ks) {
                n.x0 = lds_frag(sb + xb + xo[ks]); n.x1 = lds_frag(sb + xb + 4096 + xo[ks]); n.w0 = tr_frag(sb + w0o + ks * 4096, 1024); n.w1 = tr_frag(sb + w1o + ks * 4096, 1024); };
#define AM(e, ...) do { if (ABL & 2048) asm volatile("" :: __VA_ARGS__); else e; SBAR(); } while (0)
#define AR(e, ...) do { if (ABL & 4096) asm volatile("" : __VA_ARGS__); else { e; } SBAR(); } while (0)
            auto step = [&](const BigFrag& c, BigFrag& n, lptr sb, int ks) {
                AM(a00 = MFMA(c.w0, c.x0, a00), "v"(c.w0), "v"(c.x0)); AR(n.x0 = lds_frag(sb + xb + xo[ks]); n.x1 = lds_frag(sb + xb + 4096 + xo[ks]), "+v"(n.x0), "+v"(n.x1));
                AM(a10 = MFMA(c.w0, c.x1, a10), "v"(c.x1)); AR(n.w0 = tr_frag(sb + w0o + ks * 4096, 1024), "+v"(n.w0));
                AM(a01 = MFMA(c.w1, c.x0, a01), "v"(c.w1)); AR(n.w1 = tr_frag(sb + w1o + ks * 4096, 1024), "+v"(n.w1));
                AM(a11 = MFMA(c.w1, c.x1, a11), "v"(c.w1)); };
            auto stage = [&](auto bar) {
                step(F[0], F[3], lds + cur, 3);
                bar();
                step(F[1], F[0], lds + nxt, 0); step(F[2], F[1], lds + nxt, 1); step(F[3], F[2], lds + nxt, 2);
                cur = nxt; nxt = (nxt == 2 * L_STAGE) ? 0 : nxt + L_STAGE; };
            {
                const int f = R * 14 + cb;
                const char* t = w == 0 ? (const char*)w1 + f * 8192 : w == 1 ? (const char*)x + (R * 16 + cb) * 8192
                              : w == 2 ? (const char*)x + (R * 16 + 14 + (cb & 1)) * 8192 : (const char*)wq + (f & 63) * 8192;
                touch8k(t, lane, lds0 + L_TOUCH);
            }
            STAMP(13);
            BAR_VL(6);
            STAMP(14);
            rd(F[0], lds, 0); rd(F[1], lds, 1); rd(F[2], lds, 2);
#pragma unroll 1
            for (int s = 0; s < 5; ++s) stage([&] { BAR_VL(1); });
            const unsigned long long fv = flag_load(kvflag);
            stage([&] { BAR_L(); });
            STAMP(15);
            kv_wait(fv);
            STAMP(16);
            stage([&] { BAR_L(); kv_tile(2 * w); kv_tile(2 * w + 1); });
            STAMP(1);
            rd(F[3], lds + cur, 3);
#pragma unroll
            for (int ks = 0; ks < 4; ++ks) { const BigFrag& c = F[ks]; a00 = MFMA(c.w0, c.x0, a00); a10 = MFMA(c.w0, c.x1, a10); a01 = MFMA(c.w1, c.x0, a01); a11 = MFMA(c.w1, c.x1, a11); }
        } else {
            const int j = w - 4;
            __builtin_amdgcn_s_setprio(2);
            const unsigned xb = (2 * rh64 + rt) * 4096, qo = 32768 + wq_rd;
            lptr gp = lds + L_GW + h * 16 + fh * 32;
            float gz0 = 0.f;
            SmallFrag F[4];
            auto rd = [&](SmallFrag& n, lptr sb, int ks) { n.x = lds_frag(sb + xb + xo[ks]); n.q = tr_frag(sb + qo + ks * 2048, 512); if (!(ks & 1)) n.g = *(const LAS u32x4*)(gp + ks * 32); SBAR(); };
            auto mma2 = [&](const SmallFrag& c0, const SmallFrag& c1) {
                if (ABL & 64) return;
                const u32x4 xu = __builtin_bit_cast(u32x4, fh ? c1.x : c0.x);
#pragma unroll
                for (int e = 0; e < 4; ++e) {
                    gz0 = fmaf(__uint_as_float(xu[e] << 16), __uint_as_float(c0.g[e] << 16), gz0);
                    gz0 = fmaf(__uint_as_float(xu[e] & 0xffff0000u), __uint_as_float(c0.g[e] & 0xffff0000u), gz0);
                }
                PIN(gz0);
                SBAR(); };
            auto mma = [&](const SmallFrag& c) { a00 = MFMA(c.q, c.x, a00); SBAR(); };
            const unsigned xv2 = xv + 2 * (8192 - 1024), xv3 = xv1 + 2 * (8192 - 1024), wv2 = wv0 + 2 * (4 * DFF * 2 - 1024), wv3 = wv1 + 2 * (4 * DFF * 2 - 1024);
            const char* xs = (const char*)x + 2 * 128 + j * 32768;
            const char* w1s = (const char*)w1 + (size_t)(2 * 64 + j * 16) * (DFF * 2);
            const char* wqs = (const char*)wq + 2 * 65536 + j * 16384;
            auto fill0 = [&](unsigned slot) { glds16x4(xs, xv, xv1, xv2, xv3, lds0 + slot + j * 4096); xs += 128; };
            auto fill1 = [&](unsigned slot) { glds16x4(w1s, wv0, wv1, wv2, wv3, lds0 + slot + 16384 + j * 4096); w1s += 64 * DFF * 2; };
            auto fill2 = [&](unsigned slot) { glds16x2(wqs, w64_src, w64_src1, lds0 + slot + 32768 + j * 2048); wqs += 65536; };
            BAR_VL(5);
            rd(F[0], lds, 0); rd(F[1], lds, 1); rd(F[2], lds, 2);
            fill0(2 * L_STAGE); fill1(2 * L_STAGE); fill2(2 * L_STAGE);
#pragma unroll 1
            for (int s = 0; s < 7; ++s) {
                const bool more = (ABL & 1024) ? false : s < 5;
                rd(F[3], lds + cur, 3); mma(F[0]); mma2(F[0], F[1]);
                gp += 128;
                if (s < 6) BAR_VL(10); else BAR_VL(0);
                if (more) fill0(cur);
                rd(F[0], lds + nxt, 0); mma(F[1]);
                rd(F[1], lds + nxt, 1); mma(F[2]);
                if (more) fill1(cur);
                mma2(F[2], F[3]);
                rd(F[2], lds + nxt, 2); mma(F[3]);
                if (more) fill2(cur);
                cur = nxt; nxt = (nxt == 2 * L_STAGE) ? 0 : nxt + L_STAGE;
            }
            rd(F[3], lds + cur, 3);
            mma(F[0]); mma(F[1]); mma2(F[0], F[1]); mma(F[2]); mma(F[3]); mma2(F[2], F[3]);
            const float gz = swap_add(gz0);
            if (h == 0) *(LAS float*)(lds + L_GZ + fh * 256 + (rt * 32 + r) * 4) = gz;
        }
    } else {
        const unsigned xv0 = (unsigned)((row64 + 8 * w + (lane >> 3)) * 1024 + (((lane & 7) ^ ((4 * w + (lane >> 4)) & 7)) << 4));
        const unsigned xb = rt * 4096;
        if (big) {
#pragma unroll 1
            for (int s = 0; s < 8; ++s) glds16((const char*)x + s * 128, xv0, lds0 + s * L_KSTAGE + w * 1024);
            const unsigned wkv_rd = 8192 + g1 * 2048 + (((8 * h + q4) * 32 + 8 * p4) ^ (g1 << 7));
#pragma unroll 1
            for (int g = 0; g < 4; ++g) {
                if (g == 0) BAR_VL(6); else if (g == 1) BAR_VL(4); else if (g == 2) BAR_VL(2); else BAR_VL(0);
                const lptr sb = lds + g * 2 * L_KSTAGE;
                s16x8 xf[8], wf[8];
#pragma unroll
                for (int k = 0; k < 8; ++k) {
                    const lptr st = sb + (k >> 2) * L_KSTAGE;
                    xf[k] = lds_frag(st + xb + xo[k & 3]); wf[k] = tr_frag2(st + wkv_rd + (k & 3) * 512, st + (wkv_rd ^ 128) + (k & 3) * 512);
                }
                if (fh == 0) { _Pragma("unroll") for (int k = 0; k < 8; ++k) a00 = MFMA(wf[k], xf[k], a00); }
                else { _Pragma("unroll") for (int k = 0; k < 8; ++k) a00 = MFMA(xf[k], wf[k], a00); }
            }
            STAMP(1);
            const unsigned kvo = (unsigned)((row64 >> 5) + rt) * 2048u;
            if (fh == 0) {
                const u32x4 v = pack8<1>(a00, 1.f);
                bstore_sc1(v, rs_kv, lane * 16 + kvo);
                float n2 = 0.f;
#pragma unroll
                for (int e = 0; e < 4; ++e) n2 = dot2(v[e], v[e], n2);
                n2 = row32_max(swap_add(n2));
                if (lane == 0) *(LAS float*)(lds + 8192 + rt * 4) = n2;
            } else {
#pragma unroll
                for (int s = 0; s < 2; ++s) {
                    const u32x4 p = s ? pack8<1>(a00, 1.f) : pack8<0>(a00, 1.f);
                    const auto t0 = __builtin_amdgcn_permlane32_swap(p[0], p[2], false, false), t1 = __builtin_amdgcn_permlane32_swap(p[1], p[3], false, false);
                    bstore_sc1((u32x4){t0[0], t1[0], t0[1], t1[1]}, rs_kv, r < 16 ? (unsigned)(h * 256 + r * 16) + kvo + 1024 + s * 512 : OOB);
                }
            }
            BAR_VL(0);
            if (tid == 0) {
                const float n2 = fmaxf(*(LAS float*)(lds + 8192), *(LAS float*)(lds + 8196));
                __hip_atomic_store((gu64*)(ws + WS_KVFLAG) + (row64 >> 6) * 16, ((unsigned long long)(epoch_v + 1u) << 32) | __float_as_uint(n2), RLX_AGENT);
            }
            BAR_L();
        } else {
            const char* wkv = (const char*)((w & 2) ? wk : wv);
            const unsigned kvv = (w & 1) * 1024 + ((lane ^ ((w & 2) << 2)) << 4);
#pragma unroll 1
            for (int s = 0; s < 8; ++s) {
                glds16((const char*)x + s * 128, xv0, lds0 + s * L_KSTAGE + w * 1024);
                glds16(wkv + s * 2048, kvv, lds0 + s * L_KSTAGE + 8192 + (w & 3) * 1024);
            }
#pragma unroll 1
            for (int g = 0; g < 4; ++g) {
                BAR_VL(12);
#pragma unroll
                for (int i = 0; i < 4; ++i) { const int p = g * 16 + (w - 4) * 4 + i; glds16((const char*)wq + p * 8192, w64_src, lds0 + L_KWQ + p * 1024); }
            }
            BAR_L();
            BAR_VL(0);
        }
        float gz = 0.f;
        if (big) {
#pragma unroll 4
            for (int i = 0; i < 16; ++i) {
                const u32x4 gx = *(const u32x4*)(x + (row64 + 16 * w + (lane >> 2)) * 512 + (i * 4 + p4) * 8), gg = *(const u32x4*)(gw + (i * 4 + p4) * 8);
#pragma unroll
                for (int e = 0; e < 4; ++e) gz = dot2(gx[e], gg[e], gz);
            }
            gz += dpp<0xB1>(gz); gz += dpp<0x4E>(gz);
        } else {
#pragma unroll 1
            for (int s = 0; s < 8; ++s) {
                s16x8 xf[4], wf[4];
#pragma unroll
                for (int k = 0; k < 4; ++k) { xf[k] = lds_frag(lds + s * L_KSTAGE + xb + xo[k]); wf[k] = tr_frag(lds + L_KWQ + s * 8192 + wq_rd + k * 2048, 512); }
#pragma unroll
                for (int k = 0; k < 4; ++k) a00 = MFMA(wf[k], xf[k], a00);
            }
        }
        BAR_L();
        if (big) {
            if (p4 < 2) *(LAS float*)(lds + L_GZ + p4 * 256 + (16 * w + (lane >> 2)) * 4) = p4 ? 0.f : gz;
            kv_wait(flag_load(kvflag));
            kv_tile(2 * w); kv_tile(2 * w + 1);
        }
    }
    STAMP(2);
    const unsigned epoch = __builtin_amdgcn_readfirstlane(epoch_v) + 1u;
    u32x4 hs[8];
    const unsigned hb = ((kvcu || !big) ? OOB : (unsigned)((R * 4 + 2 * rt) * 112 + cb * 8 + fh * 4) * 1024u) + lane * 16;
    const u32x4 ones = {0x3f803f80u, 0x3f803f80u, 0x3f803f80u, 0x3f803f80u};
    if (!big) {
        lds_put(lds + L_QX + (4 * fh + rt) * 1024 + lane * 16, pack8<0>(a00, QSCALE));
        lds_put(lds + L_QX + (4 * fh + 2 + rt) * 1024 + lane * 16, pack8<1>(a00, QSCALE));
        if (w == 4 && lane < 16) lds_put(lds + (lane >> 1) * KVT + 2048 + (lane & 1) * 16, (lane & 1) ? (u32x4){} : ones);
    }
    hs[0] = pack8_relu<0>(a00); hs[1] = pack8_relu<1>(a00); hs[2] = pack8_relu<0>(a01); hs[3] = pack8_relu<1>(a01);
    hs[4] = pack8_relu<0>(a10); hs[5] = pack8_relu<1>(a10); hs[6] = pack8_relu<0>(a11); hs[7] = pack8_relu<1>(a11);
    BAR_VL(0);
    STAMP(3);
    __builtin_amdgcn_s_setprio(3);
    lptr kp = lds + lane * 16;
    const unsigned v_cst = (r & 27) != 16 ? 2064 : 2048;
    lptr vp0 = lds + (r < 16 ? 1024 + h * 256 + r * 16 : v_cst), vp1 = lds + (r < 16 ? 1536 + h * 256 + r * 16 : v_cst);
    const s16x8 qf = lds_frag(lds + L_QX + (hq * 2 + rt) * 1024 + lane * 16);
    s16x8 kf = lds_frag(kp), vf0 = lds_frag(vp0), vf1 = {};
    const float kmax2 = *(const LAS float*)(lds + L_KM);
    const LAS float* gzp = (const LAS float*)(lds + L_GZ) + r;
    const float gz0 = gzp[0] + gzp[64], gz1 = gzp[32] + gzp[96];
    kv_tile(8 + w); kv_tile(16 + w);
    if (tid >= 16 && tid < 128) lds_put(lds + (tid >> 1) * KVT + 2048 + (tid & 1) * 16, (tid & 1) ? (u32x4){} : ones);
    float mshift;
    {
        const u32x4 qu = __builtin_bit_cast(u32x4, qf);
        float n2 = 0.f;
#pragma unroll
        for (int e = 0; e < 4; ++e) n2 = dot2(qu[e], qu[e], n2);
        mshift = __builtin_amdgcn_sqrtf(swap_add(n2) * kmax2) * 1.0001f;
        if (__builtin_expect(!__all(2.f * mshift <= SAFE_RANGE), 0)) {
            float m = -__builtin_inff();
#pragma unroll 1
            for (int t = 0; t < 64; ++t) {
                const f32x16 s = MFMA(asfrag(bload_sc1(rs_kv, lane * 16, t * 2048)), qf, (f32x16){});
#pragma unroll
                for (int i = 0; i < 16; ++i) m = fmaxf(m, s[i]);
            }
            mshift = swap_max(m);
        }
    }
    f32x16 cinit;
#pragma unroll
    for (int i = 0; i < 16; ++i) cinit[i] = -mshift;
    PIN(cinit);
    f32x16 sA = MFMA(kf, qf, cinit), sB = {}, oacc = {};
    kf = lds_frag(kp + KVT);
    float gate0 = rbf(__builtin_amdgcn_rcpf(1.f + __expf(-rbf(gz0)))), gate1 = rbf(__builtin_amdgcn_rcpf(1.f + __expf(-rbf(gz1))));
    PIN(gate0); PIN(gate1);
    int spidx = 0xDDDD; PIN(spidx);

#define S1(S_, i) S_[i] = __builtin_amdgcn_exp2f(S_[i])
#define EX2(S_, i) do { S1(S_, i); S1(S_, (i) + 1); } while (0)
#define ITER(SC, SN, T, PR, PRE, MEM, G0, G1) do { \
    __builtin_amdgcn_s_setprio(PR); \
    PRE; EX2(SC, 0); PIN(SC); SBAR(); \
    oacc = pv_mma<1>(vf1, SN, oacc, spidx); vf1 = lds_frag(vp1 + (T) * KVT); SBAR(); \
    EX2(SC, 2); S1(SC, 4); PIN(SC); SBAR(); \
    SN = MFMA(kf, qf, cinit); kf = lds_frag(kp + ((T) + 2) * KVT); SBAR(); \
    S1(SC, 5); EX2(SC, 6); PIN(SC); SBAR(); \
    G0; SBAR(); \
    EX2(SC, 8); S1(SC, 10); PIN(SC); SBAR(); \
    oacc = pv_mma<0>(vf0, SC, oacc, spidx); vf0 = lds_frag(vp0 + ((T) + 1) * KVT); SBAR(); \
    S1(SC, 11); MEM; EX2(SC, 12); PIN(SC); SBAR(); \
    G1; SBAR(); \
    EX2(SC, 14); PIN(SC); SBAR(); } while (0)
#define NOP (void)0
#define ITER2(T, M0, M1) do { ITER(sA, sB, T, (2 - (T)) & 3, NOP, M0, NOP, NOP); ITER(sB, sA, (T) + 1, (1 - (T)) & 3, NOP, M1, NOP, NOP); } while (0)
#define ADV(n) do { kp += (n) * KVT; vp0 += (n) * KVT; vp1 += (n) * KVT; PIN(kp); PIN(vp0); PIN(vp1); } while (0)
#define H_ST(i) bstore_sc1(hs[i], rs_h, hb + (((i) >> 2) * 112 + ((i) & 3)) * 1024)
    STAMP(4);
    ITER2(0, H_ST(0); H_ST(1), H_ST(2); H_ST(3));
    ITER2(2, H_ST(4); H_ST(5), H_ST(6); H_ST(7));
    ITER2(4, bstore_sc1(omark, rs_o, o_own); kv_tile(24 + w), kv_tile(32 + w));
    BAR_VL(13);
    ITER2(6, kv_tile(40 + w), kv_tile(48 + w));
    ITER2(8, kv_tile(56 + w), if (w < 2) touch8k(w == 0 ? (const char*)w2 + (b < 224 ? b : b - 32) * 8192 : (const char*)wo + (b & 63) * 8192, lane, lds0 + L_TOUCH));
    ITER2(10, NOP, NOP);
    BAR_VL(0);
    if (tid == 0) __hip_atomic_store((gu64*)(ws + WS_HFLAG) + (R * 16 + cb) * 16, (unsigned long long)epoch << 32, RLX_AGENT);
    STAMP(5);
    const char* wsrc = (const char*)w2 + w * (14 * 16384);
    const char* const wow = (const char*)wo + w * 65536;
    const unsigned w_rd0 = w * 2048 + (4 * h + q4) * 128 + ((q4 >> 1) << 6) + tr_in;
    const lptr wrA0 = lds + L_WB + w_rd0, wrA1 = lds + L_WB + (w_rd0 ^ 64), wrB0 = lds + w_rd0, wrB1 = lds + (w_rd0 ^ 64);
    const unsigned wbA = lds0 + L_WB + w * 2048, wbB = lds0 + w * 2048;
    const unsigned h_voff = (unsigned)((row64 >> 5) * 112 + 14 * w) * 1024u + lane * 16;
    u32x4 hA0, hA1, hB0, hB1;
    s16x8 wf0, wf1;
#define W_LD(src, buf) glds16x2(src, w64_src, w64_src1, buf)
#define H_LD(reg, step, tile) reg = bload_sc1(rs_h, h_voff, ((tile) * 112 + (step)) * 1024)
    unsigned long long hfv;
    ITER2(12, NOP, NOP);
    ITER2(14, W_LD(wsrc, wbA), W_LD(wsrc + 16384, wbB));
    ITER2(16, NOP, hfv = flag_load(hflag));
    ITER(sA, sB, 18, 0, NOP, NOP, NOP, NOP);
    STAMP(6);
    wait_flags(hflag, hfv, epoch);
    STAMP(7);
    ITER(sB, sA, 19, 3, NOP, H_LD(hA0, 0, 0); H_LD(hA1, 0, 1), NOP, NOP);
    ITER2(20, H_LD(hB0, 1, 0), H_LD(hB1, 1, 1));
    ADV(22);
    a00 = (f32x16){}; a01 = (f32x16){}; a10 = (f32x16){}; a11 = (f32x16){};
    STAMP(8);
#define GM(acc, wf, H) acc = MFMA(wf, asfrag(H), acc); SBAR()
#define GSTEP(SX, SY, T, R0, R1, H0, H1, LW, LH0, LH1) do { \
    ITER(SX, SY, T, 2, WAIT_V(5); wf0 = tr_frag(R0, 1024); wf1 = tr_frag(R1, 1024), NOP, NOP, GM(a00, wf0, H0)); \
    ITER(SY, SX, (T) + 1, 1, NOP, LW, GM(a01, wf1, H0); LH0, GM(a10, wf0, H1)); \
    ITER(SX, SY, (T) + 2, 0, NOP, NOP, NOP, GM(a11, wf1, H1); LH1); } while (0)
#pragma unroll 1
    for (int n = 0; n < 14; n += 2) {
        wsrc = n == 12 ? wow : wsrc + 32768;
        GSTEP(sA, sB, 0, wrA0, wrA1, hA0, hA1, W_LD(wsrc, wbA), H_LD(hA0, n + 2, 0), H_LD(hA1, n + 2, 1));
        GSTEP(sB, sA, 3, wrB0, wrB1, hB0, hB1, W_LD(wsrc + 16384, wbB), H_LD(hB0, n + 3, 0), H_LD(hB1, n + 3, 1));
        ADV(6);
    }
    STAMP(9);
    __builtin_amdgcn_s_setprio(0);
    oacc = pv_mma<1>(vf1, sB, oacc, spidx);
    {
        u32x4 v = pack8<0>(oacc, __builtin_amdgcn_rcpf(oacc[8]) * (rt ? gate1 : gate0));
#pragma unroll
        for (int e = 0; e < 4; ++e) v[e] = v[e] < 0xfffffffeu ? v[e] : 0xfffffffeu;
        bstore_sc1(v, rs_o, o_own);
        asm volatile("s_nop 1");
    }
    s16x8 wof[8];
    WAIT_V(2);
    wof[0] = tr_frag(wrA0, 1024); wof[1] = tr_frag(wrA1, 1024); wof[2] = tr_frag(wrB0, 1024); wof[3] = tr_frag(wrB1, 1024);
    asm volatile("s_waitcnt lgkmcnt(0)" : "+v"(wof[0]), "+v"(wof[1]), "+v"(wof[2]), "+v"(wof[3]) :: "memory");
    W_LD(wow + 32768, wbA); W_LD(wow + 49152, wbB);
    {
        const float og0 = rbf(1.f - gate0), og1 = rbf(1.f - gate1);
        a00 *= og0; a01 *= og0; a10 *= og1; a11 *= og1;
        PIN(a00); PIN(a01); PIN(a10); PIN(a11);
    }
    const unsigned ob = (unsigned)((row64 >> 5) * 32 + 4 * w) * 1024u;
    u32x4 of[8];
    auto o_load = [&]() {
#pragma unroll
        for (int i = 0; i < 8; ++i) of[i] = bload_sc1(rs_o, lane * 16 + (i & 3) * 1024, ob + (i >> 2) * 32768);
    };
    auto o_ready = [&]() {
        unsigned m = 0;
#pragma unroll
        for (int i = 0; i < 8; ++i) { m = max(m, max(of[i][0], of[i][1])); m = max(m, max(of[i][2], of[i][3])); }
        return __all(m != ~0u);
    };
    WAIT_V(0);
    wof[4] = tr_frag(wrA0, 1024); wof[5] = tr_frag(wrA1, 1024); wof[6] = tr_frag(wrB0, 1024); wof[7] = tr_frag(wrB1, 1024);
    BAR_L();
    STAMP(10);
    o_load();
    if (!o_ready()) {
        unsigned spins = 0;
        do { CFENCE(); o_load(); } while (!o_ready() && ++spins < SPIN_CAP);
    }
    STAMP(11);
    const lptr red_w = lds + w * 16384 + r * 256;
    auto put = [&](unsigned off, const f32x16& a) {
#pragma unroll
        for (int gq = 0; gq < 4; ++gq) *(LAS f32x4*)(red_w + off + (((2 * gq + h) ^ (r & 7)) << 4)) = (f32x4){a[4 * gq], a[4 * gq + 1], a[4 * gq + 2], a[4 * gq + 3]};
    };
#pragma unroll
    for (int i = 0; i < 4; ++i) a00 = MFMA(wof[2 * i], asfrag(of[i]), a00);
#pragma unroll
    for (int i = 0; i < 4; ++i) a01 = MFMA(wof[2 * i + 1], asfrag(of[i]), a01);
    SBAR(); put(0, a00); SBAR();
#pragma unroll
    for (int i = 0; i < 4; ++i) a10 = MFMA(wof[2 * i], asfrag(of[4 + i]), a10);
    SBAR(); put(128, a01); SBAR();
#pragma unroll
    for (int i = 0; i < 4; ++i) a11 = MFMA(wof[2 * i + 1], asfrag(of[4 + i]), a11);
    put(8192, a10); put(8192 + 128, a11);
    unsigned long long ef[4];
    gu64* const efp = (gu64*)(ws + WS_HFLAG) + lane * 16;
    if (b == 112 && w == 0) {
#pragma unroll
        for (int i = 0; i < 4; ++i) ef[i] = flag_load(efp + i * 1024);
    }
    BAR_L();
    {
        const lptr red_r = lds + (8 * w + (lane >> 3)) * 256;
        const unsigned rc0 = ((2 * (lane & 7)) ^ ((lane >> 3) & 7)) << 4, rc1 = rc0 ^ 16;
        f32x4 lo = *(const LAS f32x4*)(red_r + rc0), hi = *(const LAS f32x4*)(red_r + rc1);
#pragma unroll
        for (int k = 1; k < 8; ++k) { lo += *(const LAS f32x4*)(red_r + k * 16384 + rc0); hi += *(const LAS f32x4*)(red_r + k * 16384 + rc1); }
        const u32x4 v = {cvtpk(lo[0], lo[1]), cvtpk(lo[2], lo[3]), cvtpk(hi[0], hi[1]), cvtpk(hi[2], hi[3])};
        *(u32x4*)(out + (row64 + 8 * w + (lane >> 3)) * 512 + hg * 64 + (lane & 7) * 8) = v;
    }
    if (b == 112 && w == 0) {
#pragma unroll
        for (int i = 0; i < 4; ++i) wait_flags(efp + i * 1024, ef[i], epoch);
        if (lane == 0) __hip_atomic_store((gu32*)(ws + WS_EPOCH), epoch, RLX_AGENT);
    }
    STAMP(12);
}

extern "C" void kernel_launch(void* const* d_in, const int* in_sizes, int n_in, void* d_out, int out_size, void* d_ws, size_t ws_size,
                              hipStream_t stream) {
    fused_kernel<<<256, 512, 0, stream>>>((const uint16_t*)d_in[0], (const uint16_t*)d_in[1], (const uint16_t*)d_in[2], (const uint16_t*)d_in[3],
                                          (const uint16_t*)d_in[4], (const uint16_t*)d_in[6], (const uint16_t*)d_in[7], (const uint16_t*)d_in[8],
                                          (uint16_t*)d_out, (char*)d_ws);
}
```

```cpp
#include <hip/hip_runtime.h>
#include <stdint.h>

typedef short s16x8 __attribute__((ext_vector_type(8)));
typedef short s16x4 __attribute__((ext_vector_type(4)));
typedef short s16x2 __attribute__((ext_vector_type(2)));
typedef float f32x16 __attribute__((ext_vector_type(16)));
typedef float f32x4 __attribute__((ext_vector_type(4)));
typedef float f32x2 __attribute__((ext_vector_type(2)));
typedef unsigned u32x4 __attribute__((ext_vector_type(4)));
typedef __bf16 bf16x2 __attribute__((ext_vector_type(2)));
typedef __bf16 bf16x8 __attribute__((ext_vector_type(8)));
typedef __bf16 bf16x16 __attribute__((ext_vector_type(16)));
#define LAS __attribute__((address_space(3)))
#define GAS __attribute__((address_space(1)))
typedef GAS unsigned gu32;
typedef GAS unsigned long long gu64;
typedef LAS char* lptr;
#define DI __device__ __forceinline__
#define RLX_AGENT __ATOMIC_RELAXED, __HIP_MEMORY_SCOPE_AGENT
#define MFMA(a, b, c) __builtin_amdgcn_mfma_f32_32x32x16_bf16((a), (b), (c), 0, 0, 0)
#define SBAR() __builtin_amdgcn_sched_barrier(0)
#define PIN(x) asm volatile("" : "+v"(x))
#define CFENCE() do { asm volatile("" ::: "memory"); __builtin_amdgcn_fence(__ATOMIC_ACQUIRE, "wavefront"); } while (0)
#define BAR_L() asm volatile("s_waitcnt lgkmcnt(0)\n\ts_barrier" ::: "memory")
#define BAR_VL(N) asm volatile("s_waitcnt vmcnt(" #N ") lgkmcnt(0)\n\ts_barrier" ::: "memory")

#define ABL 0
#define STW 0
#define STAMPS 0

constexpr int D = 512, DFF = 1792;
constexpr float QSCALE = 0.25f * 1.4426950408889634f;
constexpr float SAFE_RANGE = 64.f;
constexpr unsigned WS_EPOCH = 0, WS_KVFLAG = 4096, WS_HFLAG = 8192;
constexpr unsigned WS_GZ = WS_HFLAG + 32768;
constexpr unsigned WS_KV = WS_GZ + 8192, KV_BYTES = 64 * 2048;
constexpr unsigned WS_H = WS_KV + KV_BYTES, H_BYTES = 64 * 112 * 1024;
constexpr unsigned WS_O = WS_H + H_BYTES, O_BYTES = 64 * 32 * 1024;
constexpr unsigned WS_STAMP = WS_O + O_BYTES;
constexpr unsigned OOB = 0x7ff00000u;
constexpr unsigned L_STAGE = 40960;
constexpr unsigned L_KSTAGE = 12288, L_KWQ = 98304;
constexpr unsigned KVT = 2080;
constexpr unsigned L_WB = 64 * KVT;
constexpr unsigned L_QX = L_WB + 16384, L_QN = L_QX + 8192, L_KM = L_QN + 1024, L_TOUCH = L_KM + 256, LDS_BYTES = 163840;
static_assert(L_TOUCH + 256 <= LDS_BYTES, "lds map");
constexpr unsigned SPIN_CAP = 1u << 16;

DI unsigned cvtpk(float a, float b) { f32x2 v = {a, b}; return __builtin_bit_cast(unsigned, __builtin_convertvector(v, bf16x2)); }
DI float rbf(float x) { return __uint_as_float(cvtpk(x, x) << 16); }
DI s16x8 asfrag(u32x4 v) { return __builtin_bit_cast(s16x8, v); }
DI s16x8 lds_frag(lptr p) { return *(const LAS s16x8*)p; }
DI void lds_put(lptr p, u32x4 v) { *(LAS u32x4*)p = v; }
DI s16x4 lds_tr(lptr p) { return __builtin_amdgcn_ds_read_tr16_b64_v4i16((LAS s16x4*)p); }
DI s16x8 tr_frag2(lptr p, lptr q) { s16x4 lo = lds_tr(p), hi = lds_tr(q); return __builtin_shufflevector(lo, hi, 0, 1, 2, 3, 4, 5, 6, 7); }
DI s16x8 tr_frag(lptr p, int second) { return tr_frag2(p, p + second); }
DI float swap_add(float v) { auto r = __builtin_amdgcn_permlane32_swap(__float_as_uint(v), __float_as_uint(v), false, false); return __uint_as_float(r[0]) + __uint_as_float(r[1]); }
DI float swap_max(float v) { auto r = __builtin_amdgcn_permlane32_swap(__float_as_uint(v), __float_as_uint(v), false, false); return fmaxf(__uint_as_float(r[0]), __uint_as_float(r[1])); }
DI float dot2(unsigned a, unsigned b, float c) { return __builtin_amdgcn_fdot2_f32_bf16(__builtin_bit_cast(bf16x2, a), __builtin_bit_cast(bf16x2, b), c, false); }
DI unsigned relu_pk(unsigned u) { return __builtin_bit_cast(unsigned, __builtin_elementwise_max(__builtin_bit_cast(s16x2, u), (s16x2){0, 0})); }
template <int CTRL> DI float dpp(float v) { return __uint_as_float(__builtin_amdgcn_update_dpp(0, __float_as_uint(v), CTRL, 0xf, 0xf, true)); }
DI float row32_max(float v) {
    v = fmaxf(v, dpp<0xB1>(v)); v = fmaxf(v, dpp<0x4E>(v)); v = fmaxf(v, dpp<0x141>(v)); v = fmaxf(v, dpp<0x140>(v));
    auto r = __builtin_amdgcn_permlane16_swap(__float_as_uint(v), __float_as_uint(v), false, false);
    return fmaxf(__uint_as_float(r[0]), __uint_as_float(r[1]));
}
template <int HALF> DI u32x4 pack8(const f32x16& a, float m) {
    u32x4 v;
#pragma unroll
    for (int e = 0; e < 4; ++e) v[e] = cvtpk(a[8 * HALF + 2 * e] * m, a[8 * HALF + 2 * e + 1] * m);
    return v;
}
template <int HALF> DI u32x4 pack8_relu(const f32x16& a) {
    u32x4 v;
#pragma unroll
    for (int e = 0; e < 4; ++e) v[e] = relu_pk(cvtpk(a[8 * HALF + 2 * e], a[8 * HALF + 2 * e + 1]));
    return v;
}
template <int HALF> DI f32x16 pv_mma(s16x8 v, const f32x16& p, f32x16 c, int idx) {
    const auto ph = __builtin_shufflevector(p, p, 8 * HALF, 8 * HALF + 1, 8 * HALF + 2, 8 * HALF + 3, 8 * HALF + 4, 8 * HALF + 5, 8 * HALF + 6, 8 * HALF + 7);
    return __builtin_amdgcn_smfmac_f32_32x32x32_bf16(__builtin_bit_cast(bf16x8, v), __builtin_bit_cast(bf16x16, ph), c, idx, 0, 0);
}
DI void glds16(const void* sbase, unsigned voff, unsigned lds_base) {
    asm volatile("s_mov_b32 m0, %2\n\ts_nop 0\n\tglobal_load_lds_dwordx4 %0, %1" :: "v"(voff), "s"(sbase), "s"(lds_base) : "memory");
}
DI void glds16x2(const void* sbase, unsigned voff0, unsigned voff1, unsigned lds_base) {
    asm volatile("s_mov_b32 m0, %3\n\ts_nop 0\n\tglobal_load_lds_dwordx4 %0, %2\n\tglobal_load_lds_dwordx4 %1, %2 offset:1024" :: "v"(voff0), "v"(voff1), "s"(sbase), "s"(lds_base) : "memory");
}
DI void glds16x4(const void* sbase, unsigned v0, unsigned v1, unsigned v2, unsigned v3, unsigned lds_base) {
    asm volatile("s_mov_b32 m0, %5\n\ts_nop 0\n\tglobal_load_lds_dwordx4 %0, %4\n\tglobal_load_lds_dwordx4 %1, %4 offset:1024\n\t"
                 "global_load_lds_dwordx4 %2, %4 offset:2048\n\tglobal_load_lds_dwordx4 %3, %4 offset:3072" :: "v"(v0), "v"(v1), "v"(v2), "v"(v3), "s"(sbase), "s"(lds_base) : "memory");
}
DI void glds16x2_sc1(const void* sbase, unsigned voff, unsigned lds_base) {
    asm volatile("s_mov_b32 m0, %2\n\ts_nop 0\n\tglobal_load_lds_dwordx4 %0, %1 sc1\n\tglobal_load_lds_dwordx4 %0, %1 offset:1024 sc1" :: "v"(voff), "s"(sbase), "s"(lds_base) : "memory");
}
DI void touch8k(const void* sbase, int lane, unsigned lds_scratch) {
    asm volatile("s_mov_b32 m0, %2\n\ts_nop 0\n\tglobal_load_lds_dword %0, %1" :: "v"(lane * 128), "s"(sbase), "s"(lds_scratch) : "memory");
}
#define WAIT_V(N) asm volatile("s_waitcnt vmcnt(" #N ")" ::: "memory")
DI __amdgpu_buffer_rsrc_t mk_rsrc(const void* p, unsigned bytes) { return __builtin_amdgcn_make_buffer_rsrc((void*)p, 0, bytes, 0x00020000); }
DI u32x4 bload(__amdgpu_buffer_rsrc_t r, unsigned voff, unsigned soff) { return __builtin_amdgcn_raw_buffer_load_b128(r, voff, soff, 0); }
DI u32x4 bload_sc1(__amdgpu_buffer_rsrc_t r, unsigned voff, unsigned soff) { return __builtin_amdgcn_raw_buffer_load_b128(r, voff, soff, 16); }
DI void bstore_sc1(u32x4 v, __amdgpu_buffer_rsrc_t r, unsigned voff) { __builtin_amdgcn_raw_buffer_store_b128(v, r, voff, 0, 16); }

#define STAMP(i) do { } while (0)

DI unsigned long long flag_load(gu64* f) { return __hip_atomic_load(f, RLX_AGENT); }
DI unsigned wait_flags(gu64* f, unsigned long long v, unsigned epoch) {
    if (__builtin_expect(!__all((unsigned)(v >> 32) == epoch), 0)) {
        unsigned spins = 0;
        do { __builtin_amdgcn_s_sleep(6); v = flag_load(f); } while (!__all((unsigned)(v >> 32) == epoch) && ++spins < SPIN_CAP);
    }
    CFENCE();
    return (unsigned)v;
}

struct BigFrag { s16x8 x0, x1, w0, w1; };
struct SmallFrag { s16x8 x, q; };

__global__ void __launch_bounds__(512, 2)
fused_kernel(const uint16_t* __restrict__ x, const uint16_t* __restrict__ wq, const uint16_t* __restrict__ wk, const uint16_t* __restrict__ wv,
             const uint16_t* __restrict__ wo, const uint16_t* __restrict__ w1, const uint16_t* __restrict__ w2, const uint16_t* __restrict__ gw,
             uint16_t* out, char* ws) {
    __shared__ __attribute__((aligned(16))) char smem[LDS_BYTES];
    const lptr lds = (lptr)smem;
    const unsigned lds0 = (unsigned)(uintptr_t)smem;
    const int tid = threadIdx.x, lane = tid & 63, w = __builtin_amdgcn_readfirstlane(tid >> 6);
    const int r = lane & 31, h = lane >> 5, g1 = (lane >> 4) & 1, q4 = (lane & 15) >> 2, p4 = lane & 3;
    const int b = blockIdx.x, bi = b >> 3;
    const int R = 2 * (b & 7) + (bi >> 4), cb = bi & 15, rh64 = cb & 1, hg = cb >> 1;
    const bool kvcu = cb >= 14, big = w < 4;
    const int row64 = R * 128 + rh64 * 64;
    const int rt = w & 1, hq = w >> 1, fh = hq & 1;
    unsigned stv = 0;
    STAMP(0);

    asm volatile("" :: "s"(x), "s"(wq), "s"(wk), "s"(wv), "s"(wo), "s"(w1), "s"(w2), "s"(gw), "s"(out), "s"(ws));
    const unsigned w64_src = (unsigned)((lane >> 3) * 1024 + hg * 128 + (((lane & 7) ^ (g1 << 2)) << 4));
    const unsigned w64_src1 = w64_src + 8192 - 1024;
    const unsigned xv = (unsigned)((lane >> 3) * 1024 + (((lane & 7) ^ ((lane >> 4) & 3)) << 4)), xv1 = (xv ^ 64) + 8192 - 1024;
    const unsigned wv0 = (unsigned)((lane >> 4) * (DFF * 2) + cb * 256 + (((lane & 15) ^ (((lane >> 4) & 3) << 2)) << 4)), wv1 = wv0 + 4 * DFF * 2 - 1024;
    const int rot = (b + cb) & 7;
    if (!kvcu) {
#pragma unroll
        for (int s = 0; s < 2; ++s) {
            const int sr = (s + rot) & 7;
            glds16x2((const char*)x + R * 131072 + sr * 128 + w * 16384, xv, xv1, lds0 + s * L_STAGE + w * 2048);
            glds16x2((const char*)w1 + (size_t)(sr * 64 + w * 8) * (DFF * 2), wv0, wv1, lds0 + s * L_STAGE + 16384 + w * 2048);
            glds16((const char*)wq + sr * 65536 + w * 8192, w64_src, lds0 + s * L_STAGE + 32768 + w * 1024);
        }
    } else {
        const char* wkv = (const char*)((w & 2) ? wk : wv);
        const unsigned kvv = (w & 1) * 1024 + ((lane ^ ((w & 2) << 2)) << 4);
#pragma unroll 1
        for (int s = 0; s < 8; ++s) {
            glds16((const char*)x + (row64 + 8 * w) * 1024 + s * 128, xv ^ (rt << 6), lds0 + s * L_KSTAGE + w * 1024);
            if (!big) glds16(wkv + s * 2048, kvv, lds0 + s * L_KSTAGE + 8192 + (w & 3) * 1024);
        }
    }
    const unsigned epoch_v = __hip_atomic_load((gu32*)(ws + WS_EPOCH), RLX_AGENT);
    asm volatile("" ::: "memory");
    const __amdgpu_buffer_rsrc_t rs_kv = mk_rsrc(ws + WS_KV, KV_BYTES), rs_h = mk_rsrc(ws + WS_H, H_BYTES), rs_o = mk_rsrc(ws + WS_O, O_BYTES);
    const unsigned o_own = (unsigned)(((row64 >> 5) + rt) * 32 + hg * 4 + hq) * 1024u + lane * 16;
    const u32x4 omark = {~0u, ~0u, ~0u, ~0u};
    const __amdgpu_buffer_rsrc_t rs_kf = mk_rsrc(ws + WS_KVFLAG, 4096);
    gu64* const hflag = (gu64*)(ws + WS_HFLAG) + (R * 16 + (lane & 15)) * 16;
    const unsigned tr_in = 32 * g1 + 8 * p4;
    const unsigned wq_rd = (8 * h + q4) * 128 + ((fh ^ (q4 >> 1)) << 6) + tr_in;
    unsigned xo[4];
#pragma unroll
    for (int ks = 0; ks < 4; ++ks) xo[ks] = r * 128 + (((2 * ks + h) ^ ((r >> 1) & 7)) << 4);

    f32x16 a00 = {}, a01 = {}, a10 = {}, a11 = {};
    auto kv_wait = [&](u32x4 fv) {
        const unsigned ep = __builtin_amdgcn_readfirstlane(epoch_v) + 1u;
        if (__builtin_expect(!__all(fv[3] == ep), 0)) {
            unsigned spins = 0;
            do { __builtin_amdgcn_s_sleep(6); CFENCE(); fv = bload_sc1(rs_kf, r * 128, 0); } while (!__all(fv[3] == ep) && ++spins < SPIN_CAP);
        }
        CFENCE();
        const float km = row32_max(fmaxf(__uint_as_float(fv[0]), __uint_as_float(fv[2])));
        if (tid == 256) *(LAS float*)(lds + L_KM) = __builtin_amdgcn_sqrtf(km) * 1.0001f;
    };
    auto kv_tile = [&](int t) { glds16x2_sc1(ws + WS_KV + ((t + 8 * bi) & 63) * 2048, lane * 16, lds0 + t * KVT); };
    if (!kvcu) {
        unsigned cur = 0, nxt = L_STAGE;
        if (big) {
            const unsigned xb = rt * 8192, w0o = 16384 + (8 * h + q4) * 256 + (((2 * fh) ^ q4) << 6) + tr_in, w1o = w0o ^ 64;
            BigFrag F[4];
            auto rd = [&](BigFrag& n, lptr sb, int ks) {
                n.x0 = lds_frag(sb + xb + xo[ks]); n.x1 = lds_frag(sb + xb + 4096 + xo[ks]); n.w0 = tr_frag(sb + w0o + ks * 4096, 1024); n.w1 = tr_frag(sb + w1o + ks * 4096, 1024); };
#define AM(e, ...) do { if (ABL & 2048) asm volatile("" :: __VA_ARGS__); else e; SBAR(); } while (0)
#define AR(e, ...) do { if (ABL & 4096) asm volatile("" : __VA_ARGS__); else { e; } SBAR(); } while (0)
            auto step = [&](const BigFrag& c, BigFrag& n, lptr sb, int ks) {
                AM(a00 = MFMA(c.w0, c.x0, a00), "v"(c.w0), "v"(c.x0)); AR(n.x0 = lds_frag(sb + xb + xo[ks]); n.x1 = lds_frag(sb + xb + 4096 + xo[ks]), "+v"(n.x0), "+v"(n.x1));
                AM(a10 = MFMA(c.w0, c.x1, a10), "v"(c.x1)); AR(n.w0 = tr_frag(sb + w0o + ks * 4096, 1024), "+v"(n.w0));
                AM(a01 = MFMA(c.w1, c.x0, a01), "v"(c.w1)); AR(n.w1 = tr_frag(sb + w1o + ks * 4096, 1024), "+v"(n.w1));
                AM(a11 = MFMA(c.w1, c.x1, a11), "v"(c.w1)); };
            auto stage = [&](auto bar) {
                step(F[0], F[3], lds + cur, 3);
                bar();
                step(F[1], F[0], lds + nxt, 0); step(F[2], F[1], lds + nxt, 1); step(F[3], F[2], lds + nxt, 2);
                cur = nxt; nxt = (nxt == 2 * L_STAGE) ? 0 : nxt + L_STAGE; };
            STAMP(13);
            BAR_VL(6);
            STAMP(14);
            rd(F[0], lds, 0); rd(F[1], lds, 1); rd(F[2], lds, 2);
#pragma unroll 1
            for (int s = 0; s < 7; ++s) stage([&] {
                BAR_VL(1);
                if (s == 3 && w < 2) touch8k(w == 0 ? (const char*)w2 + (R * 14 + cb) * 8192 : (const char*)wo + (b & 63) * 8192, lane, lds0 + L_TOUCH); });
            STAMP(1);
            rd(F[3], lds + cur, 3);
#pragma unroll
            for (int ks = 0; ks < 4; ++ks) { const BigFrag& c = F[ks]; a00 = MFMA(c.w0, c.x0, a00); a10 = MFMA(c.w0, c.x1, a10); a01 = MFMA(c.w1, c.x0, a01); a11 = MFMA(c.w1, c.x1, a11); }
        } else {
            const int j = w - 4;
            __builtin_amdgcn_s_setprio(2);
            const unsigned xb = (2 * rh64 + rt) * 4096, qo = 32768 + wq_rd;
            SmallFrag F[4];
            auto rd = [&](SmallFrag& n, lptr sb, int ks) { n.x = lds_frag(sb + xb + xo[ks]); n.q = tr_frag(sb + qo + ks * 2048, 512); SBAR(); };
            auto mma = [&](const SmallFrag& c) { a00 = MFMA(c.q, c.x, a00); SBAR(); };
            const unsigned xv2 = xv + 2 * (8192 - 1024), xv3 = xv1 + 2 * (8192 - 1024), wv2 = wv0 + 2 * (4 * DFF * 2 - 1024), wv3 = wv1 + 2 * (4 * DFF * 2 - 1024);
            const char* xs = (const char*)x + R * 131072 + j * 32768;
            const char* w1s = (const char*)w1 + (size_t)(j * 16) * (DFF * 2);
            const char* wqs = (const char*)wq + j * 16384;
            auto fill0 = [&](int sr, unsigned slot) { glds16x4(xs + sr * 128, xv, xv1, xv2, xv3, lds0 + slot + j * 4096); };
            auto fill1 = [&](int sr, unsigned slot) { glds16x4(w1s + sr * (64 * DFF * 2), wv0, wv1, wv2, wv3, lds0 + slot + 16384 + j * 4096); };
            auto fill2 = [&](int sr, unsigned slot) { glds16x2(wqs + sr * 65536, w64_src, w64_src1, lds0 + slot + 32768 + j * 2048); };
            BAR_VL(6);
            rd(F[0], lds, 0); rd(F[1], lds, 1); rd(F[2], lds, 2);
            fill0((rot + 2) & 7, 2 * L_STAGE); fill1((rot + 2) & 7, 2 * L_STAGE); fill2((rot + 2) & 7, 2 * L_STAGE);
#pragma unroll 1
            for (int s = 0; s < 6; ++s) {
                const bool more = s < 5;
                const int sr = (rot + s + 3) & 7;
                rd(F[3], lds + cur, 3); mma(F[0]);
                BAR_VL(10);
                if (more) fill0(sr, cur);
                rd(F[0], lds + nxt, 0); mma(F[1]);
                if (more) fill1(sr, cur);
                rd(F[1], lds + nxt, 1); mma(F[2]);
                if (more) fill2(sr, cur);
                rd(F[2], lds + nxt, 2); mma(F[3]);
                cur = nxt; nxt = (nxt == 2 * L_STAGE) ? 0 : nxt + L_STAGE;
            }
            const u32x4 fv = bload_sc1(rs_kf, r * 128, 0);
            rd(F[3], lds, 3); mma(F[0]);
            BAR_VL(0);
            rd(F[0], lds + L_STAGE, 0); mma(F[1]); rd(F[1], lds + L_STAGE, 1); mma(F[2]); rd(F[2], lds + L_STAGE, 2); mma(F[3]); rd(F[3], lds + L_STAGE, 3);
            STAMP(15);
            kv_wait(fv);
            STAMP(16);
            kv_tile(2 * j); kv_tile(2 * j + 1);
            mma(F[0]); mma(F[1]); mma(F[2]); mma(F[3]);
        }
    } else {
        const unsigned xb = rt * 4096, w64_src2 = w64_src + 2 * (8192 - 1024), w64_src3 = w64_src + 3 * (8192 - 1024);
        {
            const unsigned wkv_rd = 8192 + g1 * 2048 + (((8 * h + q4) * 32 + 8 * p4) ^ (g1 << 7));
#pragma unroll
            for (int g = 0; g < 2; ++g) {
                if (g == 0) { if (big) BAR_VL(5); else BAR_VL(9); } else BAR_VL(1);
                const lptr st = lds + (g * 4 + hq) * L_KSTAGE;
#pragma unroll
                for (int k = 0; k < 4; ++k) a00 = MFMA(tr_frag2(st + wkv_rd + k * 512, st + (wkv_rd ^ 128) + k * 512), lds_frag(st + xb + xo[k]), a00);
            }
        }
        BAR_L();
        {
            const lptr pw = lds + w * L_KSTAGE + 8192 + r * 128;
            const unsigned sz = (r & 7) ^ ((r >> 1) & 4);
#pragma unroll
            for (int i = 0; i < 4; ++i) *(LAS f32x4*)(pw + (((2 * i + h) ^ sz) << 4)) = (f32x4){a00[4 * i], a00[4 * i + 1], a00[4 * i + 2], a00[4 * i + 3]};
            a00 = (f32x16){};
        }
        BAR_L();
        if (big) {
            STAMP(1);
            const unsigned kvo = (unsigned)((row64 >> 5) + rt) * 2048u + lane * 16;
            const lptr pr = lds + rt * L_KSTAGE + 8192;
            gu32* const fl = (gu32*)(ws + WS_KVFLAG) + (row64 >> 6) * 32;
            float n2 = 0.f;
            if (fh == 0) {
                const unsigned sz = (r & 7) ^ ((r >> 1) & 4);
                f32x4 lo = {}, hi = {};
#pragma unroll
                for (int i = 0; i < 4; ++i) { lo += *(const LAS f32x4*)(pr + i * 2 * L_KSTAGE + r * 128 + (((4 + h) ^ sz) << 4)); hi += *(const LAS f32x4*)(pr + i * 2 * L_KSTAGE + r * 128 + (((6 + h) ^ sz) << 4)); }
                const u32x4 v = {cvtpk(lo[0], lo[1]), cvtpk(lo[2], lo[3]), cvtpk(hi[0], hi[1]), cvtpk(hi[2], hi[3])};
                bstore_sc1(v, rs_kv, kvo);
                n2 = row32_max(swap_add(dot2(v[0], v[0], dot2(v[1], v[1], 0.f)) + dot2(v[2], v[2], dot2(v[3], v[3], 0.f))));
                if (tid == 64) __hip_atomic_store(fl, __float_as_uint(n2), RLX_AGENT);
            } else {
                float t[8];
#pragma unroll
                for (int j = 0; j < 8; ++j) {
                    const lptr q = pr + ((lane >> 4) * 8 + j) * 128 + ((q4 ^ j ^ (g1 << 2)) << 4) + p4 * 4;
                    t[j] = (*(const LAS float*)q + *(const LAS float*)(q + 2 * L_KSTAGE)) + (*(const LAS float*)(q + 4 * L_KSTAGE) + *(const LAS float*)(q + 6 * L_KSTAGE));
                }
                bstore_sc1((u32x4){cvtpk(t[0], t[1]), cvtpk(t[2], t[3]), cvtpk(t[4], t[5]), cvtpk(t[6], t[7])}, rs_kv, kvo + 1024);
            }
            STAMP(13);
            BAR_VL(0);
            STAMP(14);
            if (tid == 0) __hip_atomic_store((gu64*)(fl + 2), ((unsigned long long)(epoch_v + 1u) << 32) | __float_as_uint(n2), RLX_AGENT);
            u32x4 gf[16];
#pragma unroll
            for (int i = 0; i < 16; ++i) gf[i] = *(const u32x4*)(gw + i * 32 + (lane >> 4) * 8);
            BAR_L();
            f32x4 z = {};
            const int zr = 16 * w + (lane & 15);
#pragma unroll
            for (int i = 0; i < 16; ++i)
                z = __builtin_amdgcn_mfma_f32_16x16x32_bf16(asfrag(gf[i]), lds_frag(lds + (i >> 1) * L_KSTAGE + zr * 128 + (((4 * (i & 1) + (lane >> 4)) ^ ((zr >> 1) & 7)) << 4)), z, 0, 0, 0);
            if (lane < 16) __hip_atomic_store((gu32*)(ws + WS_GZ) + row64 + zr, __float_as_uint(z[0]), RLX_AGENT);
            BAR_L();
        } else {
#pragma unroll
            for (int i = 0; i < 4; ++i) { const int p = i * 16 + (w - 4) * 4; glds16x4((const char*)wq + p * 8192, w64_src, w64_src1, w64_src2, w64_src3, lds0 + L_KWQ + p * 1024); }
            BAR_L();
            BAR_VL(0);
            SmallFrag F[4];
            auto rd = [&](SmallFrag& n, int s, int k) { n.x = lds_frag(lds + s * L_KSTAGE + xb + xo[k]); n.q = tr_frag(lds + L_KWQ + s * 8192 + wq_rd + k * 2048, 512); SBAR(); };
#pragma unroll
            for (int k = 0; k < 4; ++k) rd(F[k], 0, k);
#pragma unroll 1
            for (int s = 1; s < 8; ++s) {
#pragma unroll
                for (int k = 0; k < 4; ++k) { a00 = MFMA(F[k].q, F[k].x, a00); SBAR(); rd(F[k], s, k); }
            }
            const u32x4 fv = bload_sc1(rs_kf, r * 128, 0);
#pragma unroll
            for (int k = 0; k < 4; ++k) a00 = MFMA(F[k].q, F[k].x, a00);
            BAR_L();
            STAMP(15);
            kv_wait(fv);
            STAMP(16);
            kv_tile(2 * (w - 4)); kv_tile(2 * (w - 4) + 1);
        }
    }
    STAMP(2);
    u32x4 hs[8];
    const unsigned hb = (kvcu ? OOB : (unsigned)((R * 4 + 2 * rt) * 112 + cb * 8 + fh * 4) * 1024u) + lane * 16;
    const u32x4 ones = {0x3f803f80u, 0x3f803f80u, 0x3f803f80u, 0x3f803f80u};
    if (big) {
        hs[0] = pack8_relu<0>(a00); hs[1] = pack8_relu<1>(a00); hs[2] = pack8_relu<0>(a01); hs[3] = pack8_relu<1>(a01);
        hs[4] = pack8_relu<0>(a10); hs[5] = pack8_relu<1>(a10); hs[6] = pack8_relu<0>(a11); hs[7] = pack8_relu<1>(a11);
    } else {
#pragma unroll
        for (int s = 0; s < 2; ++s) {
            const u32x4 v = s ? pack8<1>(a00, QSCALE) : pack8<0>(a00, QSCALE);
            lds_put(lds + L_QX + (4 * fh + 2 * s + rt) * 1024 + lane * 16, v);
            const float n2 = swap_add(dot2(v[0], v[0], dot2(v[1], v[1], 0.f)) + dot2(v[2], v[2], dot2(v[3], v[3], 0.f)));
            *(LAS float*)(lds + L_QN + (4 * fh + 2 * s + rt) * 128 + r * 4) = __builtin_amdgcn_sqrtf(n2);
        }
        if (w == 4 && lane < 16) lds_put(lds + (lane >> 1) * KVT + 2048 + (lane & 1) * 16, (lane & 1) ? (u32x4){} : ones);
#pragma unroll
        for (int i = 0; i < 8; ++i) asm volatile("" : "=v"(hs[i]));
    }
    BAR_VL(0);
    STAMP(3);
    __builtin_amdgcn_s_setprio(3);
    unsigned epoch_w = epoch_v; PIN(epoch_w);
    const unsigned epoch = __builtin_amdgcn_readfirstlane(epoch_w) + 1u;
    lptr kp = lds + lane * 16;
    const unsigned v_cst = (r & 27) != 16 ? 2064 : 2048;
    lptr vp0 = lds + (r < 16 ? 1024 + h * 256 + r * 16 : v_cst), vp1 = lds + (r < 16 ? 1536 + h * 256 + r * 16 : v_cst);
    const s16x8 qf = lds_frag(lds + L_QX + (hq * 2 + rt) * 1024 + lane * 16);
    s16x8 kf = lds_frag(kp), vf0 = lds_frag(vp0), vf1 = {};
    float mshift = *(const LAS float*)(lds + L_QN + (hq * 2 + rt) * 128 + r * 4) * *(const LAS float*)(lds + L_KM);
    {
        if (__builtin_expect(!__all(2.f * mshift <= SAFE_RANGE), 0)) {
            float m = -__builtin_inff();
#pragma unroll 1
            for (int t = 0; t < 64; ++t) {
                const f32x16 s = MFMA(asfrag(bload_sc1(rs_kv, lane * 16, t * 2048)), qf, (f32x16){});
#pragma unroll
                for (int i = 0; i < 16; ++i) m = fmaxf(m, s[i]);
            }
            mshift = swap_max(m);
        }
    }
    f32x16 cinit;
#pragma unroll
    for (int i = 0; i < 16; ++i) cinit[i] = -mshift;
    PIN(cinit);
    f32x16 sA = MFMA(kf, qf, cinit), sB = {}, oacc = {};
    kf = lds_frag(kp + KVT);
    kv_tile(8 + w); kv_tile(16 + w);
    if (tid >= 16 && tid < 128) lds_put(lds + (tid >> 1) * KVT + 2048 + (tid & 1) * 16, (tid & 1) ? (u32x4){} : ones);
    int spidx = 0xDDDD; PIN(spidx);

#define S1(S_, i) S_[i] = __builtin_amdgcn_exp2f(S_[i])
#define EX2(S_, i) do { S1(S_, i); S1(S_, (i) + 1); } while (0)
#define ITER(SC, SN, T, PR, PRE, MEM, G0, G1) do { \
    __builtin_amdgcn_s_setprio(PR); \
    PRE; EX2(SC, 0); PIN(SC); SBAR(); \
    oacc = pv_mma<1>(vf1, SN, oacc, spidx); if (!(ABL & 2) || ((T) & 1)) vf1 = lds_frag(vp1 + (T) * KVT); SBAR(); \
    EX2(SC, 2); S1(SC, 4); PIN(SC); SBAR(); \
    SN = MFMA(kf, qf, (ABL & 1) ? (f32x16){} : cinit); if (!(ABL & 2) || ((T) & 1)) kf = lds_frag(kp + ((T) + 2) * KVT); SBAR(); \
    S1(SC, 5); EX2(SC, 6); PIN(SC); SBAR(); \
    G0; SBAR(); \
    EX2(SC, 8); S1(SC, 10); PIN(SC); SBAR(); \
    oacc = pv_mma<0>(vf0, SC, oacc, spidx); if (!(ABL & 2) || ((T) & 1)) vf0 = lds_frag(vp0 + ((T) + 1) * KVT); SBAR(); \
    S1(SC, 11); MEM; EX2(SC, 12); PIN(SC); SBAR(); \
    G1; SBAR(); \
    EX2(SC, 14); PIN(SC); SBAR(); } while (0)
#define NOP (void)0
#define ITER2(T, M0, M1) do { ITER(sA, sB, T, (2 - (T)) & 3, NOP, M0, NOP, NOP); ITER(sB, sA, (T) + 1, (1 - (T)) & 3, NOP, M1, NOP, NOP); } while (0)
#define ADV(n) do { kp += (n) * KVT; vp0 += (n) * KVT; vp1 += (n) * KVT; PIN(kp); PIN(vp0); PIN(vp1); } while (0)
#define H_ST(i) bstore_sc1(hs[i], rs_h, hb + (((i) >> 2) * 112 + ((i) & 3)) * 1024)
    STAMP(4);
#define H_ST2(i) do { if (big) { H_ST(2 * (i)); H_ST(2 * (i) + 1); } } while (0)
    ITER2(0, H_ST2(0), H_ST2(1));
    ITER2(2, H_ST2(2), H_ST2(3));
    ITER2(4, bstore_sc1(omark, rs_o, o_own), kv_tile(24 + w));
    STAMP(17);
    if (big) BAR_VL(11); else BAR_VL(3);
    STAMP(18);
    ITER2(6, kv_tile(32 + w), NOP);
    ITER2(8, NOP, NOP);
    ITER2(10, NOP, NOP);
    STAMP(19);
    BAR_VL(0);
    if (tid == 0) __hip_atomic_store((gu64*)(ws + WS_HFLAG) + (R * 16 + cb) * 16, (unsigned long long)epoch << 32, RLX_AGENT);
    STAMP(5);
    const char* const w2w = (const char*)w2 + w * (14 * 16384);
    const char* const wow = (const char*)wo + w * 65536;
    int pn = (row64 >> 6) % 7;
    const unsigned w_rd0 = w * 2048 + (4 * h + q4) * 128 + ((q4 >> 1) << 6) + tr_in;
    const lptr wrA0 = lds + L_WB + w_rd0, wrA1 = lds + L_WB + (w_rd0 ^ 64), wrB0 = lds + w_rd0, wrB1 = lds + (w_rd0 ^ 64);
    const unsigned wbA = lds0 + L_WB + w * 2048, wbB = lds0 + w * 2048;
    const unsigned h_voff = (unsigned)((row64 >> 5) * 112 + 14 * w) * 1024u + lane * 16;
    u32x4 hA0, hA1, hB0, hB1;
    s16x8 wf0, wf1;
#define W_LD(src, buf) glds16x2(src, w64_src, w64_src1, buf)
    unsigned long long hfv;
    ITER2(12, kv_tile(40 + w); W_LD(w2w + pn * 32768, wbA), kv_tile(48 + w); W_LD(w2w + pn * 32768 + 16384, wbB));
    ITER2(14, kv_tile(56 + w), NOP);
    ITER2(16, NOP, hfv = flag_load(hflag));
    ITER(sA, sB, 18, 0, NOP, NOP, NOP, NOP);
    STAMP(6);
    wait_flags(hflag, hfv, epoch);
    STAMP(7);
    float gz0, gz1;
    ITER(sB, sA, 19, 3, NOP, gz0 = __uint_as_float(__hip_atomic_load((gu32*)(ws + WS_GZ) + row64 + r, RLX_AGENT)); gz1 = __uint_as_float(__hip_atomic_load((gu32*)(ws + WS_GZ) + row64 + 32 + r, RLX_AGENT));
         hA0 = bload_sc1(rs_h, h_voff, pn * 2048); hA1 = bload_sc1(rs_h, h_voff, pn * 2048 + 112 * 1024), NOP, NOP);
    ITER2(20, hB0 = bload_sc1(rs_h, h_voff, pn * 2048 + 1024), hB1 = bload_sc1(rs_h, h_voff, pn * 2048 + 113 * 1024));
    ADV(22);
    BAR_L();
    a00 = (f32x16){}; a01 = (f32x16){}; a10 = (f32x16){}; a11 = (f32x16){};
    STAMP(8);
#define GM(acc, wf, H) acc = MFMA(wf, asfrag(H), acc); SBAR()
#define GSTEP(SX, SY, T, R0, R1, H0, H1, LW, LH0, LH1) do { \
    ITER(SX, SY, T, 2, WAIT_V(5); wf0 = tr_frag(R0, 1024); wf1 = tr_frag(R1, 1024), NOP, NOP, GM(a00, wf0, H0)); \
    ITER(SY, SX, (T) + 1, 1, NOP, LW, GM(a01, wf1, H0); LH0, GM(a10, wf0, H1)); \
    ITER(SX, SY, (T) + 2, 0, NOP, NOP, NOP, GM(a11, wf1, H1); LH1); } while (0)
    const unsigned wo_voff = (unsigned)((lane >> 3) * 1024 + hg * 128 + (lane & 7) * 16);
#pragma unroll 1
    for (int n = 0; n < 7; ++n) {
        const bool last = n == 6;
        pn = pn == 6 ? 0 : pn + 1;
        const char* wsrc = last ? wow : w2w + pn * 32768;
        const __amdgpu_buffer_rsrc_t rs = mk_rsrc(last ? wow + 32768 : ws + WS_H + pn * 2048, last ? 32768 : H_BYTES);
        const unsigned vo = last ? wo_voff : h_voff;
        GSTEP(sA, sB, 0, wrA0, wrA1, hA0, hA1, W_LD(wsrc, wbA), hA0 = bload_sc1(rs, vo, 0), hA1 = bload_sc1(rs, vo, last ? 8192 : 112 * 1024));
        GSTEP(sB, sA, 3, wrB0, wrB1, hB0, hB1, W_LD(wsrc + 16384, wbB), hB0 = bload_sc1(rs, vo, last ? 16384 : 1024), hB1 = bload_sc1(rs, vo, last ? 24576 : 113 * 1024));
        ADV(6);
    }
    STAMP(9);
    __builtin_amdgcn_s_setprio(0);
    oacc = pv_mma<1>(vf1, sB, oacc, spidx);
    const float gate0 = rbf(__builtin_amdgcn_rcpf(1.f + __expf(-rbf(gz0)))), gate1 = rbf(__builtin_amdgcn_rcpf(1.f + __expf(-rbf(gz1))));
    {
        u32x4 v = pack8<0>(oacc, __builtin_amdgcn_rcpf(oacc[8]) * (rt ? gate1 : gate0));
#pragma unroll
        for (int e = 0; e < 4; ++e) v[e] = v[e] < 0xfffffffeu ? v[e] : 0xfffffffeu;
        bstore_sc1(v, rs_o, o_own);
        asm volatile("s_nop 1");
    }
    s16x8 wof[8];
    WAIT_V(2);
    wof[0] = tr_frag(wrA0, 1024); wof[1] = tr_frag(wrA1, 1024); wof[2] = tr_frag(wrB0, 1024); wof[3] = tr_frag(wrB1, 1024);
    {
        const unsigned wr = w * 2048 + (lane >> 3) * 128 + (((lane & 7) ^ (g1 << 2)) << 4);
        lds_put(lds + L_WB + wr, hA0); lds_put(lds + L_WB + wr + 1024, hA1); lds_put(lds + wr, hB0); lds_put(lds + wr + 1024, hB1);
    }
    wof[4] = tr_frag(wrA0, 1024); wof[5] = tr_frag(wrA1, 1024); wof[6] = tr_frag(wrB0, 1024); wof[7] = tr_frag(wrB1, 1024);
    {
        const float og0 = rbf(1.f - gate0), og1 = rbf(1.f - gate1);
        a00 *= og0; a01 *= og0; a10 *= og1; a11 *= og1;
        PIN(a00); PIN(a01); PIN(a10); PIN(a11);
    }
    const unsigned ob = (unsigned)((row64 >> 5) * 32 + 4 * w) * 1024u;
    u32x4 of[8];
    auto o_load = [&]() {
#pragma unroll
        for (int i = 0; i < 8; ++i) of[i] = bload_sc1(rs_o, lane * 16 + (i & 3) * 1024, ob + (i >> 2) * 32768);
    };
    auto o_ready = [&]() {
        unsigned m = 0;
#pragma unroll
        for (int i = 0; i < 8; ++i) { m = max(m, max(of[i][0], of[i][1])); m = max(m, max(of[i][2], of[i][3])); }
        return __all(m != ~0u);
    };
    BAR_L();
    STAMP(10);
    o_load();
    if (!o_ready()) {
        unsigned spins = 0;
        do { CFENCE(); o_load(); } while (!o_ready() && ++spins < SPIN_CAP);
    }
    STAMP(11);
    const lptr red_w = lds + w * 16384 + r * 256;
    auto put = [&](unsigned off, const f32x16& a) {
#pragma unroll
        for (int gq = 0; gq < 4; ++gq) *(LAS f32x4*)(red_w + off + (((2 * gq + h) ^ (r & 7)) << 4)) = (f32x4){a[4 * gq], a[4 * gq + 1], a[4 * gq + 2], a[4 * gq + 3]};
    };
#pragma unroll
    for (int i = 0; i < 4; ++i) a00 = MFMA(wof[2 * i], asfrag(of[i]), a00);
#pragma unroll
    for (int i = 0; i < 4; ++i) a01 = MFMA(wof[2 * i + 1], asfrag(of[i]), a01);
    SBAR(); put(0, a00); SBAR();
#pragma unroll
    for (int i = 0; i < 4; ++i) a10 = MFMA(wof[2 * i], asfrag(of[4 + i]), a10);
    SBAR(); put(128, a01); SBAR();
#pragma unroll
    for (int i = 0; i < 4; ++i) a11 = MFMA(wof[2 * i + 1], asfrag(of[4 + i]), a11);
    put(8192, a10); put(8192 + 128, a11);
    unsigned long long ef[4];
    gu64* const efp = (gu64*)(ws + WS_HFLAG) + lane * 16;
    if (b == 112 && w == 0) {
#pragma unroll
        for (int i = 0; i < 4; ++i) ef[i] = flag_load(efp + i * 1024);
    }
    BAR_L();
    {
        const lptr red_r = lds + (8 * w + (lane >> 3)) * 256;
        const unsigned rc0 = ((2 * (lane & 7)) ^ ((lane >> 3) & 7)) << 4, rc1 = rc0 ^ 16;
        f32x4 lo = *(const LAS f32x4*)(red_r + rc0), hi = *(const LAS f32x4*)(red_r + rc1);
#pragma unroll
        for (int k = 1; k < 8; ++k) { lo += *(const LAS f32x4*)(red_r + k * 16384 + rc0); hi += *(const LAS f32x4*)(red_r + k * 16384 + rc1); }
        const u32x4 v = {cvtpk(lo[0], lo[1]), cvtpk(lo[2], lo[3]), cvtpk(hi[0], hi[1]), cvtpk(hi[2], hi[3])};
        *(u32x4*)(out + (row64 + 8 * w + (lane >> 3)) * 512 + hg * 64 + (lane & 7) * 8) = v;
    }
    if (b == 112 && w == 0) {
#pragma unroll
        for (int i = 0; i < 4; ++i) wait_flags(efp + i * 1024, ef[i], epoch);
        if (lane == 0) __hip_atomic_store((gu32*)(ws + WS_EPOCH), epoch, RLX_AGENT);
    }
    STAMP(12);
}

extern "C" void kernel_launch(void* const* d_in, const int* in_sizes, int n_in, void* d_out, int out_size, void* d_ws, size_t ws_size,
                              hipStream_t stream) {
    fused_kernel<<<256, 512, 0, stream>>>((const uint16_t*)d_in[0], (const uint16_t*)d_in[1], (const uint16_t*)d_in[2], (const uint16_t*)d_in[3],
                                          (const uint16_t*)d_in[4], (const uint16_t*)d_in[6], (const uint16_t*)d_in[7], (const uint16_t*)d_in[8],
                                          (uint16_t*)d_out, (char*)d_ws);
}
```

```cpp
#include <hip/hip_runtime.h>
#include <stdint.h>

typedef short s16x8 __attribute__((ext_vector_type(8)));
typedef short s16x4 __attribute__((ext_vector_type(4)));
typedef short s16x2 __attribute__((ext_vector_type(2)));
typedef float f32x16 __attribute__((ext_vector_type(16)));
typedef float f32x4 __attribute__((ext_vector_type(4)));
typedef float f32x2 __attribute__((ext_vector_type(2)));
typedef unsigned u32x4 __attribute__((ext_vector_type(4)));
typedef __bf16 bf16x2 __attribute__((ext_vector_type(2)));
typedef __bf16 bf16x8 __attribute__((ext_vector_type(8)));
typedef __bf16 bf16x16 __attribute__((ext_vector_type(16)));
#define LAS __attribute__((address_space(3)))
#define GAS __attribute__((address_space(1)))
typedef GAS unsigned gu32;
typedef GAS unsigned long long gu64;
typedef LAS char* lptr;
#define DI __device__ __forceinline__
#define RLX_AGENT __ATOMIC_RELAXED, __HIP_MEMORY_SCOPE_AGENT
#define MFMA(a, b, c) __builtin_amdgcn_mfma_f32_32x32x16_bf16((a), (b), (c), 0, 0, 0)
#define SBAR() __builtin_amdgcn_sched_barrier(0)
#define PIN(x) asm volatile("" : "+v"(x))
#define CFENCE() do { asm volatile("" ::: "memory"); __builtin_amdgcn_fence(__ATOMIC_ACQUIRE, "wavefront"); } while (0)
#define BAR_L() asm volatile("s_waitcnt lgkmcnt(0)\n\ts_barrier" ::: "memory")
#define BAR_VL(N) asm volatile("s_waitcnt vmcnt(" #N ") lgkmcnt(0)\n\ts_barrier" ::: "memory")

#define ABL 0
#define STW 0
#define STAMPS 0

constexpr int D = 512, DFF = 1792;
constexpr float QSCALE = 0.25f * 1.4426950408889634f;
constexpr float SAFE_RANGE = 64.f;
constexpr unsigned WS_EPOCH = 0, WS_KVFLAG = 4096, WS_HFLAG = 8192;
constexpr unsigned WS_GZ = WS_HFLAG + 32768;
constexpr unsigned WS_KV = WS_GZ + 16384, KV_BYTES = 64 * 2048;
constexpr unsigned WS_H = WS_KV + KV_BYTES, H_BYTES = 64 * 112 * 1024;
constexpr unsigned WS_O = WS_H + H_BYTES, O_BYTES = 64 * 32 * 1024;
constexpr unsigned WS_STAMP = WS_O + O_BYTES;
constexpr unsigned OOB = 0x7ff00000u;
constexpr unsigned L_STAGE = 40960;
constexpr unsigned L_KSTAGE = 12288, L_KWQ = 98304;
constexpr unsigned KVT = 2080;
constexpr unsigned L_WB = 64 * KVT;
constexpr unsigned L_QX = L_WB + 16384, L_QN = L_QX + 8192, L_KM = L_QN + 1024, L_TOUCH = L_KM + 256, LDS_BYTES = 163840;
static_assert(L_TOUCH + 256 <= LDS_BYTES, "lds map");
constexpr unsigned SPIN_CAP = 1u << 16;

DI unsigned cvtpk(float a, float b) { f32x2 v = {a, b}; return __builtin_bit_cast(unsigned, __builtin_convertvector(v, bf16x2)); }
DI float rbf(float x) { return __uint_as_float(cvtpk(x, x) << 16); }
DI s16x8 asfrag(u32x4 v) { return __builtin_bit_cast(s16x8, v); }
DI s16x8 lds_frag(lptr p) { return *(const LAS s16x8*)p; }
DI void lds_put(lptr p, u32x4 v) { *(LAS u32x4*)p = v; }
DI s16x4 lds_tr(lptr p) { return __builtin_amdgcn_ds_read_tr16_b64_v4i16((LAS s16x4*)p); }
DI s16x8 tr_frag2(lptr p, lptr q) { s16x4 lo = lds_tr(p), hi = lds_tr(q); return __builtin_shufflevector(lo, hi, 0, 1, 2, 3, 4, 5, 6, 7); }
DI s16x8 tr_frag(lptr p, int second) { return tr_frag2(p, p + second); }
DI float swap_add(float v) { auto r = __builtin_amdgcn_permlane32_swap(__float_as_uint(v), __float_as_uint(v), false, false); return __uint_as_float(r[0]) + __uint_as_float(r[1]); }
DI float swap_max(float v) { auto r = __builtin_amdgcn_permlane32_swap(__float_as_uint(v), __float_as_uint(v), false, false); return fmaxf(__uint_as_float(r[0]), __uint_as_float(r[1])); }
DI float dot2(unsigned a, unsigned b, float c) { return __builtin_amdgcn_fdot2_f32_bf16(__builtin_bit_cast(bf16x2, a), __builtin_bit_cast(bf16x2, b), c, false); }
DI unsigned relu_pk(unsigned u) { return __builtin_bit_cast(unsigned, __builtin_elementwise_max(__builtin_bit_cast(s16x2, u), (s16x2){0, 0})); }
template <int CTRL> DI float dpp(float v) { return __uint_as_float(__builtin_amdgcn_update_dpp(0, __float_as_uint(v), CTRL, 0xf, 0xf, true)); }
DI float row32_max(float v) {
    v = fmaxf(v, dpp<0xB1>(v)); v = fmaxf(v, dpp<0x4E>(v)); v = fmaxf(v, dpp<0x141>(v)); v = fmaxf(v, dpp<0x140>(v));
    auto r = __builtin_amdgcn_permlane16_swap(__float_as_uint(v), __float_as_uint(v), false, false);
    return fmaxf(__uint_as_float(r[0]), __uint_as_float(r[1]));
}
template <int HALF> DI u32x4 pack8(const f32x16& a, float m) {
    u32x4 v;
#pragma unroll
    for (int e = 0; e < 4; ++e) v[e] = cvtpk(a[8 * HALF + 2 * e] * m, a[8 * HALF + 2 * e + 1] * m);
    return v;
}
template <int HALF> DI u32x4 pack8_relu(const f32x16& a) {
    u32x4 v;
#pragma unroll
    for (int e = 0; e < 4; ++e) v[e] = relu_pk(cvtpk(a[8 * HALF + 2 * e], a[8 * HALF + 2 * e + 1]));
    return v;
}
template <int HALF> DI f32x16 pv_mma(s16x8 v, const f32x16& p, f32x16 c, int idx) {
    const auto ph = __builtin_shufflevector(p, p, 8 * HALF, 8 * HALF + 1, 8 * HALF + 2, 8 * HALF + 3, 8 * HALF + 4, 8 * HALF + 5, 8 * HALF + 6, 8 * HALF + 7);
    return __builtin_amdgcn_smfmac_f32_32x32x32_bf16(__builtin_bit_cast(bf16x8, v), __builtin_bit_cast(bf16x16, ph), c, idx, 0, 0);
}
DI void glds16(const void* sbase, unsigned voff, unsigned lds_base) {
    asm volatile("s_mov_b32 m0, %2\n\ts_nop 0\n\tglobal_load_lds_dwordx4 %0, %1" :: "v"(voff), "s"(sbase), "s"(lds_base) : "memory");
}
DI void glds16x2(const void* sbase, unsigned voff0, unsigned voff1, unsigned lds_base) {
    asm volatile("s_mov_b32 m0, %3\n\ts_nop 0\n\tglobal_load_lds_dwordx4 %0, %2\n\tglobal_load_lds_dwordx4 %1, %2 offset:1024" :: "v"(voff0), "v"(voff1), "s"(sbase), "s"(lds_base) : "memory");
}
DI void glds16x4(const void* sbase, unsigned v0, unsigned v1, unsigned v2, unsigned v3, unsigned lds_base) {
    asm volatile("s_mov_b32 m0, %5\n\ts_nop 0\n\tglobal_load_lds_dwordx4 %0, %4\n\tglobal_load_lds_dwordx4 %1, %4 offset:1024\n\t"
                 "global_load_lds_dwordx4 %2, %4 offset:2048\n\tglobal_load_lds_dwordx4 %3, %4 offset:3072" :: "v"(v0), "v"(v1), "v"(v2), "v"(v3), "s"(sbase), "s"(lds_base) : "memory");
}
DI void glds16x2_sc1(const void* sbase, unsigned voff, unsigned lds_base) {
    asm volatile("s_mov_b32 m0, %2\n\ts_nop 0\n\tglobal_load_lds_dwordx4 %0, %1 sc1\n\tglobal_load_lds_dwordx4 %0, %1 offset:1024 sc1" :: "v"(voff), "s"(sbase), "s"(lds_base) : "memory");
}
DI void touch8k(const void* sbase, int lane, unsigned lds_scratch) {
    asm volatile("s_mov_b32 m0, %2\n\ts_nop 0\n\tglobal_load_lds_dword %0, %1" :: "v"(lane * 128), "s"(sbase), "s"(lds_scratch) : "memory");
}
#define WAIT_V(N) asm volatile("s_waitcnt vmcnt(" #N ")" ::: "memory")
DI __amdgpu_buffer_rsrc_t mk_rsrc(const void* p, unsigned bytes) { return __builtin_amdgcn_make_buffer_rsrc((void*)p, 0, bytes, 0x00020000); }
DI u32x4 bload(__amdgpu_buffer_rsrc_t r, unsigned voff, unsigned soff) { return __builtin_amdgcn_raw_buffer_load_b128(r, voff, soff, 0); }
DI u32x4 bload_sc1(__amdgpu_buffer_rsrc_t r, unsigned voff, unsigned soff) { return __builtin_amdgcn_raw_buffer_load_b128(r, voff, soff, 16); }
DI void bstore_sc1(u32x4 v, __amdgpu_buffer_rsrc_t r, unsigned voff) { __builtin_amdgcn_raw_buffer_store_b128(v, r, voff, 0, 16); }

#define STAMP(i) do { } while (0)

DI unsigned long long flag_load(gu64* f) { return __hip_atomic_load(f, RLX_AGENT); }
DI unsigned wait_flags(gu64* f, unsigned long long v, unsigned epoch) {
    if (__builtin_expect(!__all((unsigned)(v >> 32) == epoch), 0)) {
        unsigned spins = 0;
        do { __builtin_amdgcn_s_sleep(6); v = flag_load(f); } while (!__all((unsigned)(v >> 32) == epoch) && ++spins < SPIN_CAP);
    }
    CFENCE();
    return (unsigned)v;
}

struct BigFrag { s16x8 x0, x1, w0, w1; };
struct SmallFrag { s16x8 x, q; };

__global__ void __launch_bounds__(512, 2)
fused_kernel(const uint16_t* __restrict__ x, const uint16_t* __restrict__ wq, const uint16_t* __restrict__ wk, const uint16_t* __restrict__ wv,
             const uint16_t* __restrict__ wo, const uint16_t* __restrict__ w1, const uint16_t* __restrict__ w2, const uint16_t* __restrict__ gw,
             uint16_t* out, char* ws) {
    __shared__ __attribute__((aligned(16))) char smem[LDS_BYTES];
    const lptr lds = (lptr)smem;
    const unsigned lds0 = (unsigned)(uintptr_t)smem;
    const int tid = threadIdx.x, lane = tid & 63, w = __builtin_amdgcn_readfirstlane(tid >> 6);
    const int r = lane & 31, h = lane >> 5, g1 = (lane >> 4) & 1, q4 = (lane & 15) >> 2, p4 = lane & 3;
    const int b = blockIdx.x, bi = b >> 3;
    const int R = 2 * (b & 7) + (bi >> 4), cb = bi & 15, rh64 = cb & 1, hg = cb >> 1;
    const bool kvcu = cb >= 14, big = w < 4;
    const int row64 = R * 128 + rh64 * 64;
    const int rt = w & 1, hq = w >> 1, fh = hq & 1;
    unsigned stv = 0;
    STAMP(0);

    asm volatile("" :: "s"(x), "s"(wq), "s"(wk), "s"(wv), "s"(wo), "s"(w1), "s"(w2), "s"(gw), "s"(out), "s"(ws));
    const unsigned w64_src = (unsigned)((lane >> 3) * 1024 + hg * 128 + (((lane & 7) ^ (g1 << 2)) << 4));
    const unsigned w64_src1 = w64_src + 8192 - 1024;
    const unsigned xv = (unsigned)((lane >> 3) * 1024 + (((lane & 7) ^ ((lane >> 4) & 3)) << 4)), xv1 = (xv ^ 64) + 8192 - 1024;
    const unsigned wv0 = (unsigned)((lane >> 4) * (DFF * 2) + cb * 256 + (((lane & 15) ^ (((lane >> 4) & 3) << 2)) << 4)), wv1 = wv0 + 4 * DFF * 2 - 1024;
    const int rot = (b + cb) & 7;
    if (!kvcu) {
#pragma unroll
        for (int s = 0; s < 2; ++s) {
            const int sr = (s + rot) & 7;
            glds16x2((const char*)x + R * 131072 + sr * 128 + w * 16384, xv, xv1, lds0 + s * L_STAGE + w * 2048);
            glds16x2((const char*)w1 + (size_t)(sr * 64 + w * 8) * (DFF * 2), wv0, wv1, lds0 + s * L_STAGE + 16384 + w * 2048);
            glds16((const char*)wq + sr * 65536 + w * 8192, w64_src, lds0 + s * L_STAGE + 32768 + w * 1024);
        }
    } else {
        const char* wkv = (const char*)((w & 2) ? wk : wv);
        const unsigned kvv = (w & 1) * 1024 + ((lane ^ ((w & 2) << 2)) << 4);
#pragma unroll 1
        for (int s = 0; s < 8; ++s) {
            glds16((const char*)x + (row64 + 8 * w) * 1024 + s * 128, xv ^ (rt << 6), lds0 + s * L_KSTAGE + w * 1024);
            if (!big) glds16(wkv + s * 2048, kvv, lds0 + s * L_KSTAGE + 8192 + (w & 3) * 1024);
        }
    }
    const unsigned epoch_v = __hip_atomic_load((gu32*)(ws + WS_EPOCH), RLX_AGENT);
    asm volatile("" ::: "memory");
    const __amdgpu_buffer_rsrc_t rs_kv = mk_rsrc(ws + WS_KV, KV_BYTES), rs_h = mk_rsrc(ws + WS_H, H_BYTES), rs_o = mk_rsrc(ws + WS_O, O_BYTES);
    unsigned o_own = (unsigned)(((row64 >> 5) + rt) * 32 + hg * 4 + hq) * 1024u + lane * 16;
    u32x4 omark = {~0u, ~0u, ~0u, ~0u};
    const __amdgpu_buffer_rsrc_t rs_kf = mk_rsrc(ws + WS_KVFLAG, 4096);
    const unsigned tr_in = 32 * g1 + 8 * p4;
    const unsigned wq_rd = (8 * h + q4) * 128 + ((fh ^ (q4 >> 1)) << 6) + tr_in;
    unsigned xo[4];
#pragma unroll
    for (int ks = 0; ks < 4; ++ks) xo[ks] = r * 128 + (((2 * ks + h) ^ ((r >> 1) & 7)) << 4);

    lptr kp = lds + lane * 16;
    const unsigned v_cst = (r & 27) != 16 ? 2064 : 2048;
    lptr vp0 = lds + (r < 16 ? 1024 + h * 256 + r * 16 : v_cst), vp1 = lds + (r < 16 ? 1536 + h * 256 + r * 16 : v_cst);
    const unsigned w_rd0 = w * 2048 + (4 * h + q4) * 128 + ((q4 >> 1) << 6) + tr_in;
    lptr wrA0 = lds + L_WB + w_rd0, wrA1 = lds + L_WB + (w_rd0 ^ 64), wrB0 = lds + w_rd0, wrB1 = lds + (w_rd0 ^ 64);
    lptr q_rd = lds + L_QX + (hq * 2 + rt) * 1024 + lane * 16, qn_rd = lds + L_QN + (hq * 2 + rt) * 128 + r * 4, km_rd = lds + L_KM;
    unsigned h_voff = (unsigned)((row64 >> 5) * 112 + 14 * w) * 1024u + lane * 16, wo_voff = (unsigned)((lane >> 3) * 1024 + hg * 128 + (lane & 7) * 16);
    unsigned hf_voff = (unsigned)(R * 16 + (lane & 15)) * 128 + 4, g_voff0 = 32768 + (row64 + 32 * rt + r) * 8, g_voff1 = 32768 + (row64 + 8 * w + (lane >> 3)) * 8 + 4;
    int spidx = 0xDDDD;
    f32x16 oacc = {};
    u32x4 cst = (lane & 1) ? (u32x4){} : (u32x4){0x3f803f80u, 0x3f803f80u, 0x3f803f80u, 0x3f803f80u};
    lptr cst_wr = lds + (tid >> 1) * KVT + 2048 + (tid & 1) * 16;
    unsigned hb = (kvcu ? OOB : (unsigned)((R * 4 + 2 * rt) * 112 + cb * 8 + fh * 4) * 1024u) + lane * 16, hb1 = hb + 112 * 1024;
    s16x8 zf = {};
    PIN(oacc); PIN(spidx); PIN(cst); PIN(cst_wr); PIN(hb); PIN(hb1); PIN(zf); PIN(o_own); PIN(omark);
    PIN(kp); PIN(vp0); PIN(vp1); PIN(wrA0); PIN(wrA1); PIN(wrB0); PIN(wrB1); PIN(q_rd); PIN(qn_rd); PIN(km_rd);
    PIN(h_voff); PIN(wo_voff); PIN(hf_voff); PIN(g_voff0); PIN(g_voff1);
    f32x16 a00 = {}, a01 = {}, a10 = {}, a11 = {};
    auto kv_wait = [&](u32x4 fv) {
        const unsigned ep = __builtin_amdgcn_readfirstlane(epoch_v) + 1u;
        if (__builtin_expect(!__all(fv[3] == ep), 0)) {
            unsigned spins = 0;
            do { __builtin_amdgcn_s_sleep(6); CFENCE(); fv = bload_sc1(rs_kf, r * 128, 0); } while (!__all(fv[3] == ep) && ++spins < SPIN_CAP);
        }
        CFENCE();
        const float km = row32_max(fmaxf(__uint_as_float(fv[0]), __uint_as_float(fv[2])));
        if (tid == 256) *(LAS float*)(lds + L_KM) = __builtin_amdgcn_sqrtf(km) * 1.0001f;
    };
    auto kv_tile = [&](int t) { glds16x2_sc1(ws + WS_KV + ((t + 8 * bi) & 63) * 2048, lane * 16, lds0 + t * KVT); };
    if (!kvcu) {
        unsigned cur = 0, nxt = L_STAGE;
        if (big) {
            const unsigned xb = rt * 8192, w0o = 16384 + (8 * h + q4) * 256 + (((2 * fh) ^ q4) << 6) + tr_in, w1o = w0o ^ 64;
            BigFrag F[4];
            auto rd = [&](BigFrag& n, lptr sb, int ks) {
                n.x0 = lds_frag(sb + xb + xo[ks]); n.x1 = lds_frag(sb + xb + 4096 + xo[ks]); n.w0 = tr_frag(sb + w0o + ks * 4096, 1024); n.w1 = tr_frag(sb + w1o + ks * 4096, 1024); };
#define AM(e, ...) do { if (ABL & 2048) asm volatile("" :: __VA_ARGS__); else e; SBAR(); } while (0)
#define AR(e, ...) do { if (ABL & 4096) asm volatile("" : __VA_ARGS__); else { e; } SBAR(); } while (0)
            auto step = [&](const BigFrag& c, BigFrag& n, lptr sb, int ks) {
                AM(a00 = MFMA(c.w0, c.x0, a00), "v"(c.w0), "v"(c.x0)); AR(n.x0 = lds_frag(sb + xb + xo[ks]); n.x1 = lds_frag(sb + xb + 4096 + xo[ks]), "+v"(n.x0), "+v"(n.x1));
                AM(a10 = MFMA(c.w0, c.x1, a10), "v"(c.x1)); AR(n.w0 = tr_frag(sb + w0o + ks * 4096, 1024), "+v"(n.w0));
                AM(a01 = MFMA(c.w1, c.x0, a01), "v"(c.w1)); AR(n.w1 = tr_frag(sb + w1o + ks * 4096, 1024), "+v"(n.w1));
                AM(a11 = MFMA(c.w1, c.x1, a11), "v"(c.w1)); };
            auto stage = [&](auto bar) {
                step(F[0], F[3], lds + cur, 3);
                bar();
                step(F[1], F[0], lds + nxt, 0); step(F[2], F[1], lds + nxt, 1); step(F[3], F[2], lds + nxt, 2);
                cur = nxt; nxt = (nxt == 2 * L_STAGE) ? 0 : nxt + L_STAGE; };
            STAMP(13);
            BAR_VL(6);
            STAMP(14);
            rd(F[0], lds, 0); rd(F[1], lds, 1); rd(F[2], lds, 2);
#pragma unroll 1
            for (int s = 0; s < 7; ++s) stage([&] {
                BAR_VL(1);
                if (s == 3 && w < 2) touch8k(w == 0 ? (const char*)w2 + (R * 14 + cb) * 8192 : (const char*)wo + (b & 63) * 8192, lane, lds0 + L_TOUCH); });
            STAMP(1);
            rd(F[3], lds + cur, 3);
#pragma unroll
            for (int ks = 0; ks < 4; ++ks) { const BigFrag& c = F[ks]; a00 = MFMA(c.w0, c.x0, a00); a10 = MFMA(c.w0, c.x1, a10); a01 = MFMA(c.w1, c.x0, a01); a11 = MFMA(c.w1, c.x1, a11); }
        } else {
            const int j = w - 4;
            __builtin_amdgcn_s_setprio(2);
            const unsigned xb = (2 * rh64 + rt) * 4096, qo = 32768 + wq_rd;
            SmallFrag F[4];
            auto rd = [&](SmallFrag& n, lptr sb, int ks) { n.x = lds_frag(sb + xb + xo[ks]); n.q = tr_frag(sb + qo + ks * 2048, 512); SBAR(); };
            auto mma = [&](const SmallFrag& c) { a00 = MFMA(c.q, c.x, a00); SBAR(); };
            const unsigned xv2 = xv + 2 * (8192 - 1024), xv3 = xv1 + 2 * (8192 - 1024), wv2 = wv0 + 2 * (4 * DFF * 2 - 1024), wv3 = wv1 + 2 * (4 * DFF * 2 - 1024);
            const char* xs = (const char*)x + R * 131072 + j * 32768;
            const char* w1s = (const char*)w1 + (size_t)(j * 16) * (DFF * 2);
            const char* wqs = (const char*)wq + j * 16384;
            auto fill0 = [&](int sr, unsigned slot) { glds16x4(xs + sr * 128, xv, xv1, xv2, xv3, lds0 + slot + j * 4096); };
            auto fill1 = [&](int sr, unsigned slot) { glds16x4(w1s + sr * (64 * DFF * 2), wv0, wv1, wv2, wv3, lds0 + slot + 16384 + j * 4096); };
            auto fill2 = [&](int sr, unsigned slot) { glds16x2(wqs + sr * 65536, w64_src, w64_src1, lds0 + slot + 32768 + j * 2048); };
            BAR_VL(6);
            rd(F[0], lds, 0); rd(F[1], lds, 1); rd(F[2], lds, 2);
            fill0((rot + 2) & 7, 2 * L_STAGE); fill1((rot + 2) & 7, 2 * L_STAGE); fill2((rot + 2) & 7, 2 * L_STAGE);
#pragma unroll 1
            for (int s = 0; s < 6; ++s) {
                const bool more = s < 5;
                const int sr = (rot + s + 3) & 7;
                rd(F[3], lds + cur, 3); mma(F[0]);
                BAR_VL(10);
                if (more) fill0(sr, cur);
                rd(F[0], lds + nxt, 0); mma(F[1]);
                if (more) fill1(sr, cur);
                rd(F[1], lds + nxt, 1); mma(F[2]);
                if (more) fill2(sr, cur);
                rd(F[2], lds + nxt, 2); mma(F[3]);
                cur = nxt; nxt = (nxt == 2 * L_STAGE) ? 0 : nxt + L_STAGE;
            }
            const u32x4 fv = bload_sc1(rs_kf, r * 128, 0);
            rd(F[3], lds, 3); mma(F[0]);
            BAR_VL(0);
            rd(F[0], lds + L_STAGE, 0); mma(F[1]); rd(F[1], lds + L_STAGE, 1); mma(F[2]); rd(F[2], lds + L_STAGE, 2); mma(F[3]); rd(F[3], lds + L_STAGE, 3);
            STAMP(15);
            kv_wait(fv);
            STAMP(16);
            kv_tile(j); kv_tile(4 + j); kv_tile(8 + j); kv_tile(12 + j);
            mma(F[0]); mma(F[1]); mma(F[2]); mma(F[3]);
        }
    } else {
        const unsigned xb = rt * 4096, w64_src2 = w64_src + 2 * (8192 - 1024), w64_src3 = w64_src + 3 * (8192 - 1024);
        {
            const unsigned wkv_rd = 8192 + g1 * 2048 + (((8 * h + q4) * 32 + 8 * p4) ^ (g1 << 7));
#pragma unroll
            for (int g = 0; g < 2; ++g) {
                if (g == 0) { if (big) BAR_VL(5); else BAR_VL(9); } else BAR_VL(1);
                const lptr st = lds + (g * 4 + hq) * L_KSTAGE;
#pragma unroll
                for (int k = 0; k < 4; ++k) a00 = MFMA(tr_frag2(st + wkv_rd + k * 512, st + (wkv_rd ^ 128) + k * 512), lds_frag(st + xb + xo[k]), a00);
            }
        }
        BAR_L();
        {
            const lptr pw = lds + w * L_KSTAGE + 8192 + r * 128;
            const unsigned sz = (r & 7) ^ ((r >> 1) & 4);
#pragma unroll
            for (int i = 0; i < 4; ++i) *(LAS f32x4*)(pw + (((2 * i + h) ^ sz) << 4)) = (f32x4){a00[4 * i], a00[4 * i + 1], a00[4 * i + 2], a00[4 * i + 3]};
            a00 = (f32x16){};
        }
        BAR_L();
        if (big) {
            STAMP(1);
            const unsigned kvo = (unsigned)((row64 >> 5) + rt) * 2048u + lane * 16;
            const lptr pr = lds + rt * L_KSTAGE + 8192;
            gu32* const fl = (gu32*)(ws + WS_KVFLAG) + (row64 >> 6) * 32;
            float n2 = 0.f;
            if (fh == 0) {
                const unsigned sz = (r & 7) ^ ((r >> 1) & 4);
                f32x4 lo = {}, hi = {};
#pragma unroll
                for (int i = 0; i < 4; ++i) { lo += *(const LAS f32x4*)(pr + i * 2 * L_KSTAGE + r * 128 + (((4 + h) ^ sz) << 4)); hi += *(const LAS f32x4*)(pr + i * 2 * L_KSTAGE + r * 128 + (((6 + h) ^ sz) << 4)); }
                const u32x4 v = {cvtpk(lo[0], lo[1]), cvtpk(lo[2], lo[3]), cvtpk(hi[0], hi[1]), cvtpk(hi[2], hi[3])};
                bstore_sc1(v, rs_kv, kvo);
                n2 = row32_max(swap_add(dot2(v[0], v[0], dot2(v[1], v[1], 0.f)) + dot2(v[2], v[2], dot2(v[3], v[3], 0.f))));
                if (tid == 64) __hip_atomic_store(fl, __float_as_uint(n2), RLX_AGENT);
            } else {
                float t[8];
#pragma unroll
                for (int j = 0; j < 8; ++j) {
                    const lptr q = pr + ((lane >> 4) * 8 + j) * 128 + ((q4 ^ j ^ (g1 << 2)) << 4) + p4 * 4;
                    t[j] = (*(const LAS float*)q + *(const LAS float*)(q + 2 * L_KSTAGE)) + (*(const LAS float*)(q + 4 * L_KSTAGE) + *(const LAS float*)(q + 6 * L_KSTAGE));
                }
                bstore_sc1((u32x4){cvtpk(t[0], t[1]), cvtpk(t[2], t[3]), cvtpk(t[4], t[5]), cvtpk(t[6], t[7])}, rs_kv, kvo + 1024);
            }
            STAMP(13);
            BAR_VL(0);
            STAMP(14);
            if (tid == 0) __hip_atomic_store((gu64*)(fl + 2), ((unsigned long long)(epoch_v + 1u) << 32) | __float_as_uint(n2), RLX_AGENT);
            u32x4 gf[16];
#pragma unroll
            for (int i = 0; i < 16; ++i) gf[i] = *(const u32x4*)(gw + i * 32 + (lane >> 4) * 8);
            BAR_L();
            f32x4 z = {};
            const int zr = 16 * w + (lane & 15);
#pragma unroll
            for (int i = 0; i < 16; ++i)
                z = __builtin_amdgcn_mfma_f32_16x16x32_bf16(asfrag(gf[i]), lds_frag(lds + (i >> 1) * L_KSTAGE + zr * 128 + (((4 * (i & 1) + (lane >> 4)) ^ ((zr >> 1) & 7)) << 4)), z, 0, 0, 0);
            const float gt = rbf(__builtin_amdgcn_rcpf(1.f + __expf(-rbf(z[0])))), og = fmaxf(rbf(1.f - gt), 0x1p-40f);
            if (lane < 16) __hip_atomic_store((gu64*)(ws + WS_GZ) + row64 + zr, ((unsigned long long)__float_as_uint(og) << 32) | __float_as_uint(gt / og), RLX_AGENT);
            BAR_L();
        } else {
#pragma unroll
            for (int i = 0; i < 4; ++i) { const int p = i * 16 + (w - 4) * 4; glds16x4((const char*)wq + p * 8192, w64_src, w64_src1, w64_src2, w64_src3, lds0 + L_KWQ + p * 1024); }
            BAR_L();
            BAR_VL(0);
            SmallFrag F[4];
            auto rd = [&](SmallFrag& n, int s, int k) { n.x = lds_frag(lds + s * L_KSTAGE + xb + xo[k]); n.q = tr_frag(lds + L_KWQ + s * 8192 + wq_rd + k * 2048, 512); SBAR(); };
#pragma unroll
            for (int k = 0; k < 4; ++k) rd(F[k], 0, k);
#pragma unroll 1
            for (int s = 1; s < 8; ++s) {
#pragma unroll
                for (int k = 0; k < 4; ++k) { a00 = MFMA(F[k].q, F[k].x, a00); SBAR(); rd(F[k], s, k); }
            }
            const u32x4 fv = bload_sc1(rs_kf, r * 128, 0);
#pragma unroll
            for (int k = 0; k < 4; ++k) a00 = MFMA(F[k].q, F[k].x, a00);
            BAR_L();
            STAMP(15);
            kv_wait(fv);
            STAMP(16);
            kv_tile(w - 4); kv_tile(w); kv_tile(4 + w); kv_tile(8 + w);
        }
    }
    STAMP(2);
    u32x4 hs[8];
    if (big) {
        hs[0] = pack8_relu<0>(a00); hs[1] = pack8_relu<1>(a00); hs[2] = pack8_relu<0>(a01); hs[3] = pack8_relu<1>(a01);
        hs[4] = pack8_relu<0>(a10); hs[5] = pack8_relu<1>(a10); hs[6] = pack8_relu<0>(a11); hs[7] = pack8_relu<1>(a11);
        if (tid < 32) lds_put(cst_wr, cst);
    } else {
#pragma unroll
        for (int s = 0; s < 2; ++s) {
            const u32x4 v = s ? pack8<1>(a00, QSCALE) : pack8<0>(a00, QSCALE);
            lds_put(lds + L_QX + (4 * fh + 2 * s + rt) * 1024 + lane * 16, v);
            const float n2 = swap_add(dot2(v[0], v[0], dot2(v[1], v[1], 0.f)) + dot2(v[2], v[2], dot2(v[3], v[3], 0.f)));
            *(LAS float*)(lds + L_QN + (4 * fh + 2 * s + rt) * 128 + r * 4) = __builtin_amdgcn_sqrtf(n2);
        }
#pragma unroll
        for (int i = 0; i < 8; ++i) asm volatile("" : "=v"(hs[i]));
    }
    __builtin_amdgcn_s_setprio(3);
    BAR_VL(0);
    STAMP(3);
    const s16x8 qf = lds_frag(q_rd);
    s16x8 kf = lds_frag(kp), vf0, vf1;
    float mshift = *(const LAS float*)qn_rd * *(const LAS float*)km_rd;
    vf0 = lds_frag(vp0);
    if (__builtin_expect(!__all(mshift <= 0.5f * SAFE_RANGE), 0)) {
        float m = -__builtin_inff();
#pragma unroll 1
        for (int t = 0; t < 64; ++t) {
            const f32x16 s = MFMA(asfrag(bload_sc1(rs_kv, lane * 16, t * 2048)), qf, (f32x16){});
#pragma unroll
            for (int i = 0; i < 16; ++i) m = fmaxf(m, s[i]);
        }
        mshift = swap_max(m);
    }
    f32x16 cinit;
#pragma unroll
    for (int i = 0; i < 16; ++i) cinit[i] = -mshift;
    PIN(cinit);
    f32x16 sA = MFMA(kf, qf, cinit), sB;
    kf = lds_frag(kp + KVT);
    if (tid >= 32 && tid < 128) lds_put(cst_wr, cst);

#define S1(S_, i) S_[i] = __builtin_amdgcn_exp2f(S_[i])
#define EX2(S_, i) do { S1(S_, i); S1(S_, (i) + 1); } while (0)
#define EX3(S_, i) do { S1(S_, i); S1(S_, (i) + 1); S1(S_, (i) + 2); } while (0)
#define EX4(S_, i) do { EX2(S_, i); EX2(S_, (i) + 2); } while (0)
#define PV1(SN, T) do { if ((T) >= 0) oacc = pv_mma<1>(vf1, SN, oacc, spidx); vf1 = lds_frag(vp1 + ((T) < 0 ? 0 : (T)) * KVT); SBAR(); } while (0)
#define QK(SN, T) do { SN = MFMA(kf, qf, cinit); kf = lds_frag(kp + ((T) + 2) * KVT); SBAR(); } while (0)
#define PV0(SC, T) do { oacc = pv_mma<0>(vf0, SC, oacc, spidx); vf0 = lds_frag(vp0 + ((T) + 1) * KVT); SBAR(); } while (0)
#define CUT(S_) do { PIN(S_); SBAR(); } while (0)
#define ITER0(SC, SN, T, T1, PR, MEM) do { PR; \
    EX2(SC, 0); CUT(SC); PV1(SN, T1); EX4(SC, 2); CUT(SC); QK(SN, T); EX4(SC, 6); S1(SC, 10); CUT(SC); PV0(SC, T); MEM; S1(SC, 11); EX4(SC, 12); CUT(SC); } while (0)
#define ITER1(SC, SN, T, PR, PRE, MEM, G1) do { PR; PRE; \
    EX2(SC, 0); CUT(SC); PV1(SN, T); EX4(SC, 2); CUT(SC); QK(SN, T); EX4(SC, 6); CUT(SC); PV0(SC, T); MEM; EX4(SC, 10); CUT(SC); G1; SBAR(); EX2(SC, 14); CUT(SC); } while (0)
#define ITERG(SC, SN, T, PR, MEM, G0, G1) do { PR; \
    EX2(SC, 0); CUT(SC); PV1(SN, T); EX3(SC, 2); CUT(SC); QK(SN, T); EX3(SC, 5); CUT(SC); G0; SBAR(); EX3(SC, 8); CUT(SC); PV0(SC, T); MEM; EX3(SC, 11); CUT(SC); G1; SBAR(); EX2(SC, 14); CUT(SC); } while (0)
#define NOP (void)0
#define PRIO(p) __builtin_amdgcn_s_setprio(p)
#define ITER2(T, P0, P1, M0, M1) do { ITER0(sA, sB, T, T, PRIO(P0), M0); ITER0(sB, sA, (T) + 1, (T) + 1, PRIO(P1), M1); } while (0)
#define ADV(n) do { kp += (n) * KVT; vp0 += (n) * KVT; vp1 += (n) * KVT; PIN(kp); PIN(vp0); PIN(vp1); } while (0)
    const char* const w2w = (const char*)w2 + w * (14 * 16384);
    const char* const wow = (const char*)wo + w * 65536;
    int pn = (row64 >> 6) % 7;
    const unsigned wbA = lds0 + L_WB + w * 2048, wbB = lds0 + w * 2048;
    const __amdgpu_buffer_rsrc_t rs_f = mk_rsrc(ws + WS_HFLAG, 32768 + 16384);
    u32x4 hA0, hA1, hB0, hB1;
    s16x8 wf0, wf1;
#define W_LD(src, buf) glds16x2(src, w64_src, w64_src1, buf)
    STAMP(4);
#define H_ST(i) do { if (big) bstore_sc1(hs[i], rs_h, ((i) < 4 ? hb : hb1) + ((i) & 3) * 1024); } while (0)
#define EP(a, b) b
    ITER0(sA, sB, 0, -1, PRIO(EP(2, 3)), H_ST(0)); ITER0(sB, sA, 1, 1, PRIO(EP(1, 3)), H_ST(1); kv_tile(16 + w));
    ITER2(2, EP(3, 3), EP(2, 3), H_ST(2); kv_tile(24 + w), H_ST(3); kv_tile(32 + w));
    ITER2(4, EP(1, 2), EP(3, 2), H_ST(4); kv_tile(40 + w), H_ST(5); kv_tile(48 + w));
    ITER2(6, EP(2, 2), EP(1, 2), H_ST(6); kv_tile(56 + w), H_ST(7); W_LD(w2w + pn * 32768, wbA));
    ITER2(8, EP(3, 1), EP(2, 1), bstore_sc1(omark, rs_o, o_own), NOP);
    ITER2(10, EP(1, 1), EP(3, 1), NOP, NOP);
    ITER2(12, EP(2, 0), EP(0, 0), NOP, NOP);
    STAMP(19);
    BAR_VL(0);
    unsigned epoch_w = epoch_v; PIN(epoch_w);
    const unsigned epoch = __builtin_amdgcn_readfirstlane(epoch_w) + 1u;
    if (tid == 0) __hip_atomic_store((gu32*)(ws + WS_HFLAG) + (R * 16 + cb) * 32 + 1, epoch, RLX_AGENT);
    STAMP(5);
    unsigned hfv;
    ITER2(14, 1, 3, W_LD(w2w + pn * 32768 + 16384, wbB); a00 = MFMA(zf, zf, (f32x16){}); PIN(zf); a01 = MFMA(zf, zf, (f32x16){}), PIN(zf); a10 = MFMA(zf, zf, (f32x16){}); PIN(zf); a11 = MFMA(zf, zf, (f32x16){}));
    ITER2(16, 2, 1, NOP, hfv = __builtin_amdgcn_raw_buffer_load_b32(rs_f, hf_voff, 0, 16));
    ITER0(sA, sB, 18, 18, PRIO(3), NOP);
    STAMP(6);
    if (__builtin_expect(!__all(hfv == epoch), 0)) {
        unsigned spins = 0;
        do { __builtin_amdgcn_s_sleep(6); CFENCE(); hfv = __builtin_amdgcn_raw_buffer_load_b32(rs_f, hf_voff, 0, 16); } while (!__all(hfv == epoch) && ++spins < SPIN_CAP);
    }
    CFENCE();
    STAMP(7);
    unsigned gr_u, og_u;
    ITER0(sB, sA, 19, 19, PRIO(2), gr_u = __builtin_amdgcn_raw_buffer_load_b32(rs_f, g_voff0, 0, 16); og_u = __builtin_amdgcn_raw_buffer_load_b32(rs_f, g_voff1, 0, 16);
          hA0 = bload_sc1(rs_h, h_voff, pn * 2048); hA1 = bload_sc1(rs_h, h_voff, pn * 2048 + 112 * 1024));
    ITER2(20, 1, 3, hB0 = bload_sc1(rs_h, h_voff, pn * 2048 + 1024), hB1 = bload_sc1(rs_h, h_voff, pn * 2048 + 113 * 1024));
    ADV(22);
    PIN(a00); PIN(a01); PIN(a10); PIN(a11);
    STAMP(8);
#define GM(acc, wf, H) acc = MFMA(wf, asfrag(H), acc); SBAR()
#define GSTEP(SX, SY, T, R0, R1, H0, H1, LW, LH0, LH1) do { \
    ITER1(SX, SY, T, PRIO(3), WAIT_V(5); wf0 = tr_frag(R0, 1024); wf1 = tr_frag(R1, 1024), NOP, GM(a00, wf0, H0)); \
    ITERG(SY, SX, (T) + 1, PRIO(2), LW, GM(a01, wf1, H0); LH0, GM(a10, wf0, H1)); \
    ITER1(SX, SY, (T) + 2, PRIO(1), NOP, NOP, GM(a11, wf1, H1); LH1); } while (0)
#pragma unroll 1
    for (int n = 0; n < 7; ++n) {
        const bool last = n == 6;
        pn = pn == 6 ? 0 : pn + 1;
        const char* wsrc = last ? wow : w2w + pn * 32768;
        const __amdgpu_buffer_rsrc_t rs = mk_rsrc(last ? wow + 32768 : ws + WS_H + pn * 2048, last ? 32768 : H_BYTES);
        if (last) h_voff = wo_voff;
        GSTEP(sA, sB, 0, wrA0, wrA1, hA0, hA1, W_LD(wsrc, wbA), hA0 = bload_sc1(rs, h_voff, 0), hA1 = bload_sc1(rs, h_voff, last ? 8192 : 112 * 1024));
        GSTEP(sB, sA, 3, wrB0, wrB1, hB0, hB1, W_LD(wsrc + 16384, wbB), hB0 = bload_sc1(rs, h_voff, last ? 16384 : 1024), hB1 = bload_sc1(rs, h_voff, last ? 24576 : 113 * 1024));
        ADV(6);
    }
    STAMP(9);
    PRIO(3);
    oacc = pv_mma<1>(vf1, sB, oacc, spidx);
    {
        u32x4 v = pack8<0>(oacc, __builtin_amdgcn_rcpf(oacc[8]) * __uint_as_float(gr_u));
#pragma unroll
        for (int e = 0; e < 4; ++e) v[e] = v[e] < 0xfffffffeu ? v[e] : 0xfffffffeu;
        bstore_sc1(v, rs_o, o_own);
        asm volatile("s_nop 1");
    }
    PRIO(0);
    s16x8 wof[8];
    WAIT_V(2);
    wof[0] = tr_frag(wrA0, 1024); wof[1] = tr_frag(wrA1, 1024); wof[2] = tr_frag(wrB0, 1024); wof[3] = tr_frag(wrB1, 1024);
    {
        const unsigned wr = w * 2048 + (lane >> 3) * 128 + (((lane & 7) ^ (g1 << 2)) << 4);
        lds_put(lds + L_WB + wr, hA0); lds_put(lds + L_WB + wr + 1024, hA1); lds_put(lds + wr, hB0); lds_put(lds + wr + 1024, hB1);
    }
    wof[4] = tr_frag(wrA0, 1024); wof[5] = tr_frag(wrA1, 1024); wof[6] = tr_frag(wrB0, 1024); wof[7] = tr_frag(wrB1, 1024);
    const unsigned ob = (unsigned)((row64 >> 5) * 32 + 4 * w) * 1024u;
    u32x4 of[8];
    auto o_load = [&]() {
#pragma unroll
        for (int i = 0; i < 8; ++i) of[i] = bload_sc1(rs_o, lane * 16 + (i & 3) * 1024, ob + (i >> 2) * 32768);
    };
    auto o_ready = [&]() {
        unsigned m = 0;
#pragma unroll
        for (int i = 0; i < 8; ++i) { m = max(m, max(of[i][0], of[i][1])); m = max(m, max(of[i][2], of[i][3])); }
        return __all(m != ~0u);
    };
    BAR_L();
    STAMP(10);
    o_load();
    if (!o_ready()) {
        unsigned spins = 0;
        do { CFENCE(); o_load(); } while (!o_ready() && ++spins < SPIN_CAP);
    }
    STAMP(11);
    const lptr red_w = lds + w * 16384 + r * 256;
    auto put = [&](unsigned off, const f32x16& a) {
#pragma unroll
        for (int gq = 0; gq < 4; ++gq) *(LAS f32x4*)(red_w + off + (((2 * gq + h) ^ (r & 7)) << 4)) = (f32x4){a[4 * gq], a[4 * gq + 1], a[4 * gq + 2], a[4 * gq + 3]};
    };
#pragma unroll
    for (int i = 0; i < 4; ++i) a00 = MFMA(wof[2 * i], asfrag(of[i]), a00);
#pragma unroll
    for (int i = 0; i < 4; ++i) a01 = MFMA(wof[2 * i + 1], asfrag(of[i]), a01);
    SBAR(); put(0, a00); SBAR();
#pragma unroll
    for (int i = 0; i < 4; ++i) a10 = MFMA(wof[2 * i], asfrag(of[4 + i]), a10);
    SBAR(); put(128, a01); SBAR();
#pragma unroll
    for (int i = 0; i < 4; ++i) a11 = MFMA(wof[2 * i + 1], asfrag(of[4 + i]), a11);
    put(8192, a10); put(8192 + 128, a11);
    unsigned long long ef[4];
    gu64* const efp = (gu64*)(ws + WS_HFLAG) + lane * 16;
    if (b == 112 && w == 0) {
#pragma unroll
        for (int i = 0; i < 4; ++i) ef[i] = flag_load(efp + i * 1024);
    }
    BAR_L();
    {
        const lptr red_r = lds + (8 * w + (lane >> 3)) * 256;
        const unsigned rc0 = ((2 * (lane & 7)) ^ ((lane >> 3) & 7)) << 4, rc1 = rc0 ^ 16;
        f32x4 lo = *(const LAS f32x4*)(red_r + rc0), hi = *(const LAS f32x4*)(red_r + rc1);
#pragma unroll
        for (int k = 1; k < 8; ++k) { lo += *(const LAS f32x4*)(red_r + k * 16384 + rc0); hi += *(const LAS f32x4*)(red_r + k * 16384 + rc1); }
        lo *= __uint_as_float(og_u); hi *= __uint_as_float(og_u);
        const u32x4 v = {cvtpk(lo[0], lo[1]), cvtpk(lo[2], lo[3]), cvtpk(hi[0], hi[1]), cvtpk(hi[2], hi[3])};
        __builtin_nontemporal_store(v, (u32x4*)(out + (row64 + 8 * w + (lane >> 3)) * 512 + hg * 64 + (lane & 7) * 8));
    }
    if (b == 112 && w == 0) {
#pragma unroll
        for (int i = 0; i < 4; ++i) wait_flags(efp + i * 1024, ef[i], epoch);
        if (lane == 0) __hip_atomic_store((gu32*)(ws + WS_EPOCH), epoch, RLX_AGENT);
    }
    STAMP(12);
}

extern "C" void kernel_launch(void* const* d_in, const int* in_sizes, int n_in, void* d_out, int out_size, void* d_ws, size_t ws_size,
                              hipStream_t stream) {
    fused_kernel<<<256, 512, 0, stream>>>((const uint16_t*)d_in[0], (const uint16_t*)d_in[1], (const uint16_t*)d_in[2], (const uint16_t*)d_in[3],
                                          (const uint16_t*)d_in[4], (const uint16_t*)d_in[6], (const uint16_t*)d_in[7], (const uint16_t*)d_in[8],
                                          (uint16_t*)d_out, (char*)d_ws);
}
```

```cpp
#include <hip/hip_runtime.h>
#include <stdint.h>

typedef short s16x8 __attribute__((ext_vector_type(8)));
typedef short s16x4 __attribute__((ext_vector_type(4)));
typedef short s16x2 __attribute__((ext_vector_type(2)));
typedef float f32x16 __attribute__((ext_vector_type(16)));
typedef float f32x4 __attribute__((ext_vector_type(4)));
typedef float f32x2 __attribute__((ext_vector_type(2)));
typedef unsigned u32x4 __attribute__((ext_vector_type(4)));
typedef __bf16 bf16x2 __attribute__((ext_vector_type(2)));
typedef __bf16 bf16x8 __attribute__((ext_vector_type(8)));
typedef __bf16 bf16x16 __attribute__((ext_vector_type(16)));
#define LAS __attribute__((address_space(3)))
#define GAS __attribute__((address_space(1)))
typedef GAS unsigned gu32;
typedef GAS unsigned long long gu64;
typedef LAS char* lptr;
#define DI __device__ __forceinline__
#define RLX_AGENT __ATOMIC_RELAXED, __HIP_MEMORY_SCOPE_AGENT
#define MFMA(a, b, c) __builtin_amdgcn_mfma_f32_32x32x16_bf16((a), (b), (c), 0, 0, 0)
#define SBAR() __builtin_amdgcn_sched_barrier(0)
#define PIN(x) asm volatile("" : "+v"(x))
#define CFENCE() do { asm volatile("" ::: "memory"); __builtin_amdgcn_fence(__ATOMIC_ACQUIRE, "wavefront"); } while (0)
#define BAR_L() asm volatile("s_waitcnt lgkmcnt(0)\n\ts_barrier" ::: "memory")
#define BAR_VL(N) asm volatile("s_waitcnt vmcnt(" #N ") lgkmcnt(0)\n\ts_barrier" ::: "memory")

#define ABL 1
#define STW 0
#define STAMPS 0

constexpr int D = 512, DFF = 1792;
constexpr float QSCALE = 0.25f * 1.4426950408889634f;
constexpr float SAFE_RANGE = 64.f;
constexpr unsigned WS_EPOCH = 0, WS_KVFLAG = 4096, WS_HFLAG = 8192;
constexpr unsigned WS_GZ = WS_HFLAG + 32768;
constexpr unsigned WS_KV = WS_GZ + 16384, KV_BYTES = 64 * 2048;
constexpr unsigned WS_H = WS_KV + KV_BYTES, H_BYTES = 64 * 112 * 1024;
constexpr unsigned WS_O = WS_H + H_BYTES, O_BYTES = 64 * 32 * 1024;
constexpr unsigned WS_STAMP = WS_O + O_BYTES;
constexpr unsigned OOB = 0x7ff00000u;
constexpr unsigned L_STAGE = 40960;
constexpr unsigned L_KSTAGE = 12288, L_KWQ = 98304;
constexpr unsigned KVT = 2080;
constexpr unsigned L_WB = 64 * KVT;
constexpr unsigned L_QX = L_WB + 16384, L_QN = L_QX + 8192, L_KM = L_QN + 1024, L_TOUCH = L_KM + 256, LDS_BYTES = 163840;
static_assert(L_TOUCH + 256 <= LDS_BYTES, "lds map");
constexpr unsigned SPIN_CAP = 1u << 16;

DI unsigned cvtpk(float a, float b) { f32x2 v = {a, b}; return __builtin_bit_cast(unsigned, __builtin_convertvector(v, bf16x2)); }
DI float rbf(float x) { return __uint_as_float(cvtpk(x, x) << 16); }
DI s16x8 asfrag(u32x4 v) { return __builtin_bit_cast(s16x8, v); }
DI s16x8 lds_frag(lptr p) { return *(const LAS s16x8*)p; }
DI void lds_put(lptr p, u32x4 v) { *(LAS u32x4*)p = v; }
DI s16x4 lds_tr(lptr p) { return __builtin_amdgcn_ds_read_tr16_b64_v4i16((LAS s16x4*)p); }
DI s16x8 tr_frag2(lptr p, lptr q) { s16x4 lo = lds_tr(p), hi = lds_tr(q); return __builtin_shufflevector(lo, hi, 0, 1, 2, 3, 4, 5, 6, 7); }
DI s16x8 tr_frag(lptr p, int second) { return tr_frag2(p, p + second); }
DI float swap_add(float v) { auto r = __builtin_amdgcn_permlane32_swap(__float_as_uint(v), __float_as_uint(v), false, false); return __uint_as_float(r[0]) + __uint_as_float(r[1]); }
DI float swap_max(float v) { auto r = __builtin_amdgcn_permlane32_swap(__float_as_uint(v), __float_as_uint(v), false, false); return fmaxf(__uint_as_float(r[0]), __uint_as_float(r[1])); }
DI float dot2(unsigned a, unsigned b, float c) { return __builtin_amdgcn_fdot2_f32_bf16(__builtin_bit_cast(bf16x2, a), __builtin_bit_cast(bf16x2, b), c, false); }
DI unsigned relu_pk(unsigned u) { return __builtin_bit_cast(unsigned, __builtin_elementwise_max(__builtin_bit_cast(s16x2, u), (s16x2){0, 0})); }
template <int CTRL> DI float dpp(float v) { return __uint_as_float(__builtin_amdgcn_update_dpp(0, __float_as_uint(v), CTRL, 0xf, 0xf, true)); }
DI float row32_max(float v) {
    v = fmaxf(v, dpp<0xB1>(v)); v = fmaxf(v, dpp<0x4E>(v)); v = fmaxf(v, dpp<0x141>(v)); v = fmaxf(v, dpp<0x140>(v));
    auto r = __builtin_amdgcn_permlane16_swap(__float_as_uint(v), __float_as_uint(v), false, false);
    return fmaxf(__uint_as_float(r[0]), __uint_as_float(r[1]));
}
template <int HALF> DI u32x4 pack8(const f32x16& a, float m) {
    u32x4 v;
#pragma unroll
    for (int e = 0; e < 4; ++e) v[e] = cvtpk(a[8 * HALF + 2 * e] * m, a[8 * HALF + 2 * e + 1] * m);
    return v;
}
template <int HALF> DI u32x4 pack8_relu(const f32x16& a) {
    u32x4 v;
#pragma unroll
    for (int e = 0; e < 4; ++e) v[e] = relu_pk(cvtpk(a[8 * HALF + 2 * e], a[8 * HALF + 2 * e + 1]));
    return v;
}
template <int HALF> DI f32x16 pv_mma(s16x8 v, const f32x16& p, f32x16 c, int idx) {
    const auto ph = __builtin_shufflevector(p, p, 8 * HALF, 8 * HALF + 1, 8 * HALF + 2, 8 * HALF + 3, 8 * HALF + 4, 8 * HALF + 5, 8 * HALF + 6, 8 * HALF + 7);
    return __builtin_amdgcn_smfmac_f32_32x32x32_bf16(__builtin_bit_cast(bf16x8, v), __builtin_bit_cast(bf16x16, ph), c, idx, 0, 0);
}
DI void glds16(const void* sbase, unsigned voff, unsigned lds_base) {
    asm volatile("s_mov_b32 m0, %2\n\ts_nop 0\n\tglobal_load_lds_dwordx4 %0, %1" :: "v"(voff), "s"(sbase), "s"(lds_base) : "memory");
}
DI void glds16x2(const void* sbase, unsigned voff0, unsigned voff1, unsigned lds_base) {
    asm volatile("s_mov_b32 m0, %3\n\ts_nop 0\n\tglobal_load_lds_dwordx4 %0, %2\n\tglobal_load_lds_dwordx4 %1, %2 offset:1024" :: "v"(voff0), "v"(voff1), "s"(sbase), "s"(lds_base) : "memory");
}
DI void glds16x4(const void* sbase, unsigned v0, unsigned v1, unsigned v2, unsigned v3, unsigned lds_base) {
    asm volatile("s_mov_b32 m0, %5\n\ts_nop 0\n\tglobal_load_lds_dwordx4 %0, %4\n\tglobal_load_lds_dwordx4 %1, %4 offset:1024\n\t"
                 "global_load_lds_dwordx4 %2, %4 offset:2048\n\tglobal_load_lds_dwordx4 %3, %4 offset:3072" :: "v"(v0), "v"(v1), "v"(v2), "v"(v3), "s"(sbase), "s"(lds_base) : "memory");
}
DI void glds16x2_sc1(const void* sbase, unsigned voff, unsigned lds_base) {
    asm volatile("s_mov_b32 m0, %2\n\ts_nop 0\n\tglobal_load_lds_dwordx4 %0, %1 sc1\n\tglobal_load_lds_dwordx4 %0, %1 offset:1024 sc1" :: "v"(voff), "s"(sbase), "s"(lds_base) : "memory");
}
DI void touch8k(const void* sbase, int lane, unsigned lds_scratch) {
    asm volatile("s_mov_b32 m0, %2\n\ts_nop 0\n\tglobal_load_lds_dword %0, %1" :: "v"(lane * 128), "s"(sbase), "s"(lds_scratch) : "memory");
}
#define WAIT_V(N) asm volatile("s_waitcnt vmcnt(" #N ")" ::: "memory")
DI __amdgpu_buffer_rsrc_t mk_rsrc(const void* p, unsigned bytes) { return __builtin_amdgcn_make_buffer_rsrc((void*)p, 0, bytes, 0x00020000); }
DI u32x4 bload(__amdgpu_buffer_rsrc_t r, unsigned voff, unsigned soff) { return __builtin_amdgcn_raw_buffer_load_b128(r, voff, soff, 0); }
DI u32x4 bload_sc1(__amdgpu_buffer_rsrc_t r, unsigned voff, unsigned soff) { return __builtin_amdgcn_raw_buffer_load_b128(r, voff, soff, 16); }
DI void bstore_sc1(u32x4 v, __amdgpu_buffer_rsrc_t r, unsigned voff) { __builtin_amdgcn_raw_buffer_store_b128(v, r, voff, 0, 16); }

#define STAMP(i) do { } while (0)

struct BigFrag { s16x8 x0, x1, w0, w1; };
struct SmallFrag { s16x8 x, q; };

__global__ void __launch_bounds__(512, 2)
fused_kernel(const uint16_t* __restrict__ x, const uint16_t* __restrict__ wq, const uint16_t* __restrict__ wk, const uint16_t* __restrict__ wv,
             const uint16_t* __restrict__ wo, const uint16_t* __restrict__ w1, const uint16_t* __restrict__ w2, const uint16_t* __restrict__ gw,
             uint16_t* out, char* ws) {
    __shared__ __attribute__((aligned(16))) char smem[LDS_BYTES];
    const lptr lds = (lptr)smem;
    const unsigned lds0 = (unsigned)(uintptr_t)smem;
    const int tid = threadIdx.x, lane = tid & 63, w = __builtin_amdgcn_readfirstlane(tid >> 6);
    const int r = lane & 31, h = lane >> 5, g1 = (lane >> 4) & 1, q4 = (lane & 15) >> 2, p4 = lane & 3;
    const int b = blockIdx.x, bi = b >> 3;
    const int R = 2 * (b & 7) + (bi >> 4), cb = bi & 15, rh64 = cb & 1, hg = cb >> 1;
    const bool kvcu = cb >= 14, big = w < 4;
    const int row64 = R * 128 + rh64 * 64;
    const int rt = w & 1, hq = w >> 1, fh = hq & 1;
    unsigned stv = 0;
    STAMP(0);

#if !(ABL & 1)
    {
        const char* pc;
        asm volatile("s_getpc_b64 %0" : "=s"(pc));
        if (w >= 5) touch8k(pc + (7 - w) * 6144, lane, lds0 + L_TOUCH);
    }
#endif
    asm volatile("" :: "s"(x), "s"(wq), "s"(wk), "s"(wv), "s"(wo), "s"(w1), "s"(w2), "s"(gw), "s"(out), "s"(ws));
    const unsigned w64_src = (unsigned)((lane >> 3) * 1024 + hg * 128 + (((lane & 7) ^ (g1 << 2)) << 4));
    const unsigned w64_src1 = w64_src + 8192 - 1024;
    const unsigned xv = (unsigned)((lane >> 3) * 1024 + (((lane & 7) ^ ((lane >> 4) & 3)) << 4)), xv1 = (xv ^ 64) + 8192 - 1024;
    const unsigned wv0 = (unsigned)((lane >> 4) * (DFF * 2) + cb * 256 + (((lane & 15) ^ (((lane >> 4) & 3) << 2)) << 4)), wv1 = wv0 + 4 * DFF * 2 - 1024;
    const int rot = (b + cb) & 7;
    if (!kvcu) {
#pragma unroll
        for (int s = 0; s < 2; ++s) {
            const int sr = (s + rot) & 7;
            glds16x2((const char*)x + R * 131072 + sr * 128 + w * 16384, xv, xv1, lds0 + s * L_STAGE + w * 2048);
            glds16x2((const char*)w1 + (size_t)(sr * 64 + w * 8) * (DFF * 2), wv0, wv1, lds0 + s * L_STAGE + 16384 + w * 2048);
            glds16((const char*)wq + sr * 65536 + w * 8192, w64_src, lds0 + s * L_STAGE + 32768 + w * 1024);
        }
    } else {
        const char* wkv = (const char*)((w & 2) ? wk : wv);
        const unsigned kvv = (w & 1) * 1024 + ((lane ^ ((w & 2) << 2)) << 4);
#pragma unroll 1
        for (int s = 0; s < 8; ++s) {
            glds16((const char*)x + (row64 + 8 * w) * 1024 + s * 128, xv ^ (rt << 6), lds0 + s * L_KSTAGE + w * 1024);
            if (!big) glds16(wkv + s * 2048, kvv, lds0 + s * L_KSTAGE + 8192 + (w & 3) * 1024);
        }
    }
    const unsigned eo = lane == 1 ? WS_HFLAG + (R * 16 + cb) * 128 : !kvcu || lane == 0 || lane > 6 ? WS_EPOCH : lane == 2 ? WS_KVFLAG + (row64 >> 6) * 128 : WS_GZ + row64 * 8 + (lane - 3) * 128;
    const unsigned epoch_v = __hip_atomic_load((gu32*)(ws + eo), RLX_AGENT);
    asm volatile("" ::: "memory");
    const __amdgpu_buffer_rsrc_t rs_kv = mk_rsrc(ws + WS_KV, KV_BYTES), rs_h = mk_rsrc(ws + WS_H, H_BYTES), rs_o = mk_rsrc(ws + WS_O, O_BYTES);
    unsigned o_own = (unsigned)(((row64 >> 5) + rt) * 32 + hg * 4 + hq) * 1024u + lane * 16;
    u32x4 omark = {~0u, ~0u, ~0u, ~0u};
    const __amdgpu_buffer_rsrc_t rs_kf = mk_rsrc(ws + WS_KVFLAG, 4096);
    const unsigned tr_in = 32 * g1 + 8 * p4;
    const unsigned wq_rd = (8 * h + q4) * 128 + ((fh ^ (q4 >> 1)) << 6) + tr_in;
    unsigned xo[4];
#pragma unroll
    for (int ks = 0; ks < 4; ++ks) xo[ks] = r * 128 + (((2 * ks + h) ^ ((r >> 1) & 7)) << 4);

    lptr kp = lds + lane * 16;
    const unsigned v_cst = (r & 27) != 16 ? 2064 : 2048;
    lptr vp0 = lds + (r < 16 ? 1024 + h * 256 + r * 16 : v_cst), vp1 = lds + (r < 16 ? 1536 + h * 256 + r * 16 : v_cst);
    const unsigned w_rd0 = w * 2048 + (4 * h + q4) * 128 + ((q4 >> 1) << 6) + tr_in;
    lptr wrA0 = lds + L_WB + w_rd0, wrA1 = lds + L_WB + (w_rd0 ^ 64), wrB0 = lds + w_rd0, wrB1 = lds + (w_rd0 ^ 64);
    lptr q_rd = lds + L_QX + (hq * 2 + rt) * 1024 + lane * 16, qn_rd = lds + L_QN + (hq * 2 + rt) * 128 + r * 4, km_rd = lds + L_KM;
    unsigned h_voff = (unsigned)((row64 >> 5) * 112 + 14 * w) * 1024u + lane * 16;
    unsigned hf_voff = (unsigned)(R * 16 + (lane & 15)) * 128 + 4, g_voff0 = 32768 + (row64 + 32 * rt + r) * 8, g_voff1 = 32768 + (row64 + 8 * w + (lane >> 3)) * 8 + 4;
    int spidx = 0xDDDD;
    f32x16 oacc = {};
    u32x4 cst = (lane & 1) ? (u32x4){} : (u32x4){0x3f803f80u, 0x3f803f80u, 0x3f803f80u, 0x3f803f80u};
    lptr cst_wr = lds + (tid >> 1) * KVT + 2048 + (tid & 1) * 16;
    unsigned hb = (kvcu ? OOB : (unsigned)((R * 4 + 2 * rt) * 112 + cb * 8 + fh * 4) * 1024u) + lane * 16, hb1 = hb + 112 * 1024;
    s16x8 zf = {};
    PIN(oacc); PIN(spidx); PIN(cst); PIN(cst_wr); PIN(hb); PIN(hb1); PIN(zf); PIN(o_own); PIN(omark);
    PIN(kp); PIN(vp0); PIN(vp1); PIN(wrA0); PIN(wrA1); PIN(wrB0); PIN(wrB1); PIN(q_rd); PIN(qn_rd); PIN(km_rd);
    PIN(h_voff); PIN(hf_voff); PIN(g_voff0); PIN(g_voff1);
    f32x16 a00 = {}, a01 = {}, a10 = {}, a11 = {};
    auto kv_wait = [&](u32x4 fv) {
        const unsigned ep = __builtin_amdgcn_readfirstlane(epoch_v) + 1u;
        if (__builtin_expect(!__all(fv[3] == ep), 0)) {
            unsigned spins = 0;
            do { __builtin_amdgcn_s_sleep(6); CFENCE(); fv = bload_sc1(rs_kf, r * 128, 0); } while (!__all(fv[3] == ep) && ++spins < SPIN_CAP);
        }
        CFENCE();
        const float km = row32_max(fmaxf(__uint_as_float(fv[0]), __uint_as_float(fv[2])));
        if (tid == 256) *(LAS float*)(lds + L_KM) = __builtin_amdgcn_sqrtf(km) * 1.0001f;
    };
    auto kv_tile = [&](int t) { glds16x2_sc1(ws + WS_KV + ((t + 8 * bi) & 63) * 2048, lane * 16, lds0 + t * KVT); };
    if (!kvcu) {
        unsigned cur = 0, nxt = L_STAGE;
        if (big) {
            const unsigned xb = rt * 8192, w0o = 16384 + (8 * h + q4) * 256 + (((2 * fh) ^ q4) << 6) + tr_in, w1o = w0o ^ 64;
            BigFrag F[4];
            auto rd = [&](BigFrag& n, lptr sb, int ks) {
                n.x0 = lds_frag(sb + xb + xo[ks]); n.x1 = lds_frag(sb + xb + 4096 + xo[ks]); n.w0 = tr_frag(sb + w0o + ks * 4096, 1024); n.w1 = tr_frag(sb + w1o + ks * 4096, 1024); };
#define AM(e, ...) do { if (ABL & 2048) asm volatile("" :: __VA_ARGS__); else e; SBAR(); } while (0)
#define AR(e, ...) do { if (ABL & 4096) asm volatile("" : __VA_ARGS__); else { e; } SBAR(); } while (0)
            auto step = [&](const BigFrag& c, BigFrag& n, lptr sb, int ks) {
                AM(a00 = MFMA(c.w0, c.x0, a00), "v"(c.w0), "v"(c.x0)); AR(n.x0 = lds_frag(sb + xb + xo[ks]); n.x1 = lds_frag(sb + xb + 4096 + xo[ks]), "+v"(n.x0), "+v"(n.x1));
                AM(a10 = MFMA(c.w0, c.x1, a10), "v"(c.x1)); AR(n.w0 = tr_frag(sb + w0o + ks * 4096, 1024), "+v"(n.w0));
                AM(a01 = MFMA(c.w1, c.x0, a01), "v"(c.w1)); AR(n.w1 = tr_frag(sb + w1o + ks * 4096, 1024), "+v"(n.w1));
                AM(a11 = MFMA(c.w1, c.x1, a11), "v"(c.w1)); };
            auto stage = [&](auto bar) {
                step(F[0], F[3], lds + cur, 3);
                bar();
                step(F[1], F[0], lds + nxt, 0); step(F[2], F[1], lds + nxt, 1); step(F[3], F[2], lds + nxt, 2);
                cur = nxt; nxt = (nxt == 2 * L_STAGE) ? 0 : nxt + L_STAGE; };
            STAMP(13);
            BAR_VL(6);
            STAMP(14);
            rd(F[0], lds, 0); rd(F[1], lds, 1); rd(F[2], lds, 2);
#pragma unroll 1
            for (int s = 0; s < 7; ++s) stage([&] {
                BAR_VL(1);
                if (s == 3 && w < 2) touch8k(w == 0 ? (const char*)w2 + (R * 14 + cb) * 8192 : (const char*)wo + (b & 63) * 8192, lane, lds0 + L_TOUCH); });
            STAMP(1);
            rd(F[3], lds + cur, 3);
#pragma unroll
            for (int ks = 0; ks < 4; ++ks) { const BigFrag& c = F[ks]; a00 = MFMA(c.w0, c.x0, a00); a10 = MFMA(c.w0, c.x1, a10); a01 = MFMA(c.w1, c.x0, a01); a11 = MFMA(c.w1, c.x1, a11); }
        } else {
            const int j = w - 4;
            __builtin_amdgcn_s_setprio(2);
            const unsigned xb = (2 * rh64 + rt) * 4096, qo = 32768 + wq_rd;
            SmallFrag F[4];
            auto rd = [&](SmallFrag& n, lptr sb, int ks) { n.x = lds_frag(sb + xb + xo[ks]); n.q = tr_frag(sb + qo + ks * 2048, 512); SBAR(); };
            auto mma = [&](const SmallFrag& c) { a00 = MFMA(c.q, c.x, a00); SBAR(); };
            const unsigned xv2 = xv + 2 * (8192 - 1024), xv3 = xv1 + 2 * (8192 - 1024), wv2 = wv0 + 2 * (4 * DFF * 2 - 1024), wv3 = wv1 + 2 * (4 * DFF * 2 - 1024);
            const char* xs = (const char*)x + R * 131072 + j * 32768;
            const char* w1s = (const char*)w1 + (size_t)(j * 16) * (DFF * 2);
            const char* wqs = (const char*)wq + j * 16384;
            auto fill0 = [&](int sr, unsigned slot) { glds16x4(xs + sr * 128, xv, xv1, xv2, xv3, lds0 + slot + j * 4096); };
            auto fill1 = [&](int sr, unsigned slot) { glds16x4(w1s + sr * (64 * DFF * 2), wv0, wv1, wv2, wv3, lds0 + slot + 16384 + j * 4096); };
            auto fill2 = [&](int sr, unsigned slot) { glds16x2(wqs + sr * 65536, w64_src, w64_src1, lds0 + slot + 32768 + j * 2048); };
            BAR_VL(6);
            rd(F[0], lds, 0); rd(F[1], lds, 1); rd(F[2], lds, 2);
            fill0((rot + 2) & 7, 2 * L_STAGE); fill1((rot + 2) & 7, 2 * L_STAGE); fill2((rot + 2) & 7, 2 * L_STAGE);
#pragma unroll 1
            for (int s = 0; s < 6; ++s) {
                const bool more = s < 5;
                const int sr = (rot + s + 3) & 7;
                rd(F[3], lds + cur, 3); mma(F[0]);
                BAR_VL(10);
                if (more) fill0(sr, cur);
                rd(F[0], lds + nxt, 0); mma(F[1]);
                if (more) fill1(sr, cur);
                rd(F[1], lds + nxt, 1); mma(F[2]);
                if (more) fill2(sr, cur);
                rd(F[2], lds + nxt, 2); mma(F[3]);
                cur = nxt; nxt = (nxt == 2 * L_STAGE) ? 0 : nxt + L_STAGE;
            }
            const u32x4 fv = bload_sc1(rs_kf, r * 128, 0);
            rd(F[3], lds, 3); mma(F[0]);
            BAR_VL(0);
            rd(F[0], lds + L_STAGE, 0); mma(F[1]); rd(F[1], lds + L_STAGE, 1); mma(F[2]); rd(F[2], lds + L_STAGE, 2); mma(F[3]); rd(F[3], lds + L_STAGE, 3);
            STAMP(15);
            kv_wait(fv);
            STAMP(16);
            kv_tile(j); kv_tile(4 + j); kv_tile(8 + j); kv_tile(12 + j);
            mma(F[0]); mma(F[1]); mma(F[2]); mma(F[3]);
        }
    } else {
        const unsigned xb = rt * 4096, w64_src2 = w64_src + 2 * (8192 - 1024), w64_src3 = w64_src + 3 * (8192 - 1024);
        {
            const unsigned wkv_rd = 8192 + g1 * 2048 + (((8 * h + q4) * 32 + 8 * p4) ^ (g1 << 7));
#pragma unroll
            for (int g = 0; g < 2; ++g) {
                if (g == 0) { if (big) BAR_VL(5); else BAR_VL(9); } else BAR_VL(1);
                const lptr st = lds + (g * 4 + hq) * L_KSTAGE;
#pragma unroll
                for (int k = 0; k < 4; ++k) a00 = MFMA(tr_frag2(st + wkv_rd + k * 512, st + (wkv_rd ^ 128) + k * 512), lds_frag(st + xb + xo[k]), a00);
            }
        }
        BAR_L();
        {
            const lptr pw = lds + w * L_KSTAGE + 8192 + r * 128;
            const unsigned sz = (r & 7) ^ ((r >> 1) & 4);
#pragma unroll
            for (int i = 0; i < 4; ++i) *(LAS f32x4*)(pw + (((2 * i + h) ^ sz) << 4)) = (f32x4){a00[4 * i], a00[4 * i + 1], a00[4 * i + 2], a00[4 * i + 3]};
            a00 = (f32x16){};
        }
        BAR_L();
        if (big) {
            STAMP(1);
            const unsigned kvo = (unsigned)((row64 >> 5) + rt) * 2048u + lane * 16;
            const lptr pr = lds + rt * L_KSTAGE + 8192;
            gu32* const fl = (gu32*)(ws + WS_KVFLAG) + (row64 >> 6) * 32;
            float n2 = 0.f;
            if (fh == 0) {
                const unsigned sz = (r & 7) ^ ((r >> 1) & 4);
                f32x4 lo = {}, hi = {};
#pragma unroll
                for (int i = 0; i < 4; ++i) { lo += *(const LAS f32x4*)(pr + i * 2 * L_KSTAGE + r * 128 + (((4 + h) ^ sz) << 4)); hi += *(const LAS f32x4*)(pr + i * 2 * L_KSTAGE + r * 128 + (((6 + h) ^ sz) << 4)); }
                const u32x4 v = {cvtpk(lo[0], lo[1]), cvtpk(lo[2], lo[3]), cvtpk(hi[0], hi[1]), cvtpk(hi[2], hi[3])};
                bstore_sc1(v, rs_kv, kvo);
                n2 = row32_max(swap_add(dot2(v[0], v[0], dot2(v[1], v[1], 0.f)) + dot2(v[2], v[2], dot2(v[3], v[3], 0.f))));
                if (tid == 64) __hip_atomic_store(fl, __float_as_uint(n2), RLX_AGENT);
            } else {
                float t[8];
#pragma unroll
                for (int j = 0; j < 8; ++j) {
                    const lptr q = pr + ((lane >> 4) * 8 + j) * 128 + ((q4 ^ j ^ (g1 << 2)) << 4) + p4 * 4;
                    t[j] = (*(const LAS float*)q + *(const LAS float*)(q + 2 * L_KSTAGE)) + (*(const LAS float*)(q + 4 * L_KSTAGE) + *(const LAS float*)(q + 6 * L_KSTAGE));
                }
                bstore_sc1((u32x4){cvtpk(t[0], t[1]), cvtpk(t[2], t[3]), cvtpk(t[4], t[5]), cvtpk(t[6], t[7])}, rs_kv, kvo + 1024);
            }
            STAMP(13);
            BAR_VL(0);
            STAMP(14);
            if (tid == 0) __hip_atomic_store((gu64*)(fl + 2), ((unsigned long long)(epoch_v + 1u) << 32) | __float_as_uint(n2), RLX_AGENT);
            u32x4 gf[16];
#pragma unroll
            for (int i = 0; i < 16; ++i) gf[i] = *(const u32x4*)(gw + i * 32 + (lane >> 4) * 8);
            BAR_L();
            f32x4 z = {};
            const int zr = 16 * w + (lane & 15);
#pragma unroll
            for (int i = 0; i < 16; ++i)
                z = __builtin_amdgcn_mfma_f32_16x16x32_bf16(asfrag(gf[i]), lds_frag(lds + (i >> 1) * L_KSTAGE + zr * 128 + (((4 * (i & 1) + (lane >> 4)) ^ ((zr >> 1) & 7)) << 4)), z, 0, 0, 0);
            const float gt = rbf(__builtin_amdgcn_rcpf(1.f + __expf(-rbf(z[0])))), og = fmaxf(rbf(1.f - gt), 0x1p-40f);
            if (lane < 16) __hip_atomic_store((gu64*)(ws + WS_GZ) + row64 + zr, ((unsigned long long)__float_as_uint(og) << 32) | __float_as_uint(gt / og), RLX_AGENT);
            BAR_L();
        } else {
#pragma unroll
            for (int i = 0; i < 4; ++i) { const int p = i * 16 + (w - 4) * 4; glds16x4((const char*)wq + p * 8192, w64_src, w64_src1, w64_src2, w64_src3, lds0 + L_KWQ + p * 1024); }
            BAR_L();
            BAR_VL(0);
            SmallFrag F[4];
            auto rd = [&](SmallFrag& n, int s, int k) { n.x = lds_frag(lds + s * L_KSTAGE + xb + xo[k]); n.q = tr_frag(lds + L_KWQ + s * 8192 + wq_rd + k * 2048, 512); SBAR(); };
#pragma unroll
            for (int k = 0; k < 4; ++k) rd(F[k], 0, k);
#pragma unroll 1
            for (int s = 1; s < 8; ++s) {
#pragma unroll
                for (int k = 0; k < 4; ++k) { a00 = MFMA(F[k].q, F[k].x, a00); SBAR(); rd(F[k], s, k); }
            }
            const u32x4 fv = bload_sc1(rs_kf, r * 128, 0);
#pragma unroll
            for (int k = 0; k < 4; ++k) a00 = MFMA(F[k].q, F[k].x, a00);
            BAR_L();
            STAMP(15);
            kv_wait(fv);
            STAMP(16);
            kv_tile(w - 4); kv_tile(w); kv_tile(4 + w); kv_tile(8 + w);
        }
    }
    STAMP(2);
    u32x4 hs[8];
    if (big) {
        hs[0] = pack8_relu<0>(a00); hs[1] = pack8_relu<1>(a00); hs[2] = pack8_relu<0>(a01); hs[3] = pack8_relu<1>(a01);
        hs[4] = pack8_relu<0>(a10); hs[5] = pack8_relu<1>(a10); hs[6] = pack8_relu<0>(a11); hs[7] = pack8_relu<1>(a11);
        if (tid < 32) lds_put(cst_wr, cst);
    } else {
#pragma unroll
        for (int s = 0; s < 2; ++s) {
            const u32x4 v = s ? pack8<1>(a00, QSCALE) : pack8<0>(a00, QSCALE);
            lds_put(lds + L_QX + (4 * fh + 2 * s + rt) * 1024 + lane * 16, v);
            const float n2 = swap_add(dot2(v[0], v[0], dot2(v[1], v[1], 0.f)) + dot2(v[2], v[2], dot2(v[3], v[3], 0.f)));
            *(LAS float*)(lds + L_QN + (4 * fh + 2 * s + rt) * 128 + r * 4) = __builtin_amdgcn_sqrtf(n2);
        }
#pragma unroll
        for (int i = 0; i < 8; ++i) asm volatile("" : "=v"(hs[i]));
    }
    __builtin_amdgcn_s_setprio(3);
    BAR_VL(0);
    STAMP(3);
    const s16x8 qf = lds_frag(q_rd);
    s16x8 kf = lds_frag(kp), vf0, vf1;
    float mshift = *(const LAS float*)qn_rd * *(const LAS float*)km_rd;
    vf0 = lds_frag(vp0);
    if (__builtin_expect(!__all(mshift <= 0.5f * SAFE_RANGE), 0)) {
        float m = -__builtin_inff();
#pragma unroll 1
        for (int t = 0; t < 64; ++t) {
            const f32x16 s = MFMA(asfrag(bload_sc1(rs_kv, lane * 16, t * 2048)), qf, (f32x16){});
#pragma unroll
            for (int i = 0; i < 16; ++i) m = fmaxf(m, s[i]);
        }
        mshift = swap_max(m);
    }
    f32x16 cinit;
#pragma unroll
    for (int i = 0; i < 16; ++i) cinit[i] = -mshift;
    PIN(cinit);
    f32x16 sA = MFMA(kf, qf, cinit), sB;
    kf = lds_frag(kp + KVT);
    if (tid >= 32 && tid < 128) lds_put(cst_wr, cst);

#define S1(S_, i) S_[i] = __builtin_amdgcn_exp2f(S_[i])
#define EX2(S_, i) do { S1(S_, i); S1(S_, (i) + 1); } while (0)
#define EX3(S_, i) do { S1(S_, i); S1(S_, (i) + 1); S1(S_, (i) + 2); } while (0)
#define EX4(S_, i) do { EX2(S_, i); EX2(S_, (i) + 2); } while (0)
#define PV1(SN, T) do { if ((T) >= 0) oacc = pv_mma<1>(vf1, SN, oacc, spidx); vf1 = lds_frag(vp1 + ((T) < 0 ? 0 : (T)) * KVT); SBAR(); } while (0)
#define QK(SN, T) do { SN = MFMA(kf, qf, cinit); kf = lds_frag(kp + ((T) + 2) * KVT); SBAR(); } while (0)
#define PV0(SC, T) do { oacc = pv_mma<0>(vf0, SC, oacc, spidx); vf0 = lds_frag(vp0 + ((T) + 1) * KVT); SBAR(); } while (0)
#define CUT(S_) do { PIN(S_); SBAR(); } while (0)
#define ITER0(SC, SN, T, T1, PR, MEM) do { PR; \
    EX2(SC, 0); CUT(SC); PV1(SN, T1); EX4(SC, 2); CUT(SC); QK(SN, T); EX4(SC, 6); S1(SC, 10); CUT(SC); PV0(SC, T); MEM; S1(SC, 11); EX4(SC, 12); CUT(SC); } while (0)
#define ITER1(SC, SN, T, PR, PRE, MEM, G1) do { PR; PRE; \
    EX2(SC, 0); CUT(SC); PV1(SN, T); EX4(SC, 2); CUT(SC); QK(SN, T); EX4(SC, 6); CUT(SC); PV0(SC, T); MEM; EX4(SC, 10); CUT(SC); G1; SBAR(); EX2(SC, 14); CUT(SC); } while (0)
#define ITERG(SC, SN, T, PR, MEM, G0, G1) do { PR; \
    EX2(SC, 0); CUT(SC); PV1(SN, T); EX3(SC, 2); CUT(SC); QK(SN, T); EX3(SC, 5); CUT(SC); G0; SBAR(); EX3(SC, 8); CUT(SC); PV0(SC, T); MEM; EX3(SC, 11); CUT(SC); G1; SBAR(); EX2(SC, 14); CUT(SC); } while (0)
#define NOP (void)0
#define PRIO(p) __builtin_amdgcn_s_setprio(p)
#define ITER2(T, P0, P1, M0, M1) do { ITER0(sA, sB, T, T, PRIO(P0), M0); ITER0(sB, sA, (T) + 1, (T) + 1, PRIO(P1), M1); } while (0)
#define ADV(n) do { kp += (n) * KVT; vp0 += (n) * KVT; vp1 += (n) * KVT; PIN(kp); PIN(vp0); PIN(vp1); } while (0)
    const char* const w2w = (const char*)w2 + w * (14 * 16384);
    const char* const wow = (const char*)wo + w * 65536;
    int pn = (row64 >> 6) % 7;
    const unsigned wbA = lds0 + L_WB + w * 2048, wbB = lds0 + w * 2048;
    const __amdgpu_buffer_rsrc_t rs_f = mk_rsrc(ws + WS_HFLAG, 32768 + 16384);
    u32x4 hA0, hA1, hB0, hB1;
    s16x8 wf0, wf1;
#define W_LD(src, buf) glds16x2(src, w64_src, w64_src1, buf)
    STAMP(4);
#define H_ST(i) do { if (big) bstore_sc1(hs[i], rs_h, ((i) < 4 ? hb : hb1) + ((i) & 3) * 1024); } while (0)
    ITER0(sA, sB, 0, -1, PRIO(3), H_ST(0)); ITER0(sB, sA, 1, 1, NOP, H_ST(1); kv_tile(16 + w));
    ITER2(2, 3, 3, H_ST(2); kv_tile(24 + w), H_ST(3); kv_tile(32 + w));
    ITER2(4, 2, 2, H_ST(4); kv_tile(40 + w), H_ST(5); kv_tile(48 + w));
    ITER2(6, 2, 2, H_ST(6); kv_tile(56 + w), H_ST(7); W_LD(w2w + pn * 32768, wbA));
    STAMP(17);
    ITER2(8, 1, 1, bstore_sc1(omark, rs_o, o_own), NOP);
    ITER2(10, 1, 1, NOP, NOP);
    ITER2(12, 0, 0, NOP, NOP);
    STAMP(18);
    STAMP(19);
    BAR_VL(0);
    unsigned epoch_w = epoch_v; PIN(epoch_w);
    const unsigned epoch = __builtin_amdgcn_readfirstlane(epoch_w) + 1u;
    if (tid == 0) __hip_atomic_store((gu32*)(ws + WS_HFLAG) + (R * 16 + cb) * 32 + 1, epoch, RLX_AGENT);
    STAMP(5);
    unsigned hfv;
    ITER2(14, 1, 3, W_LD(w2w + pn * 32768 + 16384, wbB); a00 = MFMA(zf, zf, (f32x16){}); PIN(zf); a01 = MFMA(zf, zf, (f32x16){}), PIN(zf); a10 = MFMA(zf, zf, (f32x16){}); PIN(zf); a11 = MFMA(zf, zf, (f32x16){}));
    ITER2(16, 2, 1, NOP, hfv = __builtin_amdgcn_raw_buffer_load_b32(rs_f, hf_voff, 0, 16));
    ITER0(sA, sB, 18, 18, PRIO(3), NOP);
    STAMP(6);
    if (__builtin_expect(!__all(hfv == epoch), 0)) {
        unsigned spins = 0;
        do { __builtin_amdgcn_s_sleep(6); CFENCE(); hfv = __builtin_amdgcn_raw_buffer_load_b32(rs_f, hf_voff, 0, 16); } while (!__all(hfv == epoch) && ++spins < SPIN_CAP);
    }
    CFENCE();
    STAMP(7);
    unsigned gr_u, og_u;
    ITER0(sB, sA, 19, 19, PRIO(2), gr_u = __builtin_amdgcn_raw_buffer_load_b32(rs_f, g_voff0, 0, 16); og_u = __builtin_amdgcn_raw_buffer_load_b32(rs_f, g_voff1, 0, 16);
          hA0 = bload_sc1(rs_h, h_voff, pn * 2048); hA1 = bload_sc1(rs_h, h_voff, pn * 2048 + 112 * 1024));
    ITER2(20, 1, 3, hB0 = bload_sc1(rs_h, h_voff, pn * 2048 + 1024), hB1 = bload_sc1(rs_h, h_voff, pn * 2048 + 113 * 1024));
    ADV(22);
    PIN(a00); PIN(a01); PIN(a10); PIN(a11);
    STAMP(8);
#define GM(acc, wf, H) acc = MFMA(wf, asfrag(H), acc); SBAR()
#define GSTEP(SX, SY, T, R0, R1, H0, H1, LW, LH0, LH1) do { \
    ITER1(SX, SY, T, PRIO(3), WAIT_V(5); wf0 = tr_frag(R0, 1024); wf1 = tr_frag(R1, 1024), NOP, GM(a00, wf0, H0)); \
    ITERG(SY, SX, (T) + 1, PRIO(2), LW, GM(a01, wf1, H0); LH0, GM(a10, wf0, H1)); \
    ITER1(SX, SY, (T) + 2, PRIO(1), NOP, NOP, GM(a11, wf1, H1); LH1); } while (0)
#pragma unroll 1
    for (int n = 0; n < 5; ++n) {
        pn = pn == 6 ? 0 : pn + 1;
        const char* wsrc = w2w + pn * 32768;
        GSTEP(sA, sB, 0, wrA0, wrA1, hA0, hA1, W_LD(wsrc, wbA), hA0 = bload_sc1(rs_h, h_voff, pn * 2048), hA1 = bload_sc1(rs_h, h_voff, pn * 2048 + 112 * 1024));
        GSTEP(sB, sA, 3, wrB0, wrB1, hB0, hB1, W_LD(wsrc + 16384, wbB), hB0 = bload_sc1(rs_h, h_voff, pn * 2048 + 1024), hB1 = bload_sc1(rs_h, h_voff, pn * 2048 + 113 * 1024));
        ADV(6);
    }
    BAR_L();
    pn = pn == 6 ? 0 : pn + 1;
    const unsigned xtra = w < 3 ? 6 * 16384 : L_QX - 3 * 2048;
    u32x4 hC0, hC1, hD0, hD1;
    ITER2(0, 3, 2, W_LD(w2w + pn * 32768, wbB + 16384); hC0 = bload_sc1(rs_h, h_voff, pn * 2048), W_LD(w2w + pn * 32768 + 16384, wbB + 32768); hC1 = bload_sc1(rs_h, h_voff, pn * 2048 + 112 * 1024));
    ITER2(2, 1, 3, W_LD(wow, wbB + 49152); hD0 = bload_sc1(rs_h, h_voff, pn * 2048 + 1024), W_LD(wow + 16384, wbB + 65536); hD1 = bload_sc1(rs_h, h_voff, pn * 2048 + 113 * 1024));
    ITER2(4, 2, 1, W_LD(wow + 32768, wbB + 81920), W_LD(wow + 49152, wbB + xtra));
    const bool closer = b == 112 && w == 7;
    unsigned ef[4];
    auto ef_load = [&]() {
#pragma unroll
        for (int i = 0; i < 4; ++i) ef[i] = __builtin_amdgcn_raw_buffer_load_b32(mk_rsrc(ws + WS_HFLAG + i * 8192 + 4, closer ? 8188 : 0), lane * 128, 0, 16);
    };
    ITER2(6, 3, 3, ef_load(), NOP);
    ITER2(8, 2, 2, NOP, NOP);
    ITER2(10, 1, 0, NOP, NOP);
    STAMP(9);
#define GMA(acc, wf, H) asm volatile("v_mfma_f32_32x32x16_bf16 %0, %1, %2, %0" : "+v"(acc) : "v"(wf), "v"(H))
    PRIO(3);
    oacc = pv_mma<1>(vf1, sB, oacc, spidx);
    {
        u32x4 v = pack8<0>(oacc, __builtin_amdgcn_rcpf(oacc[8]) * __uint_as_float(gr_u));
#pragma unroll
        for (int e = 0; e < 4; ++e) v[e] = v[e] < 0xfffffffeu ? v[e] : 0xfffffffeu;
        bstore_sc1(v, rs_o, o_own);
        asm volatile("s_nop 1");
    }
    PRIO(0);
    WAIT_V(1);
    wf0 = tr_frag(wrA0, 1024); wf1 = tr_frag(wrA1, 1024);
    const s16x8 wf2 = tr_frag(wrB0, 1024), wf3 = tr_frag(wrB1, 1024);
    const s16x8 wf4 = tr_frag(wrB0 + 16384, 1024), wf5 = tr_frag(wrB1 + 16384, 1024), wf6 = tr_frag(wrB0 + 32768, 1024), wf7 = tr_frag(wrB1 + 32768, 1024);
    SBAR();
    GMA(a00, wf0, hA0); GMA(a01, wf1, hA0);
    s16x8 wof[8];
    wof[6] = tr_frag(wrB0 + xtra, 1024); wof[7] = tr_frag(wrB1 + xtra, 1024);
    wrB0 += 49152; wrB1 += 49152; PIN(wrB0); PIN(wrB1);
#pragma unroll
    for (int i = 0; i < 3; ++i) { wof[2 * i] = tr_frag(wrB0 + i * 16384, 1024); wof[2 * i + 1] = tr_frag(wrB1 + i * 16384, 1024); }
    SBAR();
    GMA(a10, wf0, hA1); GMA(a11, wf1, hA1);
    GMA(a00, wf2, hB0); GMA(a01, wf3, hB0); GMA(a10, wf2, hB1); GMA(a11, wf3, hB1);
    const unsigned ob = (unsigned)((row64 >> 5) * 32 + 4 * w) * 1024u;
    u32x4 of[8];
    auto o_load = [&]() {
#pragma unroll
        for (int i = 0; i < 8; ++i) of[i] = bload_sc1(rs_o, lane * 16 + (i & 3) * 1024, ob + (i >> 2) * 32768);
    };
    auto o_ready = [&]() {
        unsigned m = 0;
#pragma unroll
        for (int i = 0; i < 8; ++i) { m = max(m, max(of[i][0], of[i][1])); m = max(m, max(of[i][2], of[i][3])); }
        return __all(m != ~0u);
    };
    BAR_VL(0);
    STAMP(10);
    o_load();
    SBAR();
    GMA(a00, wf4, hC0); GMA(a01, wf5, hC0); GMA(a10, wf4, hC1); GMA(a11, wf5, hC1);
    GMA(a00, wf6, hD0); GMA(a01, wf7, hD0); GMA(a10, wf6, hD1); GMA(a11, wf7, hD1);
    SBAR();
    lptr pw[4], pr[4];
#pragma unroll
    for (int i = 0; i < 4; ++i) {
        pw[i] = lds + w * 16384 + r * 256 + (((2 * i + h) ^ (r & 7)) << 4);
        pr[i] = lds + (i >> 1) * 65536 + (8 * w + (lane >> 3)) * 256 + ((((2 * (lane & 7)) ^ ((lane >> 3) & 7)) << 4) ^ ((i & 1) << 4));
        PIN(pw[i]); PIN(pr[i]);
    }
    uint16_t* outp = out + (row64 + 8 * w + (lane >> 3)) * 512 + hg * 64 + (lane & 7) * 8;
    PIN(outp);
    SBAR();
    if (!o_ready()) {
        unsigned spins = 0;
        do { CFENCE(); o_load(); } while (!o_ready() && ++spins < SPIN_CAP);
    }
    STAMP(11);
#define AMM(acc, wi, oi) GMA(acc, wof[wi], of[oi])
#define PUT(a, i, off) *(LAS f32x4*)(pw[i] + (off)) = (f32x4){a[4 * (i)], a[4 * (i) + 1], a[4 * (i) + 2], a[4 * (i) + 3]}
#define MM_PUT(acc, c, o, prev, off) do { AMM(acc, c, o); asm volatile("s_nop 11" : "+v"(prev)); PUT(prev, 0, off); SBAR(); AMM(acc, 2 + (c), (o) + 1); PUT(prev, 1, off); SBAR(); \
    AMM(acc, 4 + (c), (o) + 2); PUT(prev, 2, off); SBAR(); AMM(acc, 6 + (c), (o) + 3); PUT(prev, 3, off); SBAR(); } while (0)
    AMM(a00, 0, 0); AMM(a00, 2, 1); AMM(a00, 4, 2); AMM(a00, 6, 3);
    MM_PUT(a01, 1, 0, a00, 0);
    MM_PUT(a10, 0, 4, a01, 128);
    MM_PUT(a11, 1, 4, a10, 8192);
    asm volatile("s_nop 11" : "+v"(a11));
    PUT(a11, 0, 8320); PUT(a11, 1, 8320); PUT(a11, 2, 8320); PUT(a11, 3, 8320);
    BAR_L();
    {
        f32x4 t[16];
#pragma unroll
        for (int k = 0; k < 8; ++k) { t[2 * k] = *(const LAS f32x4*)(pr[(k >> 2) * 2] + (k & 3) * 16384); t[2 * k + 1] = *(const LAS f32x4*)(pr[(k >> 2) * 2 + 1] + (k & 3) * 16384); }
        SBAR();
        f32x4 lo = t[0], hi = t[1];
#pragma unroll
        for (int k = 1; k < 8; ++k) { lo += t[2 * k]; hi += t[2 * k + 1]; }
        lo *= __uint_as_float(og_u); hi *= __uint_as_float(og_u);
        const u32x4 v = {cvtpk(lo[0], lo[1]), cvtpk(lo[2], lo[3]), cvtpk(hi[0], hi[1]), cvtpk(hi[2], hi[3])};
        __builtin_nontemporal_store(v, (GAS u32x4*)outp);
    }
    if (closer) {
        unsigned spins = 0;
        while (!__all(ef[0] == epoch && ef[1] == epoch && ef[2] == epoch && ef[3] == epoch) && ++spins < SPIN_CAP) { CFENCE(); ef_load(); }
        if (lane == 0) __hip_atomic_store((gu32*)(ws + WS_EPOCH), epoch, RLX_AGENT);
    }
    STAMP(12);
}

extern "C" void kernel_launch(void* const* d_in, const int* in_sizes, int n_in, void* d_out, int out_size, void* d_ws, size_t ws_size,
                              hipStream_t stream) {
    fused_kernel<<<256, 512, 0, stream>>>((const uint16_t*)d_in[0], (const uint16_t*)d_in[1], (const uint16_t*)d_in[2], (const uint16_t*)d_in[3],
                                          (const uint16_t*)d_in[4], (const uint16_t*)d_in[6], (const uint16_t*)d_in[7], (const uint16_t*)d_in[8],
                                          (uint16_t*)d_out, (char*)d_ws);
}
```

```cpp
#include <hip/hip_runtime.h>
#include <stdint.h>

typedef short s16x8 __attribute__((ext_vector_type(8)));
typedef short s16x4 __attribute__((ext_vector_type(4)));
typedef short s16x2 __attribute__((ext_vector_type(2)));
typedef float f32x16 __attribute__((ext_vector_type(16)));
typedef float f32x4 __attribute__((ext_vector_type(4)));
typedef float f32x2 __attribute__((ext_vector_type(2)));
typedef unsigned u32x4 __attribute__((ext_vector_type(4)));
typedef __bf16 bf16x2 __attribute__((ext_vector_type(2)));
typedef __bf16 bf16x8 __attribute__((ext_vector_type(8)));
typedef __bf16 bf16x16 __attribute__((ext_vector_type(16)));
#define LAS __attribute__((address_space(3)))
#define GAS __attribute__((address_space(1)))
typedef GAS unsigned gu32;
typedef GAS unsigned long long gu64;
typedef LAS char* lptr;
#define DI __device__ __forceinline__
#define RLX_AGENT __ATOMIC_RELAXED, __HIP_MEMORY_SCOPE_AGENT
#define MFMA(a, b, c) __builtin_amdgcn_mfma_f32_32x32x16_bf16((a), (b), (c), 0, 0, 0)
#define SBAR() __builtin_amdgcn_sched_barrier(0)
#define PIN(x) asm volatile("" : "+v"(x))
#define CFENCE() do { asm volatile("" ::: "memory"); __builtin_amdgcn_fence(__ATOMIC_ACQUIRE, "wavefront"); } while (0)
#define BAR_L() asm volatile("s_waitcnt lgkmcnt(0)\n\ts_barrier" ::: "memory")
#define BAR_VL(N) asm volatile("s_waitcnt vmcnt(" #N ") lgkmcnt(0)\n\ts_barrier" ::: "memory")

#define ABL 0
#define STW 0
#define STAMPS 0

constexpr int D = 512, DFF = 1792;
constexpr float QSCALE = 0.25f * 1.4426950408889634f;
constexpr float SAFE_RANGE = 64.f;
constexpr unsigned WS_EPOCH = 0, WS_KVFLAG = 4096, WS_HFLAG = 8192;
constexpr unsigned WS_GZ = WS_HFLAG + 32768;
constexpr unsigned WS_KV = WS_GZ + 16384, KV_BYTES = 64 * 2048;
constexpr unsigned WS_H = WS_KV + KV_BYTES, H_BYTES = 64 * 112 * 1024;
constexpr unsigned WS_O = WS_H + H_BYTES, O_BYTES = 64 * 32 * 1024;
constexpr unsigned WS_STAMP = WS_O + O_BYTES;
constexpr unsigned OOB = 0x7ff00000u;
constexpr unsigned L_STAGE = 40960;
constexpr unsigned L_KSTAGE = 12288, L_KWQ = 98304;
constexpr unsigned KVT = 2080;
constexpr unsigned L_WB = 64 * KVT;
constexpr unsigned L_QX = L_WB + 16384, L_QN = L_QX + 8192, L_KM = L_QN + 1024, L_TOUCH = L_KM + 256, LDS_BYTES = 163840;
static_assert(L_TOUCH + 256 <= LDS_BYTES, "lds map");
constexpr unsigned SPIN_CAP = 1u << 16;

DI unsigned cvtpk(float a, float b) { f32x2 v = {a, b}; return __builtin_bit_cast(unsigned, __builtin_convertvector(v, bf16x2)); }
DI float rbf(float x) { return __uint_as_float(cvtpk(x, x) << 16); }
DI s16x8 asfrag(u32x4 v) { return __builtin_bit_cast(s16x8, v); }
DI s16x8 lds_frag(lptr p) { return *(const LAS s16x8*)p; }
DI void lds_put(lptr p, u32x4 v) { *(LAS u32x4*)p = v; }
DI s16x4 lds_tr(lptr p) { return __builtin_amdgcn_ds_read_tr16_b64_v4i16((LAS s16x4*)p); }
DI s16x8 tr_frag2(lptr p, lptr q) { s16x4 lo = lds_tr(p), hi = lds_tr(q); return __builtin_shufflevector(lo, hi, 0, 1, 2, 3, 4, 5, 6, 7); }
DI s16x8 tr_frag(lptr p, int second) { return tr_frag2(p, p + second); }
DI float swap_add(float v) { auto r = __builtin_amdgcn_permlane32_swap(__float_as_uint(v), __float_as_uint(v), false, false); return __uint_as_float(r[0]) + __uint_as_float(r[1]); }
DI float swap_max(float v) { auto r = __builtin_amdgcn_permlane32_swap(__float_as_uint(v), __float_as_uint(v), false, false); return fmaxf(__uint_as_float(r[0]), __uint_as_float(r[1])); }
DI float dot2(unsigned a, unsigned b, float c) { return __builtin_amdgcn_fdot2_f32_bf16(__builtin_bit_cast(bf16x2, a), __builtin_bit_cast(bf16x2, b), c, false); }
DI unsigned relu_pk(unsigned u) { return __builtin_bit_cast(unsigned, __builtin_elementwise_max(__builtin_bit_cast(s16x2, u), (s16x2){0, 0})); }
template <int CTRL> DI float dpp(float v) { return __uint_as_float(__builtin_amdgcn_update_dpp(0, __float_as_uint(v), CTRL, 0xf, 0xf, true)); }
DI float row32_max(float v) {
    v = fmaxf(v, dpp<0xB1>(v)); v = fmaxf(v, dpp<0x4E>(v)); v = fmaxf(v, dpp<0x141>(v)); v = fmaxf(v, dpp<0x140>(v));
    auto r = __builtin_amdgcn_permlane16_swap(__float_as_uint(v), __float_as_uint(v), false, false);
    return fmaxf(__uint_as_float(r[0]), __uint_as_float(r[1]));
}
template <int HALF> DI u32x4 pack8(const f32x16& a, float m) {
    u32x4 v;
#pragma unroll
    for (int e = 0; e < 4; ++e) v[e] = cvtpk(a[8 * HALF + 2 * e] * m, a[8 * HALF + 2 * e + 1] * m);
    return v;
}
template <int HALF> DI u32x4 pack8_relu(const f32x16& a) {
    u32x4 v;
#pragma unroll
    for (int e = 0; e < 4; ++e) v[e] = relu_pk(cvtpk(a[8 * HALF + 2 * e], a[8 * HALF + 2 * e + 1]));
    return v;
}
template <int HALF> DI f32x16 pv_mma(s16x8 v, const f32x16& p, f32x16 c, int idx) {
    const auto ph = __builtin_shufflevector(p, p, 8 * HALF, 8 * HALF + 1, 8 * HALF + 2, 8 * HALF + 3, 8 * HALF + 4, 8 * HALF + 5, 8 * HALF + 6, 8 * HALF + 7);
    return __builtin_amdgcn_smfmac_f32_32x32x32_bf16(__builtin_bit_cast(bf16x8, v), __builtin_bit_cast(bf16x16, ph), c, idx, 0, 0);
}
DI void glds16(const void* sbase, unsigned voff, unsigned lds_base) {
    asm volatile("s_mov_b32 m0, %2\n\ts_nop 0\n\tglobal_load_lds_dwordx4 %0, %1" :: "v"(voff), "s"(sbase), "s"(lds_base) : "memory");
}
DI void glds16x2(const void* sbase, unsigned voff0, unsigned voff1, unsigned lds_base) {
    asm volatile("s_mov_b32 m0, %3\n\ts_nop 0\n\tglobal_load_lds_dwordx4 %0, %2\n\tglobal_load_lds_dwordx4 %1, %2 offset:1024" :: "v"(voff0), "v"(voff1), "s"(sbase), "s"(lds_base) : "memory");
}
DI void glds16x4(const void* sbase, unsigned v0, unsigned v1, unsigned v2, unsigned v3, unsigned lds_base) {
    asm volatile("s_mov_b32 m0, %5\n\ts_nop 0\n\tglobal_load_lds_dwordx4 %0, %4\n\tglobal_load_lds_dwordx4 %1, %4 offset:1024\n\t"
                 "global_load_lds_dwordx4 %2, %4 offset:2048\n\tglobal_load_lds_dwordx4 %3, %4 offset:3072" :: "v"(v0), "v"(v1), "v"(v2), "v"(v3), "s"(sbase), "s"(lds_base) : "memory");
}
DI void glds16x2_sc1(const void* sbase, unsigned voff, unsigned lds_base) {
    asm volatile("s_mov_b32 m0, %2\n\ts_nop 0\n\tglobal_load_lds_dwordx4 %0, %1 sc1\n\tglobal_load_lds_dwordx4 %0, %1 offset:1024 sc1" :: "v"(voff), "s"(sbase), "s"(lds_base) : "memory");
}
DI void touch8k(const void* sbase, int lane, unsigned lds_scratch) {
    asm volatile("s_mov_b32 m0, %2\n\ts_nop 0\n\tglobal_load_lds_dword %0, %1" :: "v"(lane * 128), "s"(sbase), "s"(lds_scratch) : "memory");
}
#define WAIT_V(N) asm volatile("s_waitcnt vmcnt(" #N ")" ::: "memory")
DI __amdgpu_buffer_rsrc_t mk_rsrc(const void* p, unsigned bytes) { return __builtin_amdgcn_make_buffer_rsrc((void*)p, 0, bytes, 0x00020000); }
DI u32x4 bload(__amdgpu_buffer_rsrc_t r, unsigned voff, unsigned soff) { return __builtin_amdgcn_raw_buffer_load_b128(r, voff, soff, 0); }
DI u32x4 bload_sc1(__amdgpu_buffer_rsrc_t r, unsigned voff, unsigned soff) { return __builtin_amdgcn_raw_buffer_load_b128(r, voff, soff, 16); }
DI void bstore_sc1(u32x4 v, __amdgpu_buffer_rsrc_t r, unsigned voff) { __builtin_amdgcn_raw_buffer_store_b128(v, r, voff, 0, 16); }

#define STAMP(i) do { } while (0)

struct BigFrag { s16x8 x0, x1, w0, w1; };
struct SmallFrag { s16x8 x, q; };

__global__ void __launch_bounds__(512, 2)
fused_kernel(const uint16_t* __restrict__ x, const uint16_t* __restrict__ wq, const uint16_t* __restrict__ wk, const uint16_t* __restrict__ wv,
             const uint16_t* __restrict__ wo, const uint16_t* __restrict__ w1, const uint16_t* __restrict__ w2, const uint16_t* __restrict__ gw,
             uint16_t* out, char* ws) {
    __shared__ __attribute__((aligned(16))) char smem[LDS_BYTES];
    const lptr lds = (lptr)smem;
    const unsigned lds0 = (unsigned)(uintptr_t)smem;
    const int tid = threadIdx.x, lane = tid & 63, w = __builtin_amdgcn_readfirstlane(tid >> 6);
    const int r = lane & 31, h = lane >> 5, g1 = (lane >> 4) & 1, q4 = (lane & 15) >> 2, p4 = lane & 3;
    const int b = blockIdx.x, bi = b >> 3;
    const int R = 2 * (b & 7) + (bi >> 4), cb = bi & 15, rh64 = cb & 1, hg = cb >> 1;
    const bool kvcu = cb >= 14, big = w < 4;
    const int row64 = R * 128 + rh64 * 64;
    const int rt = w & 1, hq = w >> 1, fh = hq & 1;
    unsigned stv = 0;
    STAMP(0);

    asm volatile("" :: "s"(x), "s"(wq), "s"(wk), "s"(wv), "s"(wo), "s"(w1), "s"(w2), "s"(gw), "s"(out), "s"(ws));
    const unsigned w64_src = (unsigned)((lane >> 3) * 1024 + hg * 128 + (((lane & 7) ^ (g1 << 2)) << 4));
    const unsigned w64_src1 = w64_src + 8192 - 1024;
    const unsigned xv = (unsigned)((lane >> 3) * 1024 + (((lane & 7) ^ ((lane >> 4) & 3)) << 4)), xv1 = (xv ^ 64) + 8192 - 1024;
    const unsigned wv0 = (unsigned)((lane >> 4) * (DFF * 2) + cb * 256 + (((lane & 15) ^ (((lane >> 4) & 3) << 2)) << 4)), wv1 = wv0 + 4 * DFF * 2 - 1024;
    const int rot = (b + cb) & 7;
    if (!kvcu) {
#pragma unroll
        for (int s = 0; s < 2; ++s) {
            const int sr = (s + rot) & 7;
            glds16x2((const char*)x + R * 131072 + sr * 128 + w * 16384, xv, xv1, lds0 + s * L_STAGE + w * 2048);
            glds16x2((const char*)w1 + (size_t)(sr * 64 + w * 8) * (DFF * 2), wv0, wv1, lds0 + s * L_STAGE + 16384 + w * 2048);
            glds16((const char*)wq + sr * 65536 + w * 8192, w64_src, lds0 + s * L_STAGE + 32768 + w * 1024);
        }
    } else {
        const char* wkv = (const char*)((w & 2) ? wk : wv);
        const unsigned kvv = (w & 1) * 1024 + ((lane ^ ((w & 2) << 2)) << 4);
#pragma unroll 1
        for (int s = 0; s < 8; ++s) {
            glds16((const char*)x + (row64 + 8 * w) * 1024 + s * 128, xv ^ (rt << 6), lds0 + s * L_KSTAGE + w * 1024);
            if (!big) glds16(wkv + s * 2048, kvv, lds0 + s * L_KSTAGE + 8192 + (w & 3) * 1024);
        }
    }
    const unsigned eo = lane == 1 ? WS_HFLAG + (R * 16 + cb) * 128 : !kvcu || lane == 0 || lane > 6 ? WS_EPOCH : lane == 2 ? WS_KVFLAG + (row64 >> 6) * 128 : WS_GZ + row64 * 8 + (lane - 3) * 128;
    const unsigned epoch_v = __hip_atomic_load((gu32*)(ws + eo), RLX_AGENT);
    asm volatile("" ::: "memory");
    const __amdgpu_buffer_rsrc_t rs_kv = mk_rsrc(ws + WS_KV, KV_BYTES), rs_h = mk_rsrc(ws + WS_H, H_BYTES), rs_o = mk_rsrc(ws + WS_O, O_BYTES);
    const __amdgpu_buffer_rsrc_t rs_kf = mk_rsrc(ws + WS_KVFLAG, 4096);
    const unsigned tr_in = 32 * g1 + 8 * p4;
    const unsigned wq_rd = (8 * h + q4) * 128 + ((fh ^ (q4 >> 1)) << 6) + tr_in;
    unsigned xo[4];
#pragma unroll
    for (int ks = 0; ks < 4; ++ks) xo[ks] = r * 128 + (((2 * ks + h) ^ ((r >> 1) & 7)) << 4);

    lptr kp, vp0, vp1, wrA0, wrA1, wrB0, wrB1, q_rd, qn_rd, km_rd, cst_wr;
    unsigned h_voff, hf_voff, g_voff0, g_voff1, hb, hb1, o_own;
    int spidx;
    f32x16 oacc;
    u32x4 cst, omark;
    s16x8 zf;
    auto consts = [&]() {
        kp = lds + lane * 16;
        const unsigned v_cst = (r & 27) != 16 ? 2064 : 2048;
        vp0 = lds + (r < 16 ? 1024 + h * 256 + r * 16 : v_cst); vp1 = lds + (r < 16 ? 1536 + h * 256 + r * 16 : v_cst);
        const unsigned w_rd0 = w * 2048 + (4 * h + q4) * 128 + ((q4 >> 1) << 6) + tr_in;
        wrA0 = lds + L_WB + w_rd0; wrA1 = lds + L_WB + (w_rd0 ^ 64); wrB0 = lds + w_rd0; wrB1 = lds + (w_rd0 ^ 64);
        q_rd = lds + L_QX + (hq * 2 + rt) * 1024 + lane * 16; qn_rd = lds + L_QN + (hq * 2 + rt) * 128 + r * 4; km_rd = lds + L_KM;
        h_voff = (unsigned)((row64 >> 5) * 112 + 14 * w) * 1024u + lane * 16;
        hf_voff = (unsigned)(R * 16 + (lane & 15)) * 128 + 4; g_voff0 = 32768 + (row64 + 32 * rt + r) * 8; g_voff1 = 32768 + (row64 + 8 * w + ((lane >> 1) & 4) + (lane >> 4)) * 8 + 4;
        spidx = 0xDDDD; oacc = (f32x16){}; zf = (s16x8){};
        cst = (lane & 1) ? (u32x4){} : (u32x4){0x3f803f80u, 0x3f803f80u, 0x3f803f80u, 0x3f803f80u};
        cst_wr = lds + (tid >> 1) * KVT + 2048 + (tid & 1) * 16;
        hb = (kvcu ? OOB : (unsigned)((R * 4 + 2 * rt) * 112 + cb * 8 + fh * 4) * 1024u) + lane * 16; hb1 = hb + 112 * 1024;
        o_own = (unsigned)(((row64 >> 5) + rt) * 32 + hg * 4 + hq) * 1024u + lane * 16;
        omark = (u32x4){~0u, ~0u, ~0u, ~0u};
        PIN(oacc); PIN(spidx); PIN(cst); PIN(cst_wr); PIN(hb); PIN(hb1); PIN(zf); PIN(o_own); PIN(omark);
        PIN(kp); PIN(vp0); PIN(vp1); PIN(wrA0); PIN(wrA1); PIN(wrB0); PIN(wrB1); PIN(q_rd); PIN(qn_rd); PIN(km_rd);
        PIN(h_voff); PIN(hf_voff); PIN(g_voff0); PIN(g_voff1);
        SBAR();
    };
    f32x16 a00 = {}, a01 = {}, a10 = {}, a11 = {};
    auto kv_wait = [&](u32x4 fv) {
        const unsigned ep = __builtin_amdgcn_readfirstlane(epoch_v) + 1u;
        if (__builtin_expect(!__all(fv[1] == ep && fv[3] == ep), 0)) {
            unsigned spins = 0;
            do { __builtin_amdgcn_s_sleep(6); CFENCE(); fv = bload_sc1(rs_kf, r * 128 + h * 16, 0); } while (!__all(fv[1] == ep && fv[3] == ep) && ++spins < SPIN_CAP);
        }
        CFENCE();
        const float km = row32_max(fmaxf(__uint_as_float(fv[0]), __uint_as_float(fv[2])));
        if (tid == 256) *(LAS float*)(lds + L_KM) = __builtin_amdgcn_sqrtf(km) * 1.0001f;
    };
    auto kv_tile = [&](int t) { glds16x2_sc1(ws + WS_KV + ((t + 8 * bi) & 63) * 2048, lane * 16, lds0 + t * KVT); };
    if (!kvcu) {
        unsigned cur = 0, nxt = L_STAGE;
        if (big) {
            const unsigned xb = rt * 8192, w0o = 16384 + (8 * h + q4) * 256 + (((2 * fh) ^ q4) << 6) + tr_in, w1o = w0o ^ 64;
            BigFrag F[4];
            auto rd = [&](BigFrag& n, lptr sb, int ks) {
                n.x0 = lds_frag(sb + xb + xo[ks]); n.x1 = lds_frag(sb + xb + 4096 + xo[ks]); n.w0 = tr_frag(sb + w0o + ks * 4096, 1024); n.w1 = tr_frag(sb + w1o + ks * 4096, 1024); };
#define AM(e, ...) do { if (ABL & 2048) asm volatile("" :: __VA_ARGS__); else e; SBAR(); } while (0)
#define AR(e, ...) do { if (ABL & 4096) asm volatile("" : __VA_ARGS__); else { e; } SBAR(); } while (0)
            auto step = [&](const BigFrag& c, BigFrag& n, lptr sb, int ks) {
                AM(a00 = MFMA(c.w0, c.x0, a00), "v"(c.w0), "v"(c.x0)); AR(n.x0 = lds_frag(sb + xb + xo[ks]); n.x1 = lds_frag(sb + xb + 4096 + xo[ks]), "+v"(n.x0), "+v"(n.x1));
                AM(a10 = MFMA(c.w0, c.x1, a10), "v"(c.x1)); AR(n.w0 = tr_frag(sb + w0o + ks * 4096, 1024), "+v"(n.w0));
                AM(a01 = MFMA(c.w1, c.x0, a01), "v"(c.w1)); AR(n.w1 = tr_frag(sb + w1o + ks * 4096, 1024), "+v"(n.w1));
                AM(a11 = MFMA(c.w1, c.x1, a11), "v"(c.w1)); };
            auto stage = [&](auto bar) {
                step(F[0], F[3], lds + cur, 3);
                bar();
                step(F[1], F[0], lds + nxt, 0); step(F[2], F[1], lds + nxt, 1); step(F[3], F[2], lds + nxt, 2);
                cur = nxt; nxt = (nxt == 2 * L_STAGE) ? 0 : nxt + L_STAGE; };
            consts();
            STAMP(13);
            BAR_VL(6);
            STAMP(14);
            rd(F[0], lds, 0); rd(F[1], lds, 1); rd(F[2], lds, 2);
#pragma unroll 1
            for (int s = 0; s < 7; ++s) stage([&] {
                BAR_VL(1);
                if (s == 3 && w < 2) touch8k(w == 0 ? (const char*)w2 + (R * 14 + cb) * 8192 : (const char*)wo + (b & 63) * 8192, lane, lds0 + L_TOUCH); });
            STAMP(1);
            rd(F[3], lds + cur, 3);
#pragma unroll
            for (int ks = 0; ks < 4; ++ks) { const BigFrag& c = F[ks]; a00 = MFMA(c.w0, c.x0, a00); a10 = MFMA(c.w0, c.x1, a10); a01 = MFMA(c.w1, c.x0, a01); a11 = MFMA(c.w1, c.x1, a11); }
        } else {
            const int j = w - 4;
            __builtin_amdgcn_s_setprio(2);
            const unsigned xb = (2 * rh64 + rt) * 4096, qo = 32768 + wq_rd;
            SmallFrag F[4];
            auto rd = [&](SmallFrag& n, lptr sb, int ks) { n.x = lds_frag(sb + xb + xo[ks]); n.q = tr_frag(sb + qo + ks * 2048, 512); SBAR(); };
            auto mma = [&](const SmallFrag& c) { a00 = MFMA(c.q, c.x, a00); SBAR(); };
            const unsigned xv2 = xv + 2 * (8192 - 1024), xv3 = xv1 + 2 * (8192 - 1024), wv2 = wv0 + 2 * (4 * DFF * 2 - 1024), wv3 = wv1 + 2 * (4 * DFF * 2 - 1024);
            const char* xs = (const char*)x + R * 131072 + j * 32768;
            const char* w1s = (const char*)w1 + (size_t)(j * 16) * (DFF * 2);
            const char* wqs = (const char*)wq + j * 16384;
            auto fill0 = [&](int sr, unsigned slot) { glds16x4(xs + sr * 128, xv, xv1, xv2, xv3, lds0 + slot + j * 4096); };
            auto fill1 = [&](int sr, unsigned slot) { glds16x4(w1s + sr * (64 * DFF * 2), wv0, wv1, wv2, wv3, lds0 + slot + 16384 + j * 4096); };
            auto fill2 = [&](int sr, unsigned slot) { glds16x2(wqs + sr * 65536, w64_src, w64_src1, lds0 + slot + 32768 + j * 2048); };
            fill0((rot + 2) & 7, 2 * L_STAGE); fill1((rot + 2) & 7, 2 * L_STAGE); fill2((rot + 2) & 7, 2 * L_STAGE);
            consts();
            BAR_VL(16);
            rd(F[0], lds, 0); rd(F[1], lds, 1); rd(F[2], lds, 2);
#pragma unroll 1
            for (int s = 0; s < 6; ++s) {
                const bool more = s < 5;
                const int sr = (rot + s + 3) & 7;
                rd(F[3], lds + cur, 3); mma(F[0]);
                BAR_VL(10);
                if (more) fill0(sr, cur);
                rd(F[0], lds + nxt, 0); mma(F[1]);
                if (more) fill1(sr, cur);
                rd(F[1], lds + nxt, 1); mma(F[2]);
                if (more) fill2(sr, cur);
                rd(F[2], lds + nxt, 2); mma(F[3]);
                cur = nxt; nxt = (nxt == 2 * L_STAGE) ? 0 : nxt + L_STAGE;
            }
            const u32x4 fv = bload_sc1(rs_kf, r * 128 + h * 16, 0);
            rd(F[3], lds, 3); mma(F[0]);
            BAR_VL(0);
            rd(F[0], lds + L_STAGE, 0); mma(F[1]); rd(F[1], lds + L_STAGE, 1); mma(F[2]); rd(F[2], lds + L_STAGE, 2); mma(F[3]); rd(F[3], lds + L_STAGE, 3);
            STAMP(15);
            kv_wait(fv);
            STAMP(16);
            kv_tile(j); kv_tile(4 + j); kv_tile(8 + j); kv_tile(12 + j);
            mma(F[0]); mma(F[1]); mma(F[2]); mma(F[3]);
        }
    } else {
        const unsigned xb = rt * 4096, w64_src2 = w64_src + 2 * (8192 - 1024), w64_src3 = w64_src + 3 * (8192 - 1024);
        {
            const unsigned wkv_rd = 8192 + g1 * 2048 + (((8 * h + q4) * 32 + 8 * p4) ^ (g1 << 7));
#pragma unroll
            for (int g = 0; g < 2; ++g) {
                if (g == 0) { if (big) BAR_VL(5); else BAR_VL(9); } else BAR_VL(1);
                const lptr st = lds + (g * 4 + hq) * L_KSTAGE;
#pragma unroll
                for (int k = 0; k < 4; ++k) a00 = MFMA(tr_frag2(st + wkv_rd + k * 512, st + (wkv_rd ^ 128) + k * 512), lds_frag(st + xb + xo[k]), a00);
            }
        }
        BAR_L();
        {
            const lptr pw = lds + w * L_KSTAGE + 8192 + r * 128;
            const unsigned sz = (r & 7) ^ ((r >> 1) & 4);
#pragma unroll
            for (int i = 0; i < 4; ++i) *(LAS f32x4*)(pw + (((2 * i + h) ^ sz) << 4)) = (f32x4){a00[4 * i], a00[4 * i + 1], a00[4 * i + 2], a00[4 * i + 3]};
            a00 = (f32x16){};
        }
        BAR_L();
        if (big) {
            STAMP(1);
            const unsigned kvo = (unsigned)((row64 >> 5) + rt) * 2048u + lane * 16;
            const lptr pr = lds + rt * L_KSTAGE + 8192;
            float n2 = 0.f;
            if (fh == 0) {
                const unsigned sz = (r & 7) ^ ((r >> 1) & 4);
                f32x4 lo = {}, hi = {};
#pragma unroll
                for (int i = 0; i < 4; ++i) { lo += *(const LAS f32x4*)(pr + i * 2 * L_KSTAGE + r * 128 + (((4 + h) ^ sz) << 4)); hi += *(const LAS f32x4*)(pr + i * 2 * L_KSTAGE + r * 128 + (((6 + h) ^ sz) << 4)); }
                const u32x4 v = {cvtpk(lo[0], lo[1]), cvtpk(lo[2], lo[3]), cvtpk(hi[0], hi[1]), cvtpk(hi[2], hi[3])};
                bstore_sc1(v, rs_kv, kvo);
                n2 = row32_max(swap_add(dot2(v[0], v[0], dot2(v[1], v[1], 0.f)) + dot2(v[2], v[2], dot2(v[3], v[3], 0.f))));
            } else {
                float t[8];
#pragma unroll
                for (int j = 0; j < 8; ++j) {
                    const lptr q = pr + ((lane >> 4) * 8 + j) * 128 + ((q4 ^ j ^ (g1 << 2)) << 4) + p4 * 4;
                    t[j] = (*(const LAS float*)q + *(const LAS float*)(q + 2 * L_KSTAGE)) + (*(const LAS float*)(q + 4 * L_KSTAGE) + *(const LAS float*)(q + 6 * L_KSTAGE));
                }
                bstore_sc1((u32x4){cvtpk(t[0], t[1]), cvtpk(t[2], t[3]), cvtpk(t[4], t[5]), cvtpk(t[6], t[7])}, rs_kv, kvo + 1024);
            }
            STAMP(13);
            WAIT_V(0);
            STAMP(14);
            if (lane == 0) __hip_atomic_store((gu64*)(ws + WS_KVFLAG) + (row64 >> 6) * 16 + w, ((unsigned long long)(epoch_v + 1u) << 32) | __float_as_uint(n2), RLX_AGENT);
            u32x4 gf[16];
#pragma unroll
            for (int i = 0; i < 16; ++i) gf[i] = *(const u32x4*)(gw + i * 32 + (lane >> 4) * 8);
            consts();
            BAR_L();
            f32x4 z = {};
            const int zr = 16 * w + (lane & 15);
#pragma unroll
            for (int i = 0; i < 16; ++i)
                z = __builtin_amdgcn_mfma_f32_16x16x32_bf16(asfrag(gf[i]), lds_frag(lds + (i >> 1) * L_KSTAGE + zr * 128 + (((4 * (i & 1) + (lane >> 4)) ^ ((zr >> 1) & 7)) << 4)), z, 0, 0, 0);
            const float gt = rbf(__builtin_amdgcn_rcpf(1.f + __expf(-rbf(z[0])))), og = fmaxf(rbf(1.f - gt), 0x1p-40f);
            if (lane < 16) __hip_atomic_store((gu64*)(ws + WS_GZ) + row64 + zr, ((unsigned long long)__float_as_uint(og) << 32) | __float_as_uint(gt / og), RLX_AGENT);
            BAR_L();
        } else {
#pragma unroll
            for (int i = 0; i < 4; ++i) { const int p = i * 16 + (w - 4) * 4; glds16x4((const char*)wq + p * 8192, w64_src, w64_src1, w64_src2, w64_src3, lds0 + L_KWQ + p * 1024); }
            consts();
            BAR_VL(0);
            SmallFrag F[4];
            auto rd = [&](SmallFrag& n, int s, int k) { n.x = lds_frag(lds + s * L_KSTAGE + xb + xo[k]); n.q = tr_frag(lds + L_KWQ + s * 8192 + wq_rd + k * 2048, 512); SBAR(); };
#pragma unroll
            for (int k = 0; k < 4; ++k) rd(F[k], 0, k);
#pragma unroll 1
            for (int s = 1; s < 8; ++s) {
#pragma unroll
                for (int k = 0; k < 4; ++k) { a00 = MFMA(F[k].q, F[k].x, a00); SBAR(); rd(F[k], s, k); }
            }
            const u32x4 fv = bload_sc1(rs_kf, r * 128 + h * 16, 0);
#pragma unroll
            for (int k = 0; k < 4; ++k) a00 = MFMA(F[k].q, F[k].x, a00);
            BAR_L();
            STAMP(15);
            kv_wait(fv);
            STAMP(16);
            kv_tile(w - 4); kv_tile(w); kv_tile(4 + w); kv_tile(8 + w);
        }
    }
    STAMP(2);
    u32x4 hs[8];
    if (big) {
        hs[0] = pack8_relu<0>(a00); hs[1] = pack8_relu<1>(a00); hs[2] = pack8_relu<0>(a01); hs[3] = pack8_relu<1>(a01);
        hs[4] = pack8_relu<0>(a10); hs[5] = pack8_relu<1>(a10); hs[6] = pack8_relu<0>(a11); hs[7] = pack8_relu<1>(a11);
        if (tid < 32) lds_put(cst_wr, cst);
    } else {
#pragma unroll
        for (int s = 0; s < 2; ++s) {
            const u32x4 v = s ? pack8<1>(a00, QSCALE) : pack8<0>(a00, QSCALE);
            lds_put(lds + L_QX + (4 * fh + 2 * s + rt) * 1024 + lane * 16, v);
            const float n2 = swap_add(dot2(v[0], v[0], dot2(v[1], v[1], 0.f)) + dot2(v[2], v[2], dot2(v[3], v[3], 0.f)));
            *(LAS float*)(lds + L_QN + (4 * fh + 2 * s + rt) * 128 + r * 4) = __builtin_amdgcn_sqrtf(n2);
        }
#pragma unroll
        for (int i = 0; i < 8; ++i) asm volatile("" : "=v"(hs[i]));
    }
    __builtin_amdgcn_s_setprio(3);
    BAR_VL(0);
    STAMP(3);
    const s16x8 qf = lds_frag(q_rd);
    s16x8 kf = lds_frag(kp), vf0, vf1;
    float mshift = *(const LAS float*)qn_rd * *(const LAS float*)km_rd;
    vf0 = lds_frag(vp0);
    if (__builtin_expect(!__all(mshift <= 0.5f * SAFE_RANGE), 0)) {
        float m = -__builtin_inff();
#pragma unroll 1
        for (int t = 0; t < 64; ++t) {
            const f32x16 s = MFMA(asfrag(bload_sc1(rs_kv, lane * 16, t * 2048)), qf, (f32x16){});
#pragma unroll
            for (int i = 0; i < 16; ++i) m = fmaxf(m, s[i]);
        }
        mshift = swap_max(m);
    }
    f32x16 cinit;
#pragma unroll
    for (int i = 0; i < 16; ++i) cinit[i] = -mshift;
    PIN(cinit);
    f32x16 sA = MFMA(kf, qf, cinit), sB;
    kf = lds_frag(kp + KVT);
    if (tid >= 32 && tid < 128) lds_put(cst_wr, cst);

#define S1(S_, i) S_[i] = __builtin_amdgcn_exp2f(S_[i])
#define EX2(S_, i) do { S1(S_, i); S1(S_, (i) + 1); } while (0)
#define EX3(S_, i) do { S1(S_, i); S1(S_, (i) + 1); S1(S_, (i) + 2); } while (0)
#define EX4(S_, i) do { EX2(S_, i); EX2(S_, (i) + 2); } while (0)
#define PV1(SN, T) do { if ((T) >= 0) oacc = pv_mma<1>(vf1, SN, oacc, spidx); vf1 = lds_frag(vp1 + ((T) < 0 ? 0 : (T)) * KVT); SBAR(); } while (0)
#define QK(SN, T) do { SN = MFMA(kf, qf, cinit); kf = lds_frag(kp + ((T) + 2) * KVT); SBAR(); } while (0)
#define PV0(SC, T) do { oacc = pv_mma<0>(vf0, SC, oacc, spidx); vf0 = lds_frag(vp0 + ((T) + 1) * KVT); SBAR(); } while (0)
#define CUT(S_) do { PIN(S_); SBAR(); } while (0)
#define ITER0(SC, SN, T, T1, PR, MEM) do { PR; \
    EX2(SC, 0); CUT(SC); PV1(SN, T1); EX4(SC, 2); CUT(SC); QK(SN, T); EX4(SC, 6); S1(SC, 10); CUT(SC); PV0(SC, T); MEM; S1(SC, 11); EX4(SC, 12); CUT(SC); } while (0)
#define ITER1(SC, SN, T, PR, PRE, MEM, G1) do { PR; PRE; \
    EX2(SC, 0); CUT(SC); PV1(SN, T); EX4(SC, 2); CUT(SC); QK(SN, T); EX4(SC, 6); CUT(SC); PV0(SC, T); MEM; EX4(SC, 10); CUT(SC); G1; SBAR(); EX2(SC, 14); CUT(SC); } while (0)
#define ITERG(SC, SN, T, PR, MEM, G0, G1) do { PR; \
    EX2(SC, 0); CUT(SC); PV1(SN, T); EX3(SC, 2); CUT(SC); QK(SN, T); EX3(SC, 5); CUT(SC); G0; SBAR(); EX3(SC, 8); CUT(SC); PV0(SC, T); MEM; EX3(SC, 11); CUT(SC); G1; SBAR(); EX2(SC, 14); CUT(SC); } while (0)
#define NOP (void)0
#define PRIO(p) __builtin_amdgcn_s_setprio(p)
#define ITER2(T, P0, P1, M0, M1) do { ITER0(sA, sB, T, T, PRIO(P0), M0); ITER0(sB, sA, (T) + 1, (T) + 1, PRIO(P1), M1); } while (0)
#define ADV(n) do { kp += (n) * KVT; vp0 += (n) * KVT; vp1 += (n) * KVT; PIN(kp); PIN(vp0); PIN(vp1); } while (0)
    const char* const w2w = (const char*)w2 + w * (14 * 16384);
    const char* const wow = (const char*)wo + w * 65536;
    int pn = (row64 >> 6) % 7;
    const unsigned wbA = lds0 + L_WB + w * 2048, wbB = lds0 + w * 2048;
    const __amdgpu_buffer_rsrc_t rs_f = mk_rsrc(ws + WS_HFLAG, 32768 + 16384);
    u32x4 hA0, hA1, hB0, hB1;
    s16x8 wf0, wf1;
#define W_LD(src, buf) glds16x2(src, w64_src, w64_src1, buf)
    STAMP(4);
#define H_ST(i) do { if (big) bstore_sc1(hs[i], rs_h, ((i) < 4 ? hb : hb1) + ((i) & 3) * 1024); } while (0)
    ITER0(sA, sB, 0, -1, PRIO(3), H_ST(0)); ITER0(sB, sA, 1, 1, NOP, H_ST(1); kv_tile(16 + w));
    ITER2(2, 3, 3, H_ST(2); kv_tile(24 + w), H_ST(3); kv_tile(32 + w));
    ITER2(4, 2, 2, H_ST(4); kv_tile(40 + w), H_ST(5); kv_tile(48 + w));
    ITER2(6, 2, 2, H_ST(6); kv_tile(56 + w), H_ST(7); W_LD(w2w + pn * 32768, wbA));
    STAMP(17);
    ITER2(8, 1, 1, bstore_sc1(omark, rs_o, o_own), NOP);
    ITER2(10, 1, 1, NOP, NOP);
    ITER2(12, 0, 0, NOP, NOP);
    STAMP(18);
    STAMP(19);
    BAR_VL(0);
    unsigned epoch_w = epoch_v; PIN(epoch_w);
    const unsigned epoch = __builtin_amdgcn_readfirstlane(epoch_w) + 1u;
    if (tid == 0) __hip_atomic_store((gu32*)(ws + WS_HFLAG) + (R * 16 + cb) * 32 + 1, epoch, RLX_AGENT);
    STAMP(5);
    unsigned hfv;
    ITER2(14, 1, 3, W_LD(w2w + pn * 32768 + 16384, wbB); a00 = MFMA(zf, zf, (f32x16){}); PIN(zf); a01 = MFMA(zf, zf, (f32x16){}), PIN(zf); a10 = MFMA(zf, zf, (f32x16){}); PIN(zf); a11 = MFMA(zf, zf, (f32x16){}));
    ITER2(16, 2, 1, NOP, hfv = __builtin_amdgcn_raw_buffer_load_b32(rs_f, hf_voff, 0, 16));
    ITER0(sA, sB, 18, 18, PRIO(3), NOP);
    STAMP(6);
    if (__builtin_expect(!__all(hfv == epoch), 0)) {
        unsigned spins = 0;
        do { __builtin_amdgcn_s_sleep(6); CFENCE(); hfv = __builtin_amdgcn_raw_buffer_load_b32(rs_f, hf_voff, 0, 16); } while (!__all(hfv == epoch) && ++spins < SPIN_CAP);
    }
    CFENCE();
    STAMP(7);
    unsigned gr_u, og_u;
    ITER0(sB, sA, 19, 19, PRIO(2), gr_u = __builtin_amdgcn_raw_buffer_load_b32(rs_f, g_voff0, 0, 16); og_u = __builtin_amdgcn_raw_buffer_load_b32(rs_f, g_voff1, 0, 16);
          hA0 = bload_sc1(rs_h, h_voff, pn * 2048); hA1 = bload_sc1(rs_h, h_voff, pn * 2048 + 112 * 1024));
    ITER2(20, 1, 3, hB0 = bload_sc1(rs_h, h_voff, pn * 2048 + 1024), hB1 = bload_sc1(rs_h, h_voff, pn * 2048 + 113 * 1024));
    ADV(22);
    PIN(a00); PIN(a01); PIN(a10); PIN(a11);
    STAMP(8);
#define GM(acc, wf, H) acc = MFMA(asfrag(H), wf, acc); SBAR()
#define GSTEP(SX, SY, T, R0, R1, H0, H1, LW, LH0, LH1) do { \
    ITER1(SX, SY, T, PRIO(3), WAIT_V(5); wf0 = tr_frag(R0, 1024); wf1 = tr_frag(R1, 1024), NOP, GM(a00, wf0, H0)); \
    ITERG(SY, SX, (T) + 1, PRIO(2), LW, GM(a01, wf1, H0); LH0, GM(a10, wf0, H1)); \
    ITER1(SX, SY, (T) + 2, PRIO(1), NOP, NOP, GM(a11, wf1, H1); LH1); } while (0)
#pragma unroll 1
    for (int n = 0; n < 5; ++n) {
        pn = pn == 6 ? 0 : pn + 1;
        const char* wsrc = w2w + pn * 32768;
        GSTEP(sA, sB, 0, wrA0, wrA1, hA0, hA1, W_LD(wsrc, wbA), hA0 = bload_sc1(rs_h, h_voff, pn * 2048), hA1 = bload_sc1(rs_h, h_voff, pn * 2048 + 112 * 1024));
        GSTEP(sB, sA, 3, wrB0, wrB1, hB0, hB1, W_LD(wsrc + 16384, wbB), hB0 = bload_sc1(rs_h, h_voff, pn * 2048 + 1024), hB1 = bload_sc1(rs_h, h_voff, pn * 2048 + 113 * 1024));
        ADV(6);
    }
    BAR_L();
    pn = pn == 6 ? 0 : pn + 1;
    const unsigned xtra = w < 3 ? 6 * 16384 : L_QX - 3 * 2048;
    u32x4 hC0, hC1, hD0, hD1;
    ITER2(0, 3, 2, W_LD(w2w + pn * 32768, wbB + 16384); hC0 = bload_sc1(rs_h, h_voff, pn * 2048), W_LD(w2w + pn * 32768 + 16384, wbB + 32768); hC1 = bload_sc1(rs_h, h_voff, pn * 2048 + 112 * 1024));
    ITER2(2, 1, 3, W_LD(wow, wbB + 49152); hD0 = bload_sc1(rs_h, h_voff, pn * 2048 + 1024), W_LD(wow + 16384, wbB + 65536); hD1 = bload_sc1(rs_h, h_voff, pn * 2048 + 113 * 1024));
    ITER2(4, 2, 1, W_LD(wow + 32768, wbB + 81920), W_LD(wow + 49152, wbB + xtra));
    const bool closer = b == 112 && w == 7;
    unsigned ef[4];
    auto ef_load = [&]() {
#pragma unroll
        for (int i = 0; i < 4; ++i) ef[i] = __builtin_amdgcn_raw_buffer_load_b32(mk_rsrc(ws + WS_HFLAG + i * 8192 + 4, closer ? 8188 : 0), lane * 128, 0, 16);
    };
    ITER2(6, 3, 3, ef_load(), NOP);
    ITER2(8, 2, 2, NOP, NOP);
    ITER2(10, 1, 0, NOP, NOP);
    STAMP(9);
#define GMA(acc, wf, H) asm volatile("v_mfma_f32_32x32x16_bf16 %0, %1, %2, %0" : "+v"(acc) : "v"(H), "v"(wf))
    PRIO(3);
    oacc = pv_mma<1>(vf1, sB, oacc, spidx);
    {
        u32x4 v = pack8<0>(oacc, __builtin_amdgcn_rcpf(oacc[8]) * __uint_as_float(gr_u));
#pragma unroll
        for (int e = 0; e < 4; ++e) v[e] = v[e] < 0xfffffffeu ? v[e] : 0xfffffffeu;
        bstore_sc1(v, rs_o, o_own);
        asm volatile("s_nop 1");
    }
    PRIO(0);
    WAIT_V(1);
    wf0 = tr_frag(wrA0, 1024); wf1 = tr_frag(wrA1, 1024);
    const s16x8 wf2 = tr_frag(wrB0, 1024), wf3 = tr_frag(wrB1, 1024);
    const s16x8 wf4 = tr_frag(wrB0 + 16384, 1024), wf5 = tr_frag(wrB1 + 16384, 1024), wf6 = tr_frag(wrB0 + 32768, 1024), wf7 = tr_frag(wrB1 + 32768, 1024);
    SBAR();
    GMA(a00, wf0, hA0); GMA(a01, wf1, hA0);
    s16x8 wof[8];
    wof[6] = tr_frag(wrB0 + xtra, 1024); wof[7] = tr_frag(wrB1 + xtra, 1024);
    wrB0 += 49152; wrB1 += 49152; PIN(wrB0); PIN(wrB1);
#pragma unroll
    for (int i = 0; i < 3; ++i) { wof[2 * i] = tr_frag(wrB0 + i * 16384, 1024); wof[2 * i + 1] = tr_frag(wrB1 + i * 16384, 1024); }
    SBAR();
    GMA(a10, wf0, hA1); GMA(a11, wf1, hA1);
    GMA(a00, wf2, hB0); GMA(a01, wf3, hB0); GMA(a10, wf2, hB1); GMA(a11, wf3, hB1);
    const unsigned ob = (unsigned)((row64 >> 5) * 32 + 4 * w) * 1024u;
    u32x4 of[8];
    auto o_load = [&]() {
#pragma unroll
        for (int i = 0; i < 8; ++i) of[i] = bload_sc1(rs_o, lane * 16 + (i & 3) * 1024, ob + (i >> 2) * 32768);
    };
    auto o_ready = [&]() {
        unsigned m = 0;
#pragma unroll
        for (int i = 0; i < 8; ++i) { m = max(m, max(of[i][0], of[i][1])); m = max(m, max(of[i][2], of[i][3])); }
        return __all(m != ~0u);
    };
    BAR_VL(0);
#pragma unroll
    for (int i = 0; i < 8; ++i) PIN(wof[i]);
    STAMP(10);
    o_load();
    SBAR();
    GMA(a00, wf4, hC0); GMA(a01, wf5, hC0); GMA(a10, wf4, hC1); GMA(a11, wf5, hC1);
    GMA(a00, wf6, hD0); GMA(a01, wf7, hD0); GMA(a10, wf6, hD1); GMA(a11, wf7, hD1);
    SBAR();
    const lptr prb = lds + (((w >> 2) * 2 + ((lane >> 2) & 1)) * 16 + 4 * (w & 3) + (lane >> 4)) * 2048 + ((lane >> 3) & 1) * 128 + p4 * 32;
    lptr pr0 = prb + g1 * 16, pr1 = prb + (g1 ^ 1) * 16;
    PIN(pr0); PIN(pr1);
    uint16_t* outp = out + (row64 + 8 * w + ((lane >> 1) & 4) + (lane >> 4)) * 512 + hg * 64 + (lane & 7) * 8;
    PIN(outp);
    SBAR();
    if (!o_ready()) {
        unsigned spins = 0;
        do { CFENCE(); o_load(); } while (!o_ready() && ++spins < SPIN_CAP);
    }
    STAMP(11);
#define AMM(acc, wi, oi) GMA(acc, wof[wi], of[oi])
#define PUT(a, i, off) asm volatile("ds_write_addtid_b32 %0 offset:%4\n\tds_write_addtid_b32 %1 offset:%5\n\tds_write_addtid_b32 %2 offset:%6\n\tds_write_addtid_b32 %3 offset:%7" \
    :: "v"(a[4 * (i)]), "v"(a[4 * (i) + 1]), "v"(a[4 * (i) + 2]), "v"(a[4 * (i) + 3]), "n"((off) + (i) * 8192), "n"((off) + (i) * 8192 + 2048), "n"((off) + (i) * 8192 + 4096), "n"((off) + (i) * 8192 + 6144) : "memory")
#define SETM0(v, prev) asm volatile("s_mov_b32 m0, %1\n\ts_nop 10" : "+v"(prev) : "s"(v))
#define MM_PUT(acc, c, o, prev, m0v, off) do { AMM(acc, c, o); SETM0(m0v, prev); PUT(prev, 0, off); AMM(acc, 2 + (c), (o) + 1); PUT(prev, 1, off); \
    AMM(acc, 4 + (c), (o) + 2); PUT(prev, 2, off); AMM(acc, 6 + (c), (o) + 3); PUT(prev, 3, off); } while (0)
    const unsigned m0a = lds0 + w * 256, m0b = m0a + 63616;
    asm volatile("s_nop 1");
    AMM(a00, 0, 0); AMM(a00, 2, 1); AMM(a00, 4, 2); AMM(a00, 6, 3);
    MM_PUT(a01, 1, 0, a00, m0a, 0);
    MM_PUT(a10, 0, 4, a01, m0a, 32768);
    MM_PUT(a11, 1, 4, a10, m0b, 65536 - 63616);
    asm volatile("s_nop 7" : "+v"(a11));
    PUT(a11, 0, 98304 - 63616); PUT(a11, 1, 98304 - 63616); PUT(a11, 2, 98304 - 63616); PUT(a11, 3, 98304 - 63616);
    BAR_L();
    {
        f32x4 t[16];
#pragma unroll
        for (int k = 0; k < 8; ++k) { t[2 * k] = *(const LAS f32x4*)(pr0 + k * 256); t[2 * k + 1] = *(const LAS f32x4*)(pr1 + k * 256); }
        SBAR();
        f32x4 lo = t[0], hi = t[1];
#pragma unroll
        for (int k = 1; k < 8; ++k) { lo += t[2 * k]; hi += t[2 * k + 1]; }
        lo *= __uint_as_float(og_u); hi *= __uint_as_float(og_u);
        const unsigned l0 = cvtpk(lo[0], lo[1]), l1 = cvtpk(lo[2], lo[3]), h0 = cvtpk(hi[0], hi[1]), h1 = cvtpk(hi[2], hi[3]);
        const u32x4 v = g1 ? (u32x4){h0, h1, l0, l1} : (u32x4){l0, l1, h0, h1};
        __builtin_nontemporal_store(v, (GAS u32x4*)outp);
    }
    if (closer) {
        unsigned spins = 0;
        while (!__all(ef[0] == epoch && ef[1] == epoch && ef[2] == epoch && ef[3] == epoch) && ++spins < SPIN_CAP) { CFENCE(); ef_load(); }
        if (lane == 0) __hip_atomic_store((gu32*)(ws + WS_EPOCH), epoch, RLX_AGENT);
    }
    STAMP(12);
}

extern "C" void kernel_launch(void* const* d_in, const int* in_sizes, int n_in, void* d_out, int out_size, void* d_ws, size_t ws_size,
                              hipStream_t stream) {
    fused_kernel<<<256, 512, 0, stream>>>((const uint16_t*)d_in[0], (const uint16_t*)d_in[1], (const uint16_t*)d_in[2], (const uint16_t*)d_in[3],
                                          (const uint16_t*)d_in[4], (const uint16_t*)d_in[6], (const uint16_t*)d_in[7], (const uint16_t*)d_in[8],
                                          (uint16_t*)d_out, (char*)d_ws);
}
```

```cpp
#include <hip/hip_runtime.h>
#include <stdint.h>

typedef short s16x8 __attribute__((ext_vector_type(8)));
typedef short s16x4 __attribute__((ext_vector_type(4)));
typedef short s16x2 __attribute__((ext_vector_type(2)));
typedef float f32x16 __attribute__((ext_vector_type(16)));
typedef float f32x4 __attribute__((ext_vector_type(4)));
typedef float f32x2 __attribute__((ext_vector_type(2)));
typedef unsigned u32x4 __attribute__((ext_vector_type(4)));
typedef __bf16 bf16x2 __attribute__((ext_vector_type(2)));
typedef __bf16 bf16x8 __attribute__((ext_vector_type(8)));
typedef __bf16 bf16x16 __attribute__((ext_vector_type(16)));
#define LAS __attribute__((address_space(3)))
#define GAS __attribute__((address_space(1)))
typedef GAS unsigned gu32;
typedef GAS unsigned long long gu64;
typedef LAS char* lptr;
#define DI __device__ __forceinline__
#define RLX_AGENT __ATOMIC_RELAXED, __HIP_MEMORY_SCOPE_AGENT
#define MFMA(a, b, c) __builtin_amdgcn_mfma_f32_32x32x16_bf16((a), (b), (c), 0, 0, 0)
#define SBAR() __builtin_amdgcn_sched_barrier(0)
#define PIN(x) asm volatile("" : "+v"(x))
#define CFENCE() do { asm volatile("" ::: "memory"); __builtin_amdgcn_fence(__ATOMIC_ACQUIRE, "wavefront"); } while (0)
#define BAR_L() asm volatile("s_waitcnt lgkmcnt(0)\n\ts_barrier" ::: "memory")
#define BAR_VL(N) asm volatile("s_waitcnt vmcnt(" #N ") lgkmcnt(0)\n\ts_barrier" ::: "memory")

constexpr int D = 512, DFF = 1792;
constexpr float QSCALE = 0.25f * 1.4426950408889634f;
constexpr float SAFE_RANGE = 64.f;
constexpr unsigned WS_EPOCH = 0, WS_KVFLAG = 4096, WS_HFLAG = 8192;
constexpr unsigned WS_GZ = WS_HFLAG + 32768;
constexpr unsigned WS_KV = WS_GZ + 16384, KV_BYTES = 64 * 2048;
constexpr unsigned WS_H = WS_KV + KV_BYTES, H_BYTES = 64 * 112 * 1024;
constexpr unsigned WS_O = WS_H + H_BYTES, O_BYTES = 64 * 32 * 1024;
constexpr unsigned OOB = 0x7ff00000u;
constexpr unsigned L_STAGE = 40960;
constexpr unsigned L_KSTAGE = 12288, L_KWQ = 98304;
constexpr unsigned KVT = 2080;
constexpr unsigned L_WB = 64 * KVT;
constexpr unsigned L_QX = L_WB + 16384, L_QN = L_QX + 8192, L_KM = L_QN + 1024, L_TOUCH = L_KM + 256, LDS_BYTES = 163840;
static_assert(L_TOUCH + 256 <= LDS_BYTES, "lds map");
constexpr unsigned SPIN_CAP = 1u << 16;

DI unsigned cvtpk(float a, float b) { f32x2 v = {a, b}; return __builtin_bit_cast(unsigned, __builtin_convertvector(v, bf16x2)); }
DI float rbf(float x) { return __uint_as_float(cvtpk(x, x) << 16); }
DI s16x8 asfrag(u32x4 v) { return __builtin_bit_cast(s16x8, v); }
DI s16x8 lds_frag(lptr p) { return *(const LAS s16x8*)p; }
DI void lds_put(lptr p, u32x4 v) { *(LAS u32x4*)p = v; }
DI s16x4 lds_tr(lptr p) { return __builtin_amdgcn_ds_read_tr16_b64_v4i16((LAS s16x4*)p); }
DI s16x8 tr_frag2(lptr p, lptr q) { s16x4 lo = lds_tr(p), hi = lds_tr(q); return __builtin_shufflevector(lo, hi, 0, 1, 2, 3, 4, 5, 6, 7); }
DI s16x8 tr_frag(lptr p, int second) { return tr_frag2(p, p + second); }
DI float swap_add(float v) { auto r = __builtin_amdgcn_permlane32_swap(__float_as_uint(v), __float_as_uint(v), false, false); return __uint_as_float(r[0]) + __uint_as_float(r[1]); }
DI float swap_max(float v) { auto r = __builtin_amdgcn_permlane32_swap(__float_as_uint(v), __float_as_uint(v), false, false); return fmaxf(__uint_as_float(r[0]), __uint_as_float(r[1])); }
DI float dot2(unsigned a, unsigned b, float c) { return __builtin_amdgcn_fdot2_f32_bf16(__builtin_bit_cast(bf16x2, a), __builtin_bit_cast(bf16x2, b), c, false); }
DI unsigned relu_pk(unsigned u) { return __builtin_bit_cast(unsigned, __builtin_elementwise_max(__builtin_bit_cast(s16x2, u), (s16x2){0, 0})); }
template <int CTRL> DI float dpp(float v) { return __uint_as_float(__builtin_amdgcn_update_dpp(0, __float_as_uint(v), CTRL, 0xf, 0xf, true)); }
DI float row32_max(float v) {
    v = fmaxf(v, dpp<0xB1>(v)); v = fmaxf(v, dpp<0x4E>(v)); v = fmaxf(v, dpp<0x141>(v)); v = fmaxf(v, dpp<0x140>(v));
    auto r = __builtin_amdgcn_permlane16_swap(__float_as_uint(v), __float_as_uint(v), false, false);
    return fmaxf(__uint_as_float(r[0]), __uint_as_float(r[1]));
}
template <int HALF> DI u32x4 pack8(const f32x16& a, float m) {
    u32x4 v;
#pragma unroll
    for (int e = 0; e < 4; ++e) v[e] = cvtpk(a[8 * HALF + 2 * e] * m, a[8 * HALF + 2 * e + 1] * m);
    return v;
}
template <int HALF> DI u32x4 pack8_relu(const f32x16& a) {
    u32x4 v;
#pragma unroll
    for (int e = 0; e < 4; ++e) v[e] = relu_pk(cvtpk(a[8 * HALF + 2 * e], a[8 * HALF + 2 * e + 1]));
    return v;
}
template <int HALF> DI f32x16 pv_mma(s16x8 v, const f32x16& p, f32x16 c, int idx) {
    const auto ph = __builtin_shufflevector(p, p, 8 * HALF, 8 * HALF + 1, 8 * HALF + 2, 8 * HALF + 3, 8 * HALF + 4, 8 * HALF + 5, 8 * HALF + 6, 8 * HALF + 7);
    return __builtin_amdgcn_smfmac_f32_32x32x32_bf16(__builtin_bit_cast(bf16x8, v), __builtin_bit_cast(bf16x16, ph), c, idx, 0, 0);
}
DI void glds16(const void* sbase, unsigned voff, unsigned lds_base) {
    asm volatile("s_mov_b32 m0, %2\n\ts_nop 0\n\tglobal_load_lds_dwordx4 %0, %1" :: "v"(voff), "s"(sbase), "s"(lds_base) : "memory");
}
DI void glds16x2(const void* sbase, unsigned voff0, unsigned voff1, unsigned lds_base) {
    asm volatile("s_mov_b32 m0, %3\n\ts_nop 0\n\tglobal_load_lds_dwordx4 %0, %2\n\tglobal_load_lds_dwordx4 %1, %2 offset:1024" :: "v"(voff0), "v"(voff1), "s"(sbase), "s"(lds_base) : "memory");
}
DI void glds16x4(const void* sbase, unsigned v0, unsigned v1, unsigned v2, unsigned v3, unsigned lds_base) {
    asm volatile("s_mov_b32 m0, %5\n\ts_nop 0\n\tglobal_load_lds_dwordx4 %0, %4\n\tglobal_load_lds_dwordx4 %1, %4 offset:1024\n\t"
                 "global_load_lds_dwordx4 %2, %4 offset:2048\n\tglobal_load_lds_dwordx4 %3, %4 offset:3072" :: "v"(v0), "v"(v1), "v"(v2), "v"(v3), "s"(sbase), "s"(lds_base) : "memory");
}
DI void glds16x2_sc1(const void* sbase, unsigned voff, unsigned lds_base) {
    asm volatile("s_mov_b32 m0, %2\n\ts_nop 0\n\tglobal_load_lds_dwordx4 %0, %1 sc1\n\tglobal_load_lds_dwordx4 %0, %1 offset:1024 sc1" :: "v"(voff), "s"(sbase), "s"(lds_base) : "memory");
}
DI void touch8k(const void* sbase, int lane, unsigned lds_scratch) {
    asm volatile("s_mov_b32 m0, %2\n\ts_nop 0\n\tglobal_load_lds_dword %0, %1" :: "v"(lane * 128), "s"(sbase), "s"(lds_scratch) : "memory");
}
#define WAIT_V(N) asm volatile("s_waitcnt vmcnt(" #N ")" ::: "memory")
DI __amdgpu_buffer_rsrc_t mk_rsrc(const void* p, unsigned bytes) { return __builtin_amdgcn_make_buffer_rsrc((void*)p, 0, bytes, 0x00020000); }
DI u32x4 bload(__amdgpu_buffer_rsrc_t r, unsigned voff, unsigned soff) { return __builtin_amdgcn_raw_buffer_load_b128(r, voff, soff, 0); }
DI u32x4 bload_sc1(__amdgpu_buffer_rsrc_t r, unsigned voff, unsigned soff) { return __builtin_amdgcn_raw_buffer_load_b128(r, voff, soff, 16); }
DI void bstore_sc1(u32x4 v, __amdgpu_buffer_rsrc_t r, unsigned voff) { __builtin_amdgcn_raw_buffer_store_b128(v, r, voff, 0, 16); }

struct BigFrag { s16x8 x0, x1, w0, w1; };
struct SmallFrag { s16x8 x, q; };

__global__ void __launch_bounds__(512, 2)
fused_kernel(const uint16_t* __restrict__ x, const uint16_t* __restrict__ wq, const uint16_t* __restrict__ wk, const uint16_t* __restrict__ wv,
             const uint16_t* __restrict__ wo, const uint16_t* __restrict__ w1, const uint16_t* __restrict__ w2, const uint16_t* __restrict__ gw,
             uint16_t* out, char* ws) {
    __shared__ __attribute__((aligned(16))) char smem[LDS_BYTES];
    const lptr lds = (lptr)smem;
    const unsigned lds0 = (unsigned)(uintptr_t)smem;
    const int tid = threadIdx.x, lane = tid & 63, w = __builtin_amdgcn_readfirstlane(tid >> 6);
    const int r = lane & 31, h = lane >> 5, g1 = (lane >> 4) & 1, q4 = (lane & 15) >> 2, p4 = lane & 3;
    const int b = blockIdx.x, bi = b >> 3;
    const int R = 2 * (b & 7) + (bi >> 4), cb = bi & 15, rh64 = cb & 1, hg = cb >> 1;
    const bool kvcu = cb >= 14, big = w < 4;
    const int row64 = R * 128 + rh64 * 64;
    const int rt = w & 1, hq = w >> 1, fh = hq & 1;

    asm volatile("" :: "s"(x), "s"(wq), "s"(wk), "s"(wv), "s"(wo), "s"(w1), "s"(w2), "s"(gw), "s"(out), "s"(ws));
    const unsigned w64_src = (unsigned)((lane >> 3) * 1024 + hg * 128 + (((lane & 7) ^ (g1 << 2)) << 4));
    const unsigned w64_src1 = w64_src + 8192 - 1024;
    const unsigned xv = (unsigned)((lane >> 3) * 1024 + (((lane & 7) ^ ((lane >> 4) & 3)) << 4)), xv1 = (xv ^ 64) + 8192 - 1024;
    const unsigned wv0 = (unsigned)((lane >> 4) * (DFF * 2) + cb * 256 + (((lane & 15) ^ (((lane >> 4) & 3) << 2)) << 4)), wv1 = wv0 + 4 * DFF * 2 - 1024;
    const int rot = (b + cb) & 7;
    if (!kvcu) {
#pragma unroll
        for (int s = 0; s < 2; ++s) {
            const int sr = (s + rot) & 7;
            glds16x2((const char*)x + R * 131072 + sr * 128 + w * 16384, xv, xv1, lds0 + s * L_STAGE + w * 2048);
            glds16x2((const char*)w1 + (size_t)(sr * 64 + w * 8) * (DFF * 2), wv0, wv1, lds0 + s * L_STAGE + 16384 + w * 2048);
            glds16((const char*)wq + sr * 65536 + w * 8192, w64_src, lds0 + s * L_STAGE + 32768 + w * 1024);
        }
    } else {
        const char* wkv = (const char*)((w & 2) ? wk : wv);
        const unsigned kvv = (w & 1) * 1024 + ((lane ^ ((w & 2) << 2)) << 4);
#pragma unroll 1
        for (int s = 0; s < 8; ++s) {
            glds16((const char*)x + (row64 + 8 * w) * 1024 + s * 128, xv ^ (rt << 6), lds0 + s * L_KSTAGE + w * 1024);
            if (!big) glds16(wkv + s * 2048, kvv, lds0 + s * L_KSTAGE + 8192 + (w & 3) * 1024);
        }
    }
    const unsigned eo = lane == 1 ? WS_HFLAG + (R * 16 + cb) * 128 : !kvcu || lane == 0 || lane > 6 ? WS_EPOCH : lane == 2 ? WS_KVFLAG + (row64 >> 6) * 128 : WS_GZ + row64 * 8 + (lane - 3) * 128;
    const unsigned epoch_v = __hip_atomic_load((gu32*)(ws + eo), RLX_AGENT);
    asm volatile("" ::: "memory");
    const __amdgpu_buffer_rsrc_t rs_kv = mk_rsrc(ws + WS_KV, KV_BYTES), rs_h = mk_rsrc(ws + WS_H, H_BYTES), rs_o = mk_rsrc(ws + WS_O, O_BYTES);
    const __amdgpu_buffer_rsrc_t rs_kf = mk_rsrc(ws + WS_KVFLAG, 4096);
    const unsigned tr_in = 32 * g1 + 8 * p4;
    const unsigned wq_rd = (8 * h + q4) * 128 + ((fh ^ (q4 >> 1)) << 6) + tr_in;
    unsigned xo[4];
#pragma unroll
    for (int ks = 0; ks < 4; ++ks) xo[ks] = r * 128 + (((2 * ks + h) ^ ((r >> 1) & 7)) << 4);

    lptr kp, vp0, vp1, wrA0, wrA1, wrB0, wrB1, q_rd, qn_rd, km_rd, cst_wr;
    unsigned h_voff, hf_voff, g_voff0, g_voff1, hb, hb1, o_own;
    int spidx;
    f32x16 oacc;
    u32x4 cst, omark;
    s16x8 zf;
    auto consts = [&]() {
        kp = lds + lane * 16;
        const unsigned v_cst = (r & 27) != 16 ? 2064 : 2048;
        vp0 = lds + (r < 16 ? 1024 + h * 256 + r * 16 : v_cst); vp1 = lds + (r < 16 ? 1536 + h * 256 + r * 16 : v_cst);
        const unsigned w_rd0 = w * 2048 + (4 * h + q4) * 128 + ((q4 >> 1) << 6) + tr_in;
        wrA0 = lds + L_WB + w_rd0; wrA1 = lds + L_WB + (w_rd0 ^ 64); wrB0 = lds + w_rd0; wrB1 = lds + (w_rd0 ^ 64);
        q_rd = lds + L_QX + (hq * 2 + rt) * 1024 + lane * 16; qn_rd = lds + L_QN + (hq * 2 + rt) * 128 + r * 4; km_rd = lds + L_KM;
        h_voff = (unsigned)((row64 >> 5) * 112 + 14 * w) * 1024u + lane * 16;
        hf_voff = (unsigned)(R * 16 + (lane & 15)) * 128 + 4; g_voff0 = 32768 + (row64 + 32 * rt + r) * 8; g_voff1 = 32768 + (row64 + 8 * w + ((lane >> 1) & 4) + (lane >> 4)) * 8 + 4;
        spidx = 0xDDDD; oacc = (f32x16){}; zf = (s16x8){};
        cst = (lane & 1) ? (u32x4){} : (u32x4){0x3f803f80u, 0x3f803f80u, 0x3f803f80u, 0x3f803f80u};
        cst_wr = lds + (tid >> 1) * KVT + 2048 + (tid & 1) * 16;
        hb = (kvcu ? OOB : (unsigned)((R * 4 + 2 * rt) * 112 + cb * 8 + fh * 4) * 1024u) + lane * 16; hb1 = hb + 112 * 1024;
        o_own = (unsigned)(((row64 >> 5) + rt) * 32 + hg * 4 + hq) * 1024u + lane * 16;
        omark = (u32x4){~0u, ~0u, ~0u, ~0u};
        PIN(oacc); PIN(spidx); PIN(cst); PIN(cst_wr); PIN(hb); PIN(hb1); PIN(zf); PIN(o_own); PIN(omark);
        PIN(kp); PIN(vp0); PIN(vp1); PIN(wrA0); PIN(wrA1); PIN(wrB0); PIN(wrB1); PIN(q_rd); PIN(qn_rd); PIN(km_rd);
        PIN(h_voff); PIN(hf_voff); PIN(g_voff0); PIN(g_voff1);
        SBAR();
    };
    u32x4 hs[8];
#pragma unroll
    for (int i = 0; i < 8; ++i) asm volatile("" : "=v"(hs[i]));
    f32x16 a00 = {}, a01 = {}, a10 = {}, a11 = {};
    auto kv_wait = [&](u32x4& fv) {
        const unsigned ep = __builtin_amdgcn_readfirstlane(epoch_v) + 1u;
        if (__builtin_expect(!__all(fv[1] == ep && fv[3] == ep), 0)) {
            unsigned spins = 0;
            do { __builtin_amdgcn_s_sleep(6); CFENCE(); fv = bload_sc1(rs_kf, r * 128 + h * 16, 0); } while (!__all(fv[1] == ep && fv[3] == ep) && ++spins < SPIN_CAP);
        }
        CFENCE();
    };
    auto kv_bound = [&](const u32x4& fv) {
        const float km = row32_max(fmaxf(__uint_as_float(fv[0]), __uint_as_float(fv[2])));
        if (tid == 256) *(LAS float*)(lds + L_KM) = __builtin_amdgcn_sqrtf(km) * 1.0001f;
    };
    auto kv_tile = [&](int t) { glds16x2_sc1(ws + WS_KV + ((t + 8 * bi) & 63) * 2048, lane * 16, lds0 + t * KVT); };
    if (!kvcu) {
        unsigned cur = 0, nxt = L_STAGE;
        if (big) {
            const unsigned xb = rt * 8192, w0o = 16384 + (8 * h + q4) * 256 + (((2 * fh) ^ q4) << 6) + tr_in, w1o = w0o ^ 64;
            BigFrag F[4];
            auto rd = [&](BigFrag& n, lptr sb, int ks) {
                n.x0 = lds_frag(sb + xb + xo[ks]); n.x1 = lds_frag(sb + xb + 4096 + xo[ks]); n.w0 = tr_frag(sb + w0o + ks * 4096, 1024); n.w1 = tr_frag(sb + w1o + ks * 4096, 1024); };
            auto step = [&](const BigFrag& c, BigFrag& n, lptr sb, int ks) {
                a00 = MFMA(c.w0, c.x0, a00); SBAR(); n.x0 = lds_frag(sb + xb + xo[ks]); n.x1 = lds_frag(sb + xb + 4096 + xo[ks]); SBAR();
                a10 = MFMA(c.w0, c.x1, a10); SBAR(); n.w0 = tr_frag(sb + w0o + ks * 4096, 1024); SBAR();
                a01 = MFMA(c.w1, c.x0, a01); SBAR(); n.w1 = tr_frag(sb + w1o + ks * 4096, 1024); SBAR();
                a11 = MFMA(c.w1, c.x1, a11); SBAR(); };
            auto stage = [&](auto bar) {
                step(F[0], F[3], lds + cur, 3);
                bar();
                step(F[1], F[0], lds + nxt, 0); step(F[2], F[1], lds + nxt, 1); step(F[3], F[2], lds + nxt, 2);
                cur = nxt; nxt = (nxt == 2 * L_STAGE) ? 0 : nxt + L_STAGE; };
            consts();
            BAR_VL(6);
            rd(F[0], lds, 0); rd(F[1], lds, 1); rd(F[2], lds, 2);
#pragma unroll 1
            for (int s = 0; s < 7; ++s) stage([&] {
                BAR_VL(1);
                if (s == 3 && w < 2) touch8k(w == 0 ? (const char*)w2 + (R * 14 + cb) * 8192 : (const char*)wo + (b & 63) * 8192, lane, lds0 + L_TOUCH); });
            rd(F[3], lds + cur, 3);
#define LAST4(acc, wm, xm) do { _Pragma("unroll") for (int ks = 0; ks < 4; ++ks) acc = MFMA(F[ks].wm, F[ks].xm, acc); } while (0)
#define LAST4P(acc, wm, xm, prev, i) do { acc = MFMA(F[0].wm, F[0].xm, acc); acc = MFMA(F[1].wm, F[1].xm, acc); SBAR(); hs[i] = pack8_relu<0>(prev); PIN(hs[i]); SBAR(); \
            acc = MFMA(F[2].wm, F[2].xm, acc); SBAR(); hs[(i) + 1] = pack8_relu<1>(prev); PIN(hs[(i) + 1]); SBAR(); acc = MFMA(F[3].wm, F[3].xm, acc); SBAR(); } while (0)
            LAST4(a00, w0, x0); SBAR();
            LAST4P(a01, w1, x0, a00, 0); LAST4P(a10, w0, x1, a01, 2); LAST4P(a11, w1, x1, a10, 4);
            hs[6] = pack8_relu<0>(a11); hs[7] = pack8_relu<1>(a11);
        } else {
            const int j = w - 4;
            __builtin_amdgcn_s_setprio(2);
            const unsigned xb = (2 * rh64 + rt) * 4096, qo = 32768 + wq_rd;
            SmallFrag F[4];
            auto rd = [&](SmallFrag& n, lptr sb, int ks) { n.x = lds_frag(sb + xb + xo[ks]); n.q = tr_frag(sb + qo + ks * 2048, 512); SBAR(); };
            auto mma = [&](const SmallFrag& c) { a00 = MFMA(c.q, c.x, a00); SBAR(); };
            const unsigned xv2 = xv + 2 * (8192 - 1024), xv3 = xv1 + 2 * (8192 - 1024), wv2 = wv0 + 2 * (4 * DFF * 2 - 1024), wv3 = wv1 + 2 * (4 * DFF * 2 - 1024);
            const char* xs = (const char*)x + R * 131072 + j * 32768;
            const char* w1s = (const char*)w1 + (size_t)(j * 16) * (DFF * 2);
            const char* wqs = (const char*)wq + j * 16384;
            auto fill0 = [&](int sr, unsigned slot) { glds16x4(xs + sr * 128, xv, xv1, xv2, xv3, lds0 + slot + j * 4096); };
            auto fill1 = [&](int sr, unsigned slot) { glds16x4(w1s + sr * (64 * DFF * 2), wv0, wv1, wv2, wv3, lds0 + slot + 16384 + j * 4096); };
            auto fill2 = [&](int sr, unsigned slot) { glds16x2(wqs + sr * 65536, w64_src, w64_src1, lds0 + slot + 32768 + j * 2048); };
            fill0((rot + 2) & 7, 2 * L_STAGE); fill1((rot + 2) & 7, 2 * L_STAGE); fill2((rot + 2) & 7, 2 * L_STAGE);
            consts();
            BAR_VL(16);
            rd(F[0], lds, 0); rd(F[1], lds, 1); rd(F[2], lds, 2);
#pragma unroll 1
            for (int s = 0; s < 6; ++s) {
                const bool more = s < 5;
                const int sr = (rot + s + 3) & 7;
                rd(F[3], lds + cur, 3); mma(F[0]);
                BAR_VL(10);
                if (more) fill0(sr, cur);
                rd(F[0], lds + nxt, 0); mma(F[1]);
                if (more) fill1(sr, cur);
                rd(F[1], lds + nxt, 1); mma(F[2]);
                if (more) fill2(sr, cur);
                rd(F[2], lds + nxt, 2); mma(F[3]);
                cur = nxt; nxt = (nxt == 2 * L_STAGE) ? 0 : nxt + L_STAGE;
            }
            u32x4 fv = bload_sc1(rs_kf, r * 128 + h * 16, 0);
            rd(F[3], lds, 3); mma(F[0]);
            BAR_VL(0);
            rd(F[0], lds + L_STAGE, 0); mma(F[1]);
            kv_wait(fv);
            kv_tile(j); kv_tile(4 + j);
            rd(F[1], lds + L_STAGE, 1); mma(F[2]);
            kv_tile(8 + j); kv_tile(12 + j);
            rd(F[2], lds + L_STAGE, 2); mma(F[3]); rd(F[3], lds + L_STAGE, 3);
            mma(F[0]); kv_bound(fv); mma(F[1]); mma(F[2]); mma(F[3]);
        }
    } else {
        const unsigned xb = rt * 4096, w64_src2 = w64_src + 2 * (8192 - 1024), w64_src3 = w64_src + 3 * (8192 - 1024);
        {
            const unsigned wkv_rd = 8192 + g1 * 2048 + (((8 * h + q4) * 32 + 8 * p4) ^ (g1 << 7));
#pragma unroll
            for (int g = 0; g < 2; ++g) {
                if (g == 0) { if (big) BAR_VL(5); else BAR_VL(9); } else BAR_VL(1);
                const lptr st = lds + (g * 4 + hq) * L_KSTAGE;
#pragma unroll
                for (int k = 0; k < 4; ++k) a00 = MFMA(tr_frag2(st + wkv_rd + k * 512, st + (wkv_rd ^ 128) + k * 512), lds_frag(st + xb + xo[k]), a00);
            }
        }
        BAR_L();
        {
            const lptr pw = lds + w * L_KSTAGE + 8192 + r * 128;
            const unsigned sz = (r & 7) ^ ((r >> 1) & 4);
#pragma unroll
            for (int i = 0; i < 4; ++i) *(LAS f32x4*)(pw + (((2 * i + h) ^ sz) << 4)) = (f32x4){a00[4 * i], a00[4 * i + 1], a00[4 * i + 2], a00[4 * i + 3]};
            a00 = (f32x16){};
        }
        BAR_L();
        if (big) {
            const unsigned kvo = (unsigned)((row64 >> 5) + rt) * 2048u + lane * 16;
            const lptr pr = lds + rt * L_KSTAGE + 8192;
            float n2 = 0.f;
            if (fh == 0) {
                const unsigned sz = (r & 7) ^ ((r >> 1) & 4);
                f32x4 lo = {}, hi = {};
#pragma unroll
                for (int i = 0; i < 4; ++i) { lo += *(const LAS f32x4*)(pr + i * 2 * L_KSTAGE + r * 128 + (((4 + h) ^ sz) << 4)); hi += *(const LAS f32x4*)(pr + i * 2 * L_KSTAGE + r * 128 + (((6 + h) ^ sz) << 4)); }
                const u32x4 v = {cvtpk(lo[0], lo[1]), cvtpk(lo[2], lo[3]), cvtpk(hi[0], hi[1]), cvtpk(hi[2], hi[3])};
                bstore_sc1(v, rs_kv, kvo);
                n2 = row32_max(swap_add(dot2(v[0], v[0], dot2(v[1], v[1], 0.f)) + dot2(v[2], v[2], dot2(v[3], v[3], 0.f))));
            } else {
                float t[8];
#pragma unroll
                for (int j = 0; j < 8; ++j) {
                    const lptr q = pr + ((lane >> 4) * 8 + j) * 128 + ((q4 ^ j ^ (g1 << 2)) << 4) + p4 * 4;
                    t[j] = (*(const LAS float*)q + *(const LAS float*)(q + 2 * L_KSTAGE)) + (*(const LAS float*)(q + 4 * L_KSTAGE) + *(const LAS float*)(q + 6 * L_KSTAGE));
                }
                bstore_sc1((u32x4){cvtpk(t[0], t[1]), cvtpk(t[2], t[3]), cvtpk(t[4], t[5]), cvtpk(t[6], t[7])}, rs_kv, kvo + 1024);
            }
            WAIT_V(0);
            if (lane == 0) __hip_atomic_store((gu64*)(ws + WS_KVFLAG) + (row64 >> 6) * 16 + w, ((unsigned long long)(epoch_v + 1u) << 32) | __float_as_uint(n2), RLX_AGENT);
            u32x4 gf[16];
#pragma unroll
            for (int i = 0; i < 16; ++i) gf[i] = *(const u32x4*)(gw + i * 32 + (lane >> 4) * 8);
            consts();
            BAR_L();
            f32x4 z = {};
            const int zr = 16 * w + (lane & 15);
#pragma unroll
            for (int i = 0; i < 16; ++i)
                z = __builtin_amdgcn_mfma_f32_16x16x32_bf16(asfrag(gf[i]), lds_frag(lds + (i >> 1) * L_KSTAGE + zr * 128 + (((4 * (i & 1) + (lane >> 4)) ^ ((zr >> 1) & 7)) << 4)), z, 0, 0, 0);
            const float gt = rbf(__builtin_amdgcn_rcpf(1.f + __expf(-rbf(z[0])))), og = fmaxf(rbf(1.f - gt), 0x1p-40f);
            if (lane < 16) __hip_atomic_store((gu64*)(ws + WS_GZ) + row64 + zr, ((unsigned long long)__float_as_uint(og) << 32) | __float_as_uint(gt / og), RLX_AGENT);
            BAR_L();
        } else {
#pragma unroll
            for (int i = 0; i < 4; ++i) { const int p = i * 16 + (w - 4) * 4; glds16x4((const char*)wq + p * 8192, w64_src, w64_src1, w64_src2, w64_src3, lds0 + L_KWQ + p * 1024); }
            consts();
            BAR_VL(0);
            SmallFrag F[4];
            auto rd = [&](SmallFrag& n, int s, int k) { n.x = lds_frag(lds + s * L_KSTAGE + xb + xo[k]); n.q = tr_frag(lds + L_KWQ + s * 8192 + wq_rd + k * 2048, 512); SBAR(); };
#pragma unroll
            for (int k = 0; k < 4; ++k) rd(F[k], 0, k);
#pragma unroll 1
            for (int s = 1; s < 8; ++s) {
#pragma unroll
                for (int k = 0; k < 4; ++k) { a00 = MFMA(F[k].q, F[k].x, a00); SBAR(); rd(F[k], s, k); }
            }
            u32x4 fv = bload_sc1(rs_kf, r * 128 + h * 16, 0);
#pragma unroll
            for (int k = 0; k < 4; ++k) a00 = MFMA(F[k].q, F[k].x, a00);
            BAR_L();
            kv_wait(fv);
            kv_tile(w - 4); kv_tile(w); kv_tile(4 + w); kv_tile(8 + w);
            kv_bound(fv);
        }
    }
    if (big) {
        if (tid < 32) lds_put(cst_wr, cst);
    } else {
#pragma unroll
        for (int s = 0; s < 2; ++s) {
            const u32x4 v = s ? pack8<1>(a00, QSCALE) : pack8<0>(a00, QSCALE);
            lds_put(lds + L_QX + (4 * fh + 2 * s + rt) * 1024 + lane * 16, v);
            const float n2 = swap_add(dot2(v[0], v[0], dot2(v[1], v[1], 0.f)) + dot2(v[2], v[2], dot2(v[3], v[3], 0.f)));
            *(LAS float*)(lds + L_QN + (4 * fh + 2 * s + rt) * 128 + r * 4) = __builtin_amdgcn_sqrtf(n2);
        }
    }
    __builtin_amdgcn_s_setprio(3);
    BAR_VL(0);
    const s16x8 qf = lds_frag(q_rd);
    s16x8 kf = lds_frag(kp), vf0, vf1;
    float mshift = *(const LAS float*)qn_rd * *(const LAS float*)km_rd;
    vf0 = lds_frag(vp0);
    if (__builtin_expect(!__all(mshift <= 0.5f * SAFE_RANGE), 0)) {
        float m = -__builtin_inff();
#pragma unroll 1
        for (int t = 0; t < 64; ++t) {
            const f32x16 s = MFMA(asfrag(bload_sc1(rs_kv, lane * 16, t * 2048)), qf, (f32x16){});
#pragma unroll
            for (int i = 0; i < 16; ++i) m = fmaxf(m, s[i]);
        }
        mshift = swap_max(m);
    }
    f32x16 cinit;
#pragma unroll
    for (int i = 0; i < 16; ++i) cinit[i] = -mshift;
    PIN(cinit);
    f32x16 sA = MFMA(kf, qf, cinit), sB;
    kf = lds_frag(kp + KVT);
    if (tid >= 32 && tid < 128) lds_put(cst_wr, cst);

#define S1(S_, i) S_[i] = __builtin_amdgcn_exp2f(S_[i])
#define EX2(S_, i) do { S1(S_, i); S1(S_, (i) + 1); } while (0)
#define EX3(S_, i) do { S1(S_, i); S1(S_, (i) + 1); S1(S_, (i) + 2); } while (0)
#define EX4(S_, i) do { EX2(S_, i); EX2(S_, (i) + 2); } while (0)
#define PV1(SN, T) do { if ((T) >= 0) oacc = pv_mma<1>(vf1, SN, oacc, spidx); vf1 = lds_frag(vp1 + ((T) < 0 ? 0 : (T)) * KVT); SBAR(); } while (0)
#define QK(SN, T) do { SN = MFMA(kf, qf, cinit); kf = lds_frag(kp + ((T) + 2) * KVT); SBAR(); } while (0)
#define PV0(SC, T) do { oacc = pv_mma<0>(vf0, SC, oacc, spidx); vf0 = lds_frag(vp0 + ((T) + 1) * KVT); SBAR(); } while (0)
#define CUT(S_) do { PIN(S_); SBAR(); } while (0)
#define ITER0(SC, SN, T, T1, PR, MEM) do { PR; \
    EX2(SC, 0); CUT(SC); PV1(SN, T1); EX4(SC, 2); CUT(SC); QK(SN, T); EX4(SC, 6); S1(SC, 10); CUT(SC); PV0(SC, T); MEM; S1(SC, 11); EX4(SC, 12); CUT(SC); } while (0)
#define ITER1(SC, SN, T, PR, PRE, MEM, G1) do { PR; PRE; \
    EX2(SC, 0); CUT(SC); PV1(SN, T); EX4(SC, 2); CUT(SC); QK(SN, T); EX4(SC, 6); CUT(SC); PV0(SC, T); MEM; EX4(SC, 10); CUT(SC); G1; SBAR(); EX2(SC, 14); CUT(SC); } while (0)
#define ITERG(SC, SN, T, PR, MEM, G0, G1) do { PR; \
    EX2(SC, 0); CUT(SC); PV1(SN, T); EX3(SC, 2); CUT(SC); QK(SN, T); EX3(SC, 5); CUT(SC); G0; SBAR(); EX3(SC, 8); CUT(SC); PV0(SC, T); MEM; EX3(SC, 11); CUT(SC); G1; SBAR(); EX2(SC, 14); CUT(SC); } while (0)
#define NOP (void)0
#define PRIO(p) __builtin_amdgcn_s_setprio(p)
#define ITER2(T, P0, P1, M0, M1) do { ITER0(sA, sB, T, T, PRIO(P0), M0); ITER0(sB, sA, (T) + 1, (T) + 1, PRIO(P1), M1); } while (0)
#define ADV(n) do { kp += (n) * KVT; vp0 += (n) * KVT; vp1 += (n) * KVT; PIN(kp); PIN(vp0); PIN(vp1); } while (0)
    const char* const w2w = (const char*)w2 + w * (14 * 16384);
    const char* const wow = (const char*)wo + w * 65536;
    int pn = (row64 >> 6) % 7;
    const unsigned wbA = lds0 + L_WB + w * 2048, wbB = lds0 + w * 2048;
    const __amdgpu_buffer_rsrc_t rs_f = mk_rsrc(ws + WS_HFLAG, 32768 + 16384);
    u32x4 hA0, hA1, hB0, hB1;
    s16x8 wf0, wf1;
#define W_LD(src, buf) glds16x2(src, w64_src, w64_src1, buf)
#define H_ST(i) do { if (big) bstore_sc1(hs[i], rs_h, ((i) < 4 ? hb : hb1) + ((i) & 3) * 1024); } while (0)
    ITER0(sA, sB, 0, -1, PRIO(3), H_ST(0)); ITER0(sB, sA, 1, 1, NOP, H_ST(1); kv_tile(16 + w));
    ITER2(2, 3, 3, H_ST(2); kv_tile(24 + w), H_ST(3); kv_tile(32 + w));
    ITER2(4, 2, 2, H_ST(4); kv_tile(40 + w), H_ST(5); kv_tile(48 + w));
    ITER2(6, 2, 2, H_ST(6); kv_tile(56 + w), H_ST(7); W_LD(w2w + pn * 32768, wbA));
    ITER2(8, 1, 1, bstore_sc1(omark, rs_o, o_own), NOP);
    ITER2(10, 1, 1, NOP, NOP);
    ITER2(12, 0, 0, NOP, NOP);
    BAR_VL(0);
    unsigned epoch_w = epoch_v; PIN(epoch_w);
    const unsigned epoch = __builtin_amdgcn_readfirstlane(epoch_w) + 1u;
    if (tid == 0) __hip_atomic_store((gu32*)(ws + WS_HFLAG) + (R * 16 + cb) * 32 + 1, epoch, RLX_AGENT);
    unsigned hfv;
    ITER2(14, 1, 3, W_LD(w2w + pn * 32768 + 16384, wbB); a00 = MFMA(zf, zf, (f32x16){}); PIN(zf); a01 = MFMA(zf, zf, (f32x16){}), PIN(zf); a10 = MFMA(zf, zf, (f32x16){}); PIN(zf); a11 = MFMA(zf, zf, (f32x16){}));
    ITER2(16, 2, 1, NOP, hfv = __builtin_amdgcn_raw_buffer_load_b32(rs_f, hf_voff, 0, 16));
    ITER0(sA, sB, 18, 18, PRIO(3), NOP);
    if (__builtin_expect(!__all(hfv == epoch), 0)) {
        unsigned spins = 0;
        do { __builtin_amdgcn_s_sleep(6); CFENCE(); hfv = __builtin_amdgcn_raw_buffer_load_b32(rs_f, hf_voff, 0, 16); } while (!__all(hfv == epoch) && ++spins < SPIN_CAP);
    }
    CFENCE();
    unsigned gr_u, og_u;
    ITER0(sB, sA, 19, 19, PRIO(2), gr_u = __builtin_amdgcn_raw_buffer_load_b32(rs_f, g_voff0, 0, 16); og_u = __builtin_amdgcn_raw_buffer_load_b32(rs_f, g_voff1, 0, 16);
          hA0 = bload_sc1(rs_h, h_voff, pn * 2048); hA1 = bload_sc1(rs_h, h_voff, pn * 2048 + 112 * 1024));
    ITER2(20, 1, 3, hB0 = bload_sc1(rs_h, h_voff, pn * 2048 + 1024), hB1 = bload_sc1(rs_h, h_voff, pn * 2048 + 113 * 1024));
    ADV(22);
    PIN(a00); PIN(a01); PIN(a10); PIN(a11);
#define GM(acc, wf, H) acc = MFMA(asfrag(H), wf, acc); SBAR()
#define GSTEP(SX, SY, T, R0, R1, H0, H1, LW, LH0, LH1) do { \
    ITER1(SX, SY, T, PRIO(3), WAIT_V(5); wf0 = tr_frag(R0, 1024); wf1 = tr_frag(R1, 1024), NOP, GM(a00, wf0, H0)); \
    ITERG(SY, SX, (T) + 1, PRIO(2), LW, GM(a01, wf1, H0); LH0, GM(a10, wf0, H1)); \
    ITER1(SX, SY, (T) + 2, PRIO(1), NOP, NOP, GM(a11, wf1, H1); LH1); } while (0)
#pragma unroll 1
    for (int n = 0; n < 4; ++n) {
        pn = pn == 6 ? 0 : pn + 1;
        const char* wsrc = w2w + pn * 32768;
        GSTEP(sA, sB, 0, wrA0, wrA1, hA0, hA1, W_LD(wsrc, wbA), hA0 = bload_sc1(rs_h, h_voff, pn * 2048), hA1 = bload_sc1(rs_h, h_voff, pn * 2048 + 112 * 1024));
        GSTEP(sB, sA, 3, wrB0, wrB1, hB0, hB1, W_LD(wsrc + 16384, wbB), hB0 = bload_sc1(rs_h, h_voff, pn * 2048 + 1024), hB1 = bload_sc1(rs_h, h_voff, pn * 2048 + 113 * 1024));
        ADV(6);
    }
    BAR_L();
    pn = pn == 6 ? 0 : pn + 1;
    const int pm = pn == 6 ? 0 : pn + 1;
    u32x4 hC0, hC1, hD0, hD1, hE0, hE1, hF0, hF1;
    ITER2(0, 3, 2, W_LD(w2w + pn * 32768, wbB + 16384); hC0 = bload_sc1(rs_h, h_voff, pn * 2048), hC1 = bload_sc1(rs_h, h_voff, pn * 2048 + 112 * 1024));
    ITER2(2, 1, 3, W_LD(w2w + pn * 32768 + 16384, wbB + 32768); hD0 = bload_sc1(rs_h, h_voff, pn * 2048 + 1024), hD1 = bload_sc1(rs_h, h_voff, pn * 2048 + 113 * 1024));
    ITER2(4, 2, 1, W_LD(w2w + pm * 32768, wbB + 49152); hE0 = bload_sc1(rs_h, h_voff, pm * 2048), hE1 = bload_sc1(rs_h, h_voff, pm * 2048 + 112 * 1024));
    ITER2(6, 3, 2, W_LD(w2w + pm * 32768 + 16384, wbB + 65536); hF0 = bload_sc1(rs_h, h_voff, pm * 2048 + 1024), hF1 = bload_sc1(rs_h, h_voff, pm * 2048 + 113 * 1024));
    const bool closer = b == 112 && w == 7;
    unsigned ef[4];
    auto ef_load = [&]() {
#pragma unroll
        for (int i = 0; i < 4; ++i) ef[i] = __builtin_amdgcn_raw_buffer_load_b32(mk_rsrc(ws + WS_HFLAG + i * 8192 + 4, closer ? 8188 : 0), lane * 128, 0, 16);
    };
    const unsigned xtra = w < 3 ? 5 * 16384 : L_QX - 3 * 2048;
    s16x8 wf2, wf3, wf4, wf5;
    ITER2(8, 1, 3, WAIT_V(14); wf0 = tr_frag(wrA0, 1024); wf1 = tr_frag(wrA1, 1024); W_LD(wow + 49152, wbB + xtra), wf2 = tr_frag(wrB0, 1024); wf3 = tr_frag(wrB1, 1024); ef_load());
    ITER2(10, 2, 1, wf4 = tr_frag(wrB0 + 16384, 1024); wf5 = tr_frag(wrB1 + 16384, 1024); PIN(wf0); PIN(wf1); W_LD(wow, wbA), PIN(wf2); PIN(wf3); W_LD(wow + 16384, wbB));
    ITER2(12, 3, 3, PIN(wf4); PIN(wf5); W_LD(wow + 32768, wbB + 16384), NOP);
    ITER2(14, 2, 2, NOP, NOP);
    ITER2(16, 1, 0, NOP, NOP);
#define GMA(acc, wf, H) asm volatile("v_mfma_f32_32x32x16_bf16 %0, %1, %2, %0" : "+v"(acc) : "v"(H), "v"(wf))
    PRIO(3);
    oacc = pv_mma<1>(vf1, sB, oacc, spidx);
    {
        u32x4 v = pack8<0>(oacc, __builtin_amdgcn_rcpf(oacc[8]) * __uint_as_float(gr_u));
#pragma unroll
        for (int e = 0; e < 4; ++e) v[e] = v[e] < 0xfffffffeu ? v[e] : 0xfffffffeu;
        bstore_sc1(v, rs_o, o_own);
        asm volatile("s_nop 1");
    }
    PRIO(0);
    GMA(a00, wf0, hA0); GMA(a01, wf1, hA0);
    WAIT_V(1);
    lptr wrE0 = wrB0 + 32768, wrE1 = wrB1 + 32768; PIN(wrE0); PIN(wrE1);
    const s16x8 wf6 = tr_frag(wrE0, 1024), wf7 = tr_frag(wrE1, 1024), wf8 = tr_frag(wrE0 + 16384, 1024), wf9 = tr_frag(wrE1 + 16384, 1024);
    SBAR();
    GMA(a10, wf0, hA1); GMA(a11, wf1, hA1);
    const s16x8 wfa = tr_frag(wrE0 + 32768, 1024), wfb = tr_frag(wrE1 + 32768, 1024);
    s16x8 wof[8];
    wof[0] = tr_frag(wrA0, 1024); wof[1] = tr_frag(wrA1, 1024);
    SBAR();
    GMA(a00, wf2, hB0); GMA(a01, wf3, hB0);
    wof[2] = tr_frag(wrB0, 1024); wof[3] = tr_frag(wrB1, 1024); wof[4] = tr_frag(wrB0 + 16384, 1024); wof[5] = tr_frag(wrB1 + 16384, 1024);
    SBAR();
    GMA(a10, wf2, hB1); GMA(a11, wf3, hB1);
    wof[6] = tr_frag(wrB0 + xtra, 1024); wof[7] = tr_frag(wrB1 + xtra, 1024);
    SBAR();
    GMA(a00, wf4, hC0); GMA(a01, wf5, hC0); GMA(a10, wf4, hC1); GMA(a11, wf5, hC1);
    const unsigned ob = (unsigned)((row64 >> 5) * 32 + 4 * w) * 1024u;
    u32x4 of[8];
    auto o_load = [&]() {
#pragma unroll
        for (int i = 0; i < 8; ++i) of[i] = bload_sc1(rs_o, lane * 16 + (i & 3) * 1024, ob + (i >> 2) * 32768);
    };
    auto o_ready = [&]() {
        unsigned m = 0;
#pragma unroll
        for (int i = 0; i < 8; ++i) { m = max(m, max(of[i][0], of[i][1])); m = max(m, max(of[i][2], of[i][3])); }
        return __all(m != ~0u);
    };
    BAR_VL(0);
#pragma unroll
    for (int i = 0; i < 8; ++i) PIN(wof[i]);
    o_load();
    SBAR();
    GMA(a00, wf6, hD0); GMA(a01, wf7, hD0); GMA(a10, wf6, hD1); GMA(a11, wf7, hD1);
    GMA(a00, wf8, hE0); GMA(a01, wf9, hE0); GMA(a10, wf8, hE1); GMA(a11, wf9, hE1);
    GMA(a00, wfa, hF0); GMA(a01, wfb, hF0); GMA(a10, wfa, hF1); GMA(a11, wfb, hF1);
    SBAR();
    const lptr prb = lds + (((w >> 2) * 2 + ((lane >> 2) & 1)) * 16 + 4 * (w & 3) + (lane >> 4)) * 2048 + ((lane >> 3) & 1) * 128 + p4 * 32;
    lptr pr0 = prb + g1 * 16, pr1 = prb + (g1 ^ 1) * 16;
    PIN(pr0); PIN(pr1);
    uint16_t* outp = out + (row64 + 8 * w + ((lane >> 1) & 4) + (lane >> 4)) * 512 + hg * 64 + (lane & 7) * 8;
    PIN(outp);
    SBAR();
    if (!o_ready()) {
        unsigned spins = 0;
        do { CFENCE(); o_load(); } while (!o_ready() && ++spins < SPIN_CAP);
    }
#define AMM(acc, wi, oi) GMA(acc, wof[wi], of[oi])
#define PUT(a, i, off) asm volatile("ds_write_addtid_b32 %0 offset:%4\n\tds_write_addtid_b32 %1 offset:%5\n\tds_write_addtid_b32 %2 offset:%6\n\tds_write_addtid_b32 %3 offset:%7" \
    :: "v"(a[4 * (i)]), "v"(a[4 * (i) + 1]), "v"(a[4 * (i) + 2]), "v"(a[4 * (i) + 3]), "n"((off) + (i) * 8192), "n"((off) + (i) * 8192 + 2048), "n"((off) + (i) * 8192 + 4096), "n"((off) + (i) * 8192 + 6144) : "memory")
#define SETM0(v, prev) asm volatile("s_mov_b32 m0, %1\n\ts_nop 10" : "+v"(prev) : "s"(v))
#define MM_PUT(acc, c, o, prev, m0v, off) do { AMM(acc, c, o); SETM0(m0v, prev); PUT(prev, 0, off); AMM(acc, 2 + (c), (o) + 1); PUT(prev, 1, off); \
    AMM(acc, 4 + (c), (o) + 2); PUT(prev, 2, off); AMM(acc, 6 + (c), (o) + 3); PUT(prev, 3, off); } while (0)
    const unsigned m0a = lds0 + w * 256, m0b = m0a + 63616;
    asm volatile("s_nop 1");
    AMM(a00, 0, 0); AMM(a00, 2, 1); AMM(a00, 4, 2); AMM(a00, 6, 3);
    MM_PUT(a01, 1, 0, a00, m0a, 0);
    MM_PUT(a10, 0, 4, a01, m0a, 32768);
    MM_PUT(a11, 1, 4, a10, m0b, 65536 - 63616);
    asm volatile("s_nop 7" : "+v"(a11));
    PUT(a11, 0, 98304 - 63616); PUT(a11, 1, 98304 - 63616); PUT(a11, 2, 98304 - 63616); PUT(a11, 3, 98304 - 63616);
    BAR_L();
    {
        f32x4 t[16];
#pragma unroll
        for (int k = 0; k < 8; ++k) { t[2 * k] = *(const LAS f32x4*)(pr0 + k * 256); t[2 * k + 1] = *(const LAS f32x4*)(pr1 + k * 256); }
        SBAR();
        f32x4 lo = t[0], hi = t[1];
#pragma unroll
        for (int k = 1; k < 8; ++k) { lo += t[2 * k]; hi += t[2 * k + 1]; }
        lo *= __uint_as_float(og_u); hi *= __uint_as_float(og_u);
        const unsigned l0 = cvtpk(lo[0], lo[1]), l1 = cvtpk(lo[2], lo[3]), h0 = cvtpk(hi[0], hi[1]), h1 = cvtpk(hi[2], hi[3]);
        const u32x4 v = g1 ? (u32x4){h0, h1, l0, l1} : (u32x4){l0, l1, h0, h1};
        __builtin_nontemporal_store(v, (GAS u32x4*)outp);
    }
    if (closer) {
        unsigned spins = 0;
        while (!__all(ef[0] == epoch && ef[1] == epoch && ef[2] == epoch && ef[3] == epoch) && ++spins < SPIN_CAP) { CFENCE(); ef_load(); }
        if (lane == 0) __hip_atomic_store((gu32*)(ws + WS_EPOCH), epoch, RLX_AGENT);
    }
}

extern "C" void kernel_launch(void* const* d_in, const int* in_sizes, int n_in, void* d_out, int out_size, void* d_ws, size_t ws_size,
                              hipStream_t stream) {
    fused_kernel<<<256, 512, 0, stream>>>((const uint16_t*)d_in[0], (const uint16_t*)d_in[1], (const uint16_t*)d_in[2], (const uint16_t*)d_in[3],
                                          (const uint16_t*)d_in[4], (const uint16_t*)d_in[6], (const uint16_t*)d_in[7], (const uint16_t*)d_in[8],
                                          (uint16_t*)d_out, (char*)d_ws);
}
```

```cpp
#include <hip/hip_runtime.h>
#include <stdint.h>

typedef short s16x8 __attribute__((ext_vector_type(8)));
typedef short s16x4 __attribute__((ext_vector_type(4)));
typedef short s16x2 __attribute__((ext_vector_type(2)));
typedef float f32x16 __attribute__((ext_vector_type(16)));
typedef float f32x4 __attribute__((ext_vector_type(4)));
typedef float f32x2 __attribute__((ext_vector_type(2)));
typedef unsigned u32x4 __attribute__((ext_vector_type(4)));
typedef __bf16 bf16x2 __attribute__((ext_vector_type(2)));
typedef __bf16 bf16x8 __attribute__((ext_vector_type(8)));
typedef __bf16 bf16x16 __attribute__((ext_vector_type(16)));
#define LAS __attribute__((address_space(3)))
#define GAS __attribute__((address_space(1)))
typedef GAS unsigned gu32;
typedef GAS unsigned long long gu64;
typedef LAS char* lptr;
#define DI __device__ __forceinline__
#define RLX_AGENT __ATOMIC_RELAXED, __HIP_MEMORY_SCOPE_AGENT
#define MFMA(a, b, c) __builtin_amdgcn_mfma_f32_32x32x16_bf16((a), (b), (c), 0, 0, 0)
#define SBAR() __builtin_amdgcn_sched_barrier(0)
#define PIN(x) asm volatile("" : "+v"(x))
#define CFENCE() do { asm volatile("" ::: "memory"); __builtin_amdgcn_fence(__ATOMIC_ACQUIRE, "wavefront"); } while (0)
#define BAR_L() asm volatile("s_waitcnt lgkmcnt(0)\n\ts_barrier" ::: "memory")
#define BAR_VL(N) asm volatile("s_waitcnt vmcnt(" #N ") lgkmcnt(0)\n\ts_barrier" ::: "memory")

#define ABL 0
#define STW 0
#define STAMPS 0

constexpr int D = 512, DFF = 1792;
constexpr float QSCALE = 0.25f * 1.4426950408889634f;
constexpr float SAFE_RANGE = 64.f;
constexpr unsigned WS_EPOCH = 0, WS_KVFLAG = 4096, WS_HFLAG = 8192;
constexpr unsigned WS_GZ = WS_HFLAG + 32768;
constexpr unsigned WS_KV = WS_GZ + 16384, KV_BYTES = 64 * 2048;
constexpr unsigned WS_H = WS_KV + KV_BYTES, H_BYTES = 64 * 112 * 1024;
constexpr unsigned WS_O = WS_H + H_BYTES, O_BYTES = 64 * 32 * 1024;
constexpr unsigned WS_STAMP = WS_O + O_BYTES;
constexpr unsigned OOB = 0x7ff00000u;
constexpr unsigned L_STAGE = 40960;
constexpr unsigned L_KSTAGE = 12288, L_KWQ = 98304;
constexpr unsigned KVT = 2080;
constexpr unsigned L_WB = 64 * KVT;
constexpr unsigned L_QX = L_WB + 16384, L_QN = L_QX + 8192, L_KM = L_QN + 1024, L_TOUCH = L_KM + 256, LDS_BYTES = 163840;
static_assert(L_TOUCH + 256 <= LDS_BYTES, "lds map");
constexpr unsigned SPIN_CAP = 1u << 16;

DI unsigned cvtpk(float a, float b) { f32x2 v = {a, b}; return __builtin_bit_cast(unsigned, __builtin_convertvector(v, bf16x2)); }
DI float rbf(float x) { return __uint_as_float(cvtpk(x, x) << 16); }
DI s16x8 asfrag(u32x4 v) { return __builtin_bit_cast(s16x8, v); }
DI s16x8 lds_frag(lptr p) { return *(const LAS s16x8*)p; }
DI void lds_put(lptr p, u32x4 v) { *(LAS u32x4*)p = v; }
DI s16x4 lds_tr(lptr p) { return __builtin_amdgcn_ds_read_tr16_b64_v4i16((LAS s16x4*)p); }
DI s16x8 tr_frag2(lptr p, lptr q) { s16x4 lo = lds_tr(p), hi = lds_tr(q); return __builtin_shufflevector(lo, hi, 0, 1, 2, 3, 4, 5, 6, 7); }
DI s16x8 tr_frag(lptr p, int second) { return tr_frag2(p, p + second); }
DI float swap_add(float v) { auto r = __builtin_amdgcn_permlane32_swap(__float_as_uint(v), __float_as_uint(v), false, false); return __uint_as_float(r[0]) + __uint_as_float(r[1]); }
DI float swap_max(float v) { auto r = __builtin_amdgcn_permlane32_swap(__float_as_uint(v), __float_as_uint(v), false, false); return fmaxf(__uint_as_float(r[0]), __uint_as_float(r[1])); }
DI float dot2(unsigned a, unsigned b, float c) { return __builtin_amdgcn_fdot2_f32_bf16(__builtin_bit_cast(bf16x2, a), __builtin_bit_cast(bf16x2, b), c, false); }
DI unsigned relu_pk(unsigned u) { return __builtin_bit_cast(unsigned, __builtin_elementwise_max(__builtin_bit_cast(s16x2, u), (s16x2){0, 0})); }
template <int CTRL> DI float dpp(float v) { return __uint_as_float(__builtin_amdgcn_update_dpp(0, __float_as_uint(v), CTRL, 0xf, 0xf, true)); }
DI float row32_max(float v) {
    v = fmaxf(v, dpp<0xB1>(v)); v = fmaxf(v, dpp<0x4E>(v)); v = fmaxf(v, dpp<0x141>(v)); v = fmaxf(v, dpp<0x140>(v));
    auto r = __builtin_amdgcn_permlane16_swap(__float_as_uint(v), __float_as_uint(v), false, false);
    return fmaxf(__uint_as_float(r[0]), __uint_as_float(r[1]));
}
template <int HALF> DI u32x4 pack8(const f32x16& a, float m) {
    u32x4 v;
#pragma unroll
    for (int e = 0; e < 4; ++e) v[e] = cvtpk(a[8 * HALF + 2 * e] * m, a[8 * HALF + 2 * e + 1] * m);
    return v;
}
template <int HALF> DI u32x4 pack8_relu(const f32x16& a) {
    u32x4 v;
#pragma unroll
    for (int e = 0; e < 4; ++e) v[e] = relu_pk(cvtpk(a[8 * HALF + 2 * e], a[8 * HALF + 2 * e + 1]));
    return v;
}
template <int HALF> DI f32x16 pv_mma(s16x8 v, const f32x16& p, f32x16 c, int idx) {
    const auto ph = __builtin_shufflevector(p, p, 8 * HALF, 8 * HALF + 1, 8 * HALF + 2, 8 * HALF + 3, 8 * HALF + 4, 8 * HALF + 5, 8 * HALF + 6, 8 * HALF + 7);
    return __builtin_amdgcn_smfmac_f32_32x32x32_bf16(__builtin_bit_cast(bf16x8, v), __builtin_bit_cast(bf16x16, ph), c, idx, 0, 0);
}
DI void glds16(const void* sbase, unsigned voff, unsigned lds_base) {
    asm volatile("s_mov_b32 m0, %2\n\ts_nop 0\n\tglobal_load_lds_dwordx4 %0, %1" :: "v"(voff), "s"(sbase), "s"(lds_base) : "memory");
}
DI void glds16x2(const void* sbase, unsigned voff0, unsigned voff1, unsigned lds_base) {
    asm volatile("s_mov_b32 m0, %3\n\ts_nop 0\n\tglobal_load_lds_dwordx4 %0, %2\n\tglobal_load_lds_dwordx4 %1, %2 offset:1024" :: "v"(voff0), "v"(voff1), "s"(sbase), "s"(lds_base) : "memory");
}
DI void glds16x4(const void* sbase, unsigned v0, unsigned v1, unsigned v2, unsigned v3, unsigned lds_base) {
    asm volatile("s_mov_b32 m0, %5\n\ts_nop 0\n\tglobal_load_lds_dwordx4 %0, %4\n\tglobal_load_lds_dwordx4 %1, %4 offset:1024\n\t"
                 "global_load_lds_dwordx4 %2, %4 offset:2048\n\tglobal_load_lds_dwordx4 %3, %4 offset:3072" :: "v"(v0), "v"(v1), "v"(v2), "v"(v3), "s"(sbase), "s"(lds_base) : "memory");
}
DI void glds16x2_sc1(const void* sbase, unsigned voff, unsigned lds_base) {
    asm volatile("s_mov_b32 m0, %2\n\ts_nop 0\n\tglobal_load_lds_dwordx4 %0, %1 sc1\n\tglobal_load_lds_dwordx4 %0, %1 offset:1024 sc1" :: "v"(voff), "s"(sbase), "s"(lds_base) : "memory");
}
DI void touch8k(const void* sbase, int lane, unsigned lds_scratch) {
    asm volatile("s_mov_b32 m0, %2\n\ts_nop 0\n\tglobal_load_lds_dword %0, %1" :: "v"(lane * 128), "s"(sbase), "s"(lds_scratch) : "memory");
}
#define WAIT_V(N) asm volatile("s_waitcnt vmcnt(" #N ")" ::: "memory")
DI __amdgpu_buffer_rsrc_t mk_rsrc(const void* p, unsigned bytes) { return __builtin_amdgcn_make_buffer_rsrc((void*)p, 0, bytes, 0x00020000); }
DI u32x4 bload(__amdgpu_buffer_rsrc_t r, unsigned voff, unsigned soff) { return __builtin_amdgcn_raw_buffer_load_b128(r, voff, soff, 0); }
DI u32x4 bload_sc1(__amdgpu_buffer_rsrc_t r, unsigned voff, unsigned soff) { return __builtin_amdgcn_raw_buffer_load_b128(r, voff, soff, 16); }
DI void bstore_sc1(u32x4 v, __amdgpu_buffer_rsrc_t r, unsigned voff) { __builtin_amdgcn_raw_buffer_store_b128(v, r, voff, 0, 16); }

#define STAMP(i) do { } while (0)

struct BigFrag { s16x8 x0, x1, w0, w1; };
struct SmallFrag { s16x8 x, q; };

__global__ void __launch_bounds__(512, 2)
fused_kernel(const uint16_t* __restrict__ x, const uint16_t* __restrict__ wq, const uint16_t* __restrict__ wk, const uint16_t* __restrict__ wv,
             const uint16_t* __restrict__ wo, const uint16_t* __restrict__ w1, const uint16_t* __restrict__ w2, const uint16_t* __restrict__ gw,
             uint16_t* out, char* ws) {
    __shared__ __attribute__((aligned(16))) char smem[LDS_BYTES];
    const lptr lds = (lptr)smem;
    const unsigned lds0 = (unsigned)(uintptr_t)smem;
    const int tid = threadIdx.x, lane = tid & 63, w = __builtin_amdgcn_readfirstlane(tid >> 6);
    const int r = lane & 31, h = lane >> 5, g1 = (lane >> 4) & 1, q4 = (lane & 15) >> 2, p4 = lane & 3;
    const int b = blockIdx.x, bi = b >> 3;
    const int R = 2 * (b & 7) + (bi >> 4), cb = bi & 15, rh64 = cb & 1, hg = cb >> 1;
    const bool kvcu = cb >= 14, big = w < 4;
    const int row64 = R * 128 + rh64 * 64;
    const int rt = w & 1, hq = w >> 1, fh = hq & 1;
    unsigned stv = 0;
    STAMP(0);

    asm volatile("" :: "s"(x), "s"(wq), "s"(wk), "s"(wv), "s"(wo), "s"(w1), "s"(w2), "s"(gw), "s"(out), "s"(ws));
    const unsigned w64_src = (unsigned)((lane >> 3) * 1024 + hg * 128 + (((lane & 7) ^ (g1 << 2)) << 4));
    const unsigned w64_src1 = w64_src + 8192 - 1024;
    const unsigned xv = (unsigned)((lane >> 3) * 1024 + (((lane & 7) ^ ((lane >> 4) & 3)) << 4)), xv1 = (xv ^ 64) + 8192 - 1024;
    const unsigned wv0 = (unsigned)((lane >> 4) * (DFF * 2) + cb * 256 + (((lane & 15) ^ (((lane >> 4) & 3) << 2)) << 4)), wv1 = wv0 + 4 * DFF * 2 - 1024;
    const int rot = (b + cb) & 7;
    if (!kvcu) {
#pragma unroll
        for (int s = 0; s < 2; ++s) {
            const int sr = (s + rot) & 7;
            glds16x2((const char*)x + R * 131072 + sr * 128 + w * 16384, xv, xv1, lds0 + s * L_STAGE + w * 2048);
            glds16x2((const char*)w1 + (size_t)(sr * 64 + w * 8) * (DFF * 2), wv0, wv1, lds0 + s * L_STAGE + 16384 + w * 2048);
            glds16((const char*)wq + sr * 65536 + w * 8192, w64_src, lds0 + s * L_STAGE + 32768 + w * 1024);
        }
    } else {
        const char* wkv = (const char*)((w & 2) ? wk : wv);
        const unsigned kvv = (w & 1) * 1024 + ((lane ^ ((w & 2) << 2)) << 4);
#pragma unroll 1
        for (int s = 0; s < 8; ++s) {
            glds16((const char*)x + (row64 + 8 * w) * 1024 + s * 128, xv ^ (rt << 6), lds0 + s * L_KSTAGE + w * 1024);
            if (!big) glds16(wkv + s * 2048, kvv, lds0 + s * L_KSTAGE + 8192 + (w & 3) * 1024);
        }
    }
    const unsigned eo = lane == 1 ? WS_HFLAG + (R * 16 + cb) * 128 : !kvcu || lane == 0 || lane > 6 ? WS_EPOCH : lane == 2 ? WS_KVFLAG + (row64 >> 6) * 128 : WS_GZ + row64 * 8 + (lane - 3) * 128;
    const unsigned epoch_v = __hip_atomic_load((gu32*)(ws + eo), RLX_AGENT);
    asm volatile("" ::: "memory");
    const __amdgpu_buffer_rsrc_t rs_kv = mk_rsrc(ws + WS_KV, KV_BYTES), rs_h = mk_rsrc(ws + WS_H, H_BYTES), rs_o = mk_rsrc(ws + WS_O, O_BYTES);
    const __amdgpu_buffer_rsrc_t rs_kf = mk_rsrc(ws + WS_KVFLAG, 4096);
    const unsigned tr_in = 32 * g1 + 8 * p4;
    const unsigned wq_rd = (8 * h + q4) * 128 + ((fh ^ (q4 >> 1)) << 6) + tr_in;
    unsigned xo[4];
#pragma unroll
    for (int ks = 0; ks < 4; ++ks) xo[ks] = r * 128 + (((2 * ks + h) ^ ((r >> 1) & 7)) << 4);

    lptr kp, vp0, vp1, wrA0, wrA1, wrB0, wrB1, q_rd, qn_rd, km_rd, cst_wr;
    unsigned h_voff, hf_voff, g_voff0, g_voff1, hb, hb1, o_own;
    int spidx;
    f32x16 oacc;
    u32x4 cst, omark;
    s16x8 zf;
    auto consts = [&]() {
        kp = lds + lane * 16;
        const unsigned v_cst = (r & 27) != 16 ? 2064 : 2048;
        vp0 = lds + (r < 16 ? 1024 + h * 256 + r * 16 : v_cst); vp1 = lds + (r < 16 ? 1536 + h * 256 + r * 16 : v_cst);
        const unsigned w_rd0 = w * 2048 + (4 * h + q4) * 128 + ((q4 >> 1) << 6) + tr_in;
        wrA0 = lds + L_WB + w_rd0; wrA1 = lds + L_WB + (w_rd0 ^ 64); wrB0 = lds + w_rd0; wrB1 = lds + (w_rd0 ^ 64);
        q_rd = lds + L_QX + (hq * 2 + rt) * 1024 + lane * 16; qn_rd = lds + L_QN + (hq * 2 + rt) * 128 + r * 4; km_rd = lds + L_KM;
        h_voff = (unsigned)((row64 >> 5) * 112 + 14 * w) * 1024u + lane * 16;
        hf_voff = (unsigned)(R * 16 + (lane & 15)) * 128 + 4; g_voff0 = 32768 + (row64 + 32 * rt + r) * 8; g_voff1 = 32768 + (row64 + 8 * w + ((lane >> 1) & 4) + (lane >> 4)) * 8 + 4;
        spidx = 0xDDDD; oacc = (f32x16){}; zf = (s16x8){};
        cst = (lane & 1) ? (u32x4){} : (u32x4){0x3f803f80u, 0x3f803f80u, 0x3f803f80u, 0x3f803f80u};
        cst_wr = lds + (tid >> 1) * KVT + 2048 + (tid & 1) * 16;
        hb = (kvcu ? OOB : (unsigned)((R * 4 + 2 * rt) * 112 + cb * 8 + fh * 4) * 1024u) + lane * 16; hb1 = hb + 112 * 1024;
        o_own = (unsigned)(((row64 >> 5) + rt) * 32 + hg * 4 + hq) * 1024u + lane * 16;
        omark = (u32x4){~0u, ~0u, ~0u, ~0u};
        PIN(oacc); PIN(spidx); PIN(cst); PIN(cst_wr); PIN(hb); PIN(hb1); PIN(zf); PIN(o_own); PIN(omark);
        PIN(kp); PIN(vp0); PIN(vp1); PIN(wrA0); PIN(wrA1); PIN(wrB0); PIN(wrB1); PIN(q_rd); PIN(qn_rd); PIN(km_rd);
        PIN(h_voff); PIN(hf_voff); PIN(g_voff0); PIN(g_voff1);
        SBAR();
    };
    u32x4 hs[8];
#pragma unroll
    for (int i = 0; i < 8; ++i) asm volatile("" : "=v"(hs[i]));
    f32x16 a00 = {}, a01 = {}, a10 = {}, a11 = {};
    auto kv_wait = [&](u32x4& fv) {
        const unsigned ep = __builtin_amdgcn_readfirstlane(epoch_v) + 1u;
        if (__builtin_expect(!__all(fv[1] == ep && fv[3] == ep), 0)) {
            unsigned spins = 0;
            do { __builtin_amdgcn_s_sleep(6); CFENCE(); fv = bload_sc1(rs_kf, r * 128 + h * 16, 0); } while (!__all(fv[1] == ep && fv[3] == ep) && ++spins < SPIN_CAP);
        }
        CFENCE();
    };
    auto kv_bound = [&](const u32x4& fv) {
        const float km = row32_max(fmaxf(__uint_as_float(fv[0]), __uint_as_float(fv[2])));
        if (tid == 256) *(LAS float*)(lds + L_KM) = __builtin_amdgcn_sqrtf(km) * 1.0001f;
    };
    auto kv_tile = [&](int t) { glds16x2_sc1(ws + WS_KV + ((t + 8 * bi) & 63) * 2048, lane * 16, lds0 + t * KVT); };
    if (!kvcu) {
        unsigned cur = 0, nxt = L_STAGE;
        if (big) {
            const unsigned xb = rt * 8192, w0o = 16384 + (8 * h + q4) * 256 + (((2 * fh) ^ q4) << 6) + tr_in, w1o = w0o ^ 64;
            BigFrag F[4];
            auto rd = [&](BigFrag& n, lptr sb, int ks) {
                n.x0 = lds_frag(sb + xb + xo[ks]); n.x1 = lds_frag(sb + xb + 4096 + xo[ks]); n.w0 = tr_frag(sb + w0o + ks * 4096, 1024); n.w1 = tr_frag(sb + w1o + ks * 4096, 1024); };
#define AM(e, ...) do { if (ABL & 2048) asm volatile("" :: __VA_ARGS__); else e; SBAR(); } while (0)
#define AR(e, ...) do { if (ABL & 4096) asm volatile("" : __VA_ARGS__); else { e; } SBAR(); } while (0)
            auto step = [&](const BigFrag& c, BigFrag& n, lptr sb, int ks) {
                AM(a00 = MFMA(c.w0, c.x0, a00), "v"(c.w0), "v"(c.x0)); AR(n.x0 = lds_frag(sb + xb + xo[ks]); n.x1 = lds_frag(sb + xb + 4096 + xo[ks]), "+v"(n.x0), "+v"(n.x1));
                AM(a10 = MFMA(c.w0, c.x1, a10), "v"(c.x1)); AR(n.w0 = tr_frag(sb + w0o + ks * 4096, 1024), "+v"(n.w0));
                AM(a01 = MFMA(c.w1, c.x0, a01), "v"(c.w1)); AR(n.w1 = tr_frag(sb + w1o + ks * 4096, 1024), "+v"(n.w1));
                AM(a11 = MFMA(c.w1, c.x1, a11), "v"(c.w1)); };
            auto stage = [&](auto bar) {
                step(F[0], F[3], lds + cur, 3);
                bar();
                step(F[1], F[0], lds + nxt, 0); step(F[2], F[1], lds + nxt, 1); step(F[3], F[2], lds + nxt, 2);
                cur = nxt; nxt = (nxt == 2 * L_STAGE) ? 0 : nxt + L_STAGE; };
            consts();
            STAMP(13);
            BAR_VL(6);
            STAMP(14);
            rd(F[0], lds, 0); rd(F[1], lds, 1); rd(F[2], lds, 2);
#pragma unroll 1
            for (int s = 0; s < 7; ++s) stage([&] {
                BAR_VL(1);
                if (s == 3 && w < 2) touch8k(w == 0 ? (const char*)w2 + (R * 14 + cb) * 8192 : (const char*)wo + (b & 63) * 8192, lane, lds0 + L_TOUCH); });
            STAMP(1);
            rd(F[3], lds + cur, 3);
#define LAST4(acc, wm, xm) do { _Pragma("unroll") for (int ks = 0; ks < 4; ++ks) acc = MFMA(F[ks].wm, F[ks].xm, acc); } while (0)
#define LAST4P(acc, wm, xm, prev, i) do { acc = MFMA(F[0].wm, F[0].xm, acc); acc = MFMA(F[1].wm, F[1].xm, acc); SBAR(); hs[i] = pack8_relu<0>(prev); PIN(hs[i]); SBAR(); \
            acc = MFMA(F[2].wm, F[2].xm, acc); SBAR(); hs[(i) + 1] = pack8_relu<1>(prev); PIN(hs[(i) + 1]); SBAR(); acc = MFMA(F[3].wm, F[3].xm, acc); SBAR(); } while (0)
            LAST4(a00, w0, x0); SBAR();
            LAST4P(a01, w1, x0, a00, 0); LAST4P(a10, w0, x1, a01, 2); LAST4P(a11, w1, x1, a10, 4);
            hs[6] = pack8_relu<0>(a11); hs[7] = pack8_relu<1>(a11);
        } else {
            const int j = w - 4;
            __builtin_amdgcn_s_setprio(2);
            const unsigned xb = (2 * rh64 + rt) * 4096, qo = 32768 + wq_rd;
            SmallFrag F[4];
            auto rd = [&](SmallFrag& n, lptr sb, int ks) { n.x = lds_frag(sb + xb + xo[ks]); n.q = tr_frag(sb + qo + ks * 2048, 512); SBAR(); };
            auto mma = [&](const SmallFrag& c) { a00 = MFMA(c.q, c.x, a00); SBAR(); };
            const unsigned xv2 = xv + 2 * (8192 - 1024), xv3 = xv1 + 2 * (8192 - 1024), wv2 = wv0 + 2 * (4 * DFF * 2 - 1024), wv3 = wv1 + 2 * (4 * DFF * 2 - 1024);
            const char* xs = (const char*)x + R * 131072 + j * 32768;
            const char* w1s = (const char*)w1 + (size_t)(j * 16) * (DFF * 2);
            const char* wqs = (const char*)wq + j * 16384;
            auto fill0 = [&](int sr, unsigned slot) { glds16x4(xs + sr * 128, xv, xv1, xv2, xv3, lds0 + slot + j * 4096); };
            auto fill1 = [&](int sr, unsigned slot) { glds16x4(w1s + sr * (64 * DFF * 2), wv0, wv1, wv2, wv3, lds0 + slot + 16384 + j * 4096); };
            auto fill2 = [&](int sr, unsigned slot) { glds16x2(wqs + sr * 65536, w64_src, w64_src1, lds0 + slot + 32768 + j * 2048); };
            fill0((rot + 2) & 7, 2 * L_STAGE); fill1((rot + 2) & 7, 2 * L_STAGE); fill2((rot + 2) & 7, 2 * L_STAGE);
            consts();
            BAR_VL(16);
            rd(F[0], lds, 0); rd(F[1], lds, 1); rd(F[2], lds, 2);
#pragma unroll 1
            for (int s = 0; s < 6; ++s) {
                const bool more = s < 5;
                const int sr = (rot + s + 3) & 7;
                rd(F[3], lds + cur, 3); mma(F[0]);
                BAR_VL(10);
                if (more) fill0(sr, cur);
                rd(F[0], lds + nxt, 0); mma(F[1]);
                if (more) fill1(sr, cur);
                rd(F[1], lds + nxt, 1); mma(F[2]);
                if (more) fill2(sr, cur);
                rd(F[2], lds + nxt, 2); mma(F[3]);
                cur = nxt; nxt = (nxt == 2 * L_STAGE) ? 0 : nxt + L_STAGE;
            }
            u32x4 fv = bload_sc1(rs_kf, r * 128 + h * 16, 0);
            rd(F[3], lds, 3); mma(F[0]);
            BAR_VL(0);
            rd(F[0], lds + L_STAGE, 0); mma(F[1]);
            STAMP(15);
            kv_wait(fv);
            STAMP(16);
            kv_tile(j); kv_tile(4 + j);
            rd(F[1], lds + L_STAGE, 1); mma(F[2]);
            kv_tile(8 + j); kv_tile(12 + j);
            rd(F[2], lds + L_STAGE, 2); mma(F[3]); rd(F[3], lds + L_STAGE, 3);
            mma(F[0]); kv_bound(fv); mma(F[1]); mma(F[2]); mma(F[3]);
        }
    } else {
        const unsigned xb = rt * 4096, w64_src2 = w64_src + 2 * (8192 - 1024), w64_src3 = w64_src + 3 * (8192 - 1024);
        {
            const unsigned wkv_rd = 8192 + g1 * 2048 + (((8 * h + q4) * 32 + 8 * p4) ^ (g1 << 7));
#pragma unroll
            for (int g = 0; g < 2; ++g) {
                if (g == 0) { if (big) BAR_VL(5); else BAR_VL(9); } else BAR_VL(1);
                const lptr st = lds + (g * 4 + hq) * L_KSTAGE;
#pragma unroll
                for (int k = 0; k < 4; ++k) a00 = MFMA(tr_frag2(st + wkv_rd + k * 512, st + (wkv_rd ^ 128) + k * 512), lds_frag(st + xb + xo[k]), a00);
            }
        }
        BAR_L();
        {
            const lptr pw = lds + w * L_KSTAGE + 8192 + r * 128;
            const unsigned sz = (r & 7) ^ ((r >> 1) & 4);
#pragma unroll
            for (int i = 0; i < 4; ++i) *(LAS f32x4*)(pw + (((2 * i + h) ^ sz) << 4)) = (f32x4){a00[4 * i], a00[4 * i + 1], a00[4 * i + 2], a00[4 * i + 3]};
            a00 = (f32x16){};
        }
        BAR_L();
        if (big) {
            STAMP(1);
            const unsigned kvo = (unsigned)((row64 >> 5) + rt) * 2048u + lane * 16;
            const lptr pr = lds + rt * L_KSTAGE + 8192;
            float n2 = 0.f;
            if (fh == 0) {
                const unsigned sz = (r & 7) ^ ((r >> 1) & 4);
                f32x4 lo = {}, hi = {};
#pragma unroll
                for (int i = 0; i < 4; ++i) { lo += *(const LAS f32x4*)(pr + i * 2 * L_KSTAGE + r * 128 + (((4 + h) ^ sz) << 4)); hi += *(const LAS f32x4*)(pr + i * 2 * L_KSTAGE + r * 128 + (((6 + h) ^ sz) << 4)); }
                const u32x4 v = {cvtpk(lo[0], lo[1]), cvtpk(lo[2], lo[3]), cvtpk(hi[0], hi[1]), cvtpk(hi[2], hi[3])};
                bstore_sc1(v, rs_kv, kvo);
                n2 = row32_max(swap_add(dot2(v[0], v[0], dot2(v[1], v[1], 0.f)) + dot2(v[2], v[2], dot2(v[3], v[3], 0.f))));
            } else {
                float t[8];
#pragma unroll
                for (int j = 0; j < 8; ++j) {
                    const lptr q = pr + ((lane >> 4) * 8 + j) * 128 + ((q4 ^ j ^ (g1 << 2)) << 4) + p4 * 4;
                    t[j] = (*(const LAS float*)q + *(const LAS float*)(q + 2 * L_KSTAGE)) + (*(const LAS float*)(q + 4 * L_KSTAGE) + *(const LAS float*)(q + 6 * L_KSTAGE));
                }
                bstore_sc1((u32x4){cvtpk(t[0], t[1]), cvtpk(t[2], t[3]), cvtpk(t[4], t[5]), cvtpk(t[6], t[7])}, rs_kv, kvo + 1024);
            }
            STAMP(13);
            WAIT_V(0);
            STAMP(14);
            if (lane == 0) __hip_atomic_store((gu64*)(ws + WS_KVFLAG) + (row64 >> 6) * 16 + w, ((unsigned long long)(epoch_v + 1u) << 32) | __float_as_uint(n2), RLX_AGENT);
            u32x4 gf[16];
#pragma unroll
            for (int i = 0; i < 16; ++i) gf[i] = *(const u32x4*)(gw + i * 32 + (lane >> 4) * 8);
            consts();
            BAR_L();
            f32x4 z = {};
            const int zr = 16 * w + (lane & 15);
#pragma unroll
            for (int i = 0; i < 16; ++i)
                z = __builtin_amdgcn_mfma_f32_16x16x32_bf16(asfrag(gf[i]), lds_frag(lds + (i >> 1) * L_KSTAGE + zr * 128 + (((4 * (i & 1) + (lane >> 4)) ^ ((zr >> 1) & 7)) << 4)), z, 0, 0, 0);
            const float gt = rbf(__builtin_amdgcn_rcpf(1.f + __expf(-rbf(z[0])))), og = fmaxf(rbf(1.f - gt), 0x1p-40f);
            if (lane < 16) __hip_atomic_store((gu64*)(ws + WS_GZ) + row64 + zr, ((unsigned long long)__float_as_uint(og) << 32) | __float_as_uint(gt / og), RLX_AGENT);
            BAR_L();
        } else {
#pragma unroll
            for (int i = 0; i < 4; ++i) { const int p = i * 16 + (w - 4) * 4; glds16x4((const char*)wq + p * 8192, w64_src, w64_src1, w64_src2, w64_src3, lds0 + L_KWQ + p * 1024); }
            consts();
            BAR_VL(0);
            SmallFrag F[4];
            auto rd = [&](SmallFrag& n, int s, int k) { n.x = lds_frag(lds + s * L_KSTAGE + xb + xo[k]); n.q = tr_frag(lds + L_KWQ + s * 8192 + wq_rd + k * 2048, 512); SBAR(); };
#pragma unroll
            for (int k = 0; k < 4; ++k) rd(F[k], 0, k);
#pragma unroll 1
            for (int s = 1; s < 8; ++s) {
#pragma unroll
                for (int k = 0; k < 4; ++k) { a00 = MFMA(F[k].q, F[k].x, a00); SBAR(); rd(F[k], s, k); }
            }
            u32x4 fv = bload_sc1(rs_kf, r * 128 + h * 16, 0);
#pragma unroll
            for (int k = 0; k < 4; ++k) a00 = MFMA(F[k].q, F[k].x, a00);
            BAR_L();
            STAMP(15);
            kv_wait(fv);
            STAMP(16);
            kv_tile(w - 4); kv_tile(w); kv_tile(4 + w); kv_tile(8 + w);
            kv_bound(fv);
        }
    }
    STAMP(2);
    if (big) {
        if (tid < 32) lds_put(cst_wr, cst);
    } else {
#pragma unroll
        for (int s = 0; s < 2; ++s) {
            const u32x4 v = s ? pack8<1>(a00, QSCALE) : pack8<0>(a00, QSCALE);
            lds_put(lds + L_QX + (4 * fh + 2 * s + rt) * 1024 + lane * 16, v);
            const float n2 = swap_add(dot2(v[0], v[0], dot2(v[1], v[1], 0.f)) + dot2(v[2], v[2], dot2(v[3], v[3], 0.f)));
            *(LAS float*)(lds + L_QN + (4 * fh + 2 * s + rt) * 128 + r * 4) = __builtin_amdgcn_sqrtf(n2);
        }
    }
    __builtin_amdgcn_s_setprio(3);
    BAR_VL(0);
    STAMP(3);
    const s16x8 qf = lds_frag(q_rd);
    s16x8 kf = lds_frag(kp), vf0, vf1;
    float mshift = *(const LAS float*)qn_rd * *(const LAS float*)km_rd;
    vf0 = lds_frag(vp0);
    if (__builtin_expect(!__all(mshift <= 0.5f * SAFE_RANGE), 0)) {
        float m = -__builtin_inff();
#pragma unroll 1
        for (int t = 0; t < 64; ++t) {
            const f32x16 s = MFMA(asfrag(bload_sc1(rs_kv, lane * 16, t * 2048)), qf, (f32x16){});
#pragma unroll
            for (int i = 0; i < 16; ++i) m = fmaxf(m, s[i]);
        }
        mshift = swap_max(m);
    }
    f32x16 cinit;
#pragma unroll
    for (int i = 0; i < 16; ++i) cinit[i] = -mshift;
    PIN(cinit);
    f32x16 sA = MFMA(kf, qf, cinit), sB;
    kf = lds_frag(kp + KVT);
    if (tid >= 32 && tid < 128) lds_put(cst_wr, cst);

#define S1(S_, i) S_[i] = __builtin_amdgcn_exp2f(S_[i])
#define EX2(S_, i) do { S1(S_, i); S1(S_, (i) + 1); } while (0)
#define EX3(S_, i) do { S1(S_, i); S1(S_, (i) + 1); S1(S_, (i) + 2); } while (0)
#define EX4(S_, i) do { EX2(S_, i); EX2(S_, (i) + 2); } while (0)
#define PV1(SN, T) do { if ((T) >= 0) oacc = pv_mma<1>(vf1, SN, oacc, spidx); vf1 = lds_frag(vp1 + ((T) < 0 ? 0 : (T)) * KVT); SBAR(); } while (0)
#define QK(SN, T) do { SN = MFMA(kf, qf, cinit); kf = lds_frag(kp + ((T) + 2) * KVT); SBAR(); } while (0)
#define PV0(SC, T) do { oacc = pv_mma<0>(vf0, SC, oacc, spidx); vf0 = lds_frag(vp0 + ((T) + 1) * KVT); SBAR(); } while (0)
#define CUT(S_) do { PIN(S_); SBAR(); } while (0)
#define ITER0(SC, SN, T, T1, PR, MEM) do { PR; \
    EX2(SC, 0); CUT(SC); PV1(SN, T1); EX4(SC, 2); CUT(SC); QK(SN, T); EX4(SC, 6); S1(SC, 10); CUT(SC); PV0(SC, T); MEM; S1(SC, 11); EX4(SC, 12); CUT(SC); } while (0)
#define ITER1(SC, SN, T, PR, PRE, MEM, G1) do { PR; PRE; \
    EX2(SC, 0); CUT(SC); PV1(SN, T); EX4(SC, 2); CUT(SC); QK(SN, T); EX4(SC, 6); CUT(SC); PV0(SC, T); MEM; EX4(SC, 10); CUT(SC); G1; SBAR(); EX2(SC, 14); CUT(SC); } while (0)
#define ITERG(SC, SN, T, PR, MEM, G0, G1) do { PR; \
    EX2(SC, 0); CUT(SC); PV1(SN, T); EX3(SC, 2); CUT(SC); QK(SN, T); EX3(SC, 5); CUT(SC); G0; SBAR(); EX3(SC, 8); CUT(SC); PV0(SC, T); MEM; EX3(SC, 11); CUT(SC); G1; SBAR(); EX2(SC, 14); CUT(SC); } while (0)
#define NOP (void)0
#define PRIO(p) __builtin_amdgcn_s_setprio(p)
#define ITER2(T, P0, P1, M0, M1) do { ITER0(sA, sB, T, T, PRIO(P0), M0); ITER0(sB, sA, (T) + 1, (T) + 1, PRIO(P1), M1); } while (0)
#define ADV(n) do { kp += (n) * KVT; vp0 += (n) * KVT; vp1 += (n) * KVT; PIN(kp); PIN(vp0); PIN(vp1); } while (0)
    const char* const w2w = (const char*)w2 + w * (14 * 16384);
    const char* const wow = (const char*)wo + w * 65536;
    int pn = (row64 >> 6) % 7;
    const unsigned wbA = lds0 + L_WB + w * 2048, wbB = lds0 + w * 2048;
    const __amdgpu_buffer_rsrc_t rs_f = mk_rsrc(ws + WS_HFLAG, 32768 + 16384);
    u32x4 hA0, hA1, hB0, hB1;
    s16x8 wf0, wf1;
#define W_LD(src, buf) glds16x2(src, w64_src, w64_src1, buf)
    STAMP(4);
#define H_ST(i) do { if (big) bstore_sc1(hs[i], rs_h, ((i) < 4 ? hb : hb1) + ((i) & 3) * 1024); } while (0)
    ITER0(sA, sB, 0, -1, PRIO(3), H_ST(0)); ITER0(sB, sA, 1, 1, NOP, H_ST(1); kv_tile(16 + w));
    ITER2(2, 3, 3, H_ST(2); kv_tile(24 + w), H_ST(3); kv_tile(32 + w));
    ITER2(4, 2, 2, H_ST(4); kv_tile(40 + w), H_ST(5); kv_tile(48 + w));
    ITER2(6, 2, 2, H_ST(6); kv_tile(56 + w), H_ST(7); W_LD(w2w + pn * 32768, wbA));
    STAMP(17);
    ITER2(8, 1, 1, bstore_sc1(omark, rs_o, o_own), NOP);
    ITER2(10, 1, 1, NOP, NOP);
    ITER2(12, 0, 0, NOP, NOP);
    STAMP(18);
    STAMP(19);
    BAR_VL(0);
    unsigned epoch_w = epoch_v; PIN(epoch_w);
    const unsigned epoch = __builtin_amdgcn_readfirstlane(epoch_w) + 1u;
    if (tid == 0) __hip_atomic_store((gu32*)(ws + WS_HFLAG) + (R * 16 + cb) * 32 + 1, epoch, RLX_AGENT);
    STAMP(5);
    unsigned hfv;
    ITER2(14, 1, 3, W_LD(w2w + pn * 32768 + 16384, wbB); a00 = MFMA(zf, zf, (f32x16){}); PIN(zf); a01 = MFMA(zf, zf, (f32x16){}), PIN(zf); a10 = MFMA(zf, zf, (f32x16){}); PIN(zf); a11 = MFMA(zf, zf, (f32x16){}));
    ITER2(16, 2, 1, NOP, hfv = __builtin_amdgcn_raw_buffer_load_b32(rs_f, hf_voff, 0, 16));
    ITER0(sA, sB, 18, 18, PRIO(3), NOP);
    STAMP(6);
    if (__builtin_expect(!__all(hfv == epoch), 0)) {
        unsigned spins = 0;
        do { __builtin_amdgcn_s_sleep(6); CFENCE(); hfv = __builtin_amdgcn_raw_buffer_load_b32(rs_f, hf_voff, 0, 16); } while (!__all(hfv == epoch) && ++spins < SPIN_CAP);
    }
    CFENCE();
    STAMP(7);
    unsigned gr_u, og_u;
    ITER0(sB, sA, 19, 19, PRIO(2), gr_u = __builtin_amdgcn_raw_buffer_load_b32(rs_f, g_voff0, 0, 16); og_u = __builtin_amdgcn_raw_buffer_load_b32(rs_f, g_voff1, 0, 16);
          hA0 = bload_sc1(rs_h, h_voff, pn * 2048); hA1 = bload_sc1(rs_h, h_voff, pn * 2048 + 112 * 1024));
    ITER2(20, 1, 3, hB0 = bload_sc1(rs_h, h_voff, pn * 2048 + 1024), hB1 = bload_sc1(rs_h, h_voff, pn * 2048 + 113 * 1024));
    ADV(22);
    PIN(a00); PIN(a01); PIN(a10); PIN(a11);
    STAMP(8);
#define GM(acc, wf, H) acc = MFMA(asfrag(H), wf, acc); SBAR()
#define GSTEP(SX, SY, T, R0, R1, H0, H1, LW, LH0, LH1) do { \
    ITER1(SX, SY, T, PRIO(3), WAIT_V(5); wf0 = tr_frag(R0, 1024); wf1 = tr_frag(R1, 1024), NOP, GM(a00, wf0, H0)); \
    ITERG(SY, SX, (T) + 1, PRIO(2), LW, GM(a01, wf1, H0); LH0, GM(a10, wf0, H1)); \
    ITER1(SX, SY, (T) + 2, PRIO(1), NOP, NOP, GM(a11, wf1, H1); LH1); } while (0)
#pragma unroll 1
    for (int n = 0; n < 4; ++n) {
        pn = pn == 6 ? 0 : pn + 1;
        const char* wsrc = w2w + pn * 32768;
        GSTEP(sA, sB, 0, wrA0, wrA1, hA0, hA1, W_LD(wsrc, wbA), hA0 = bload_sc1(rs_h, h_voff, pn * 2048), hA1 = bload_sc1(rs_h, h_voff, pn * 2048 + 112 * 1024));
        GSTEP(sB, sA, 3, wrB0, wrB1, hB0, hB1, W_LD(wsrc + 16384, wbB), hB0 = bload_sc1(rs_h, h_voff, pn * 2048 + 1024), hB1 = bload_sc1(rs_h, h_voff, pn * 2048 + 113 * 1024));
        ADV(6);
    }
    BAR_L();
    pn = pn == 6 ? 0 : pn + 1;
    const int pm = pn == 6 ? 0 : pn + 1;
    u32x4 hC0, hC1, hD0, hD1, hE0, hE1, hF0, hF1;
    ITER2(0, 3, 2, W_LD(w2w + pn * 32768, wbB + 16384); hC0 = bload_sc1(rs_h, h_voff, pn * 2048), hC1 = bload_sc1(rs_h, h_voff, pn * 2048 + 112 * 1024));
    ITER2(2, 1, 3, W_LD(w2w + pn * 32768 + 16384, wbB + 32768); hD0 = bload_sc1(rs_h, h_voff, pn * 2048 + 1024), hD1 = bload_sc1(rs_h, h_voff, pn * 2048 + 113 * 1024));
    ITER2(4, 2, 1, W_LD(w2w + pm * 32768, wbB + 49152); hE0 = bload_sc1(rs_h, h_voff, pm * 2048), hE1 = bload_sc1(rs_h, h_voff, pm * 2048 + 112 * 1024));
    ITER2(6, 3, 2, W_LD(w2w + pm * 32768 + 16384, wbB + 65536); hF0 = bload_sc1(rs_h, h_voff, pm * 2048 + 1024), hF1 = bload_sc1(rs_h, h_voff, pm * 2048 + 113 * 1024));
    const bool closer = b == 112 && w == 7;
    unsigned ef[4];
    auto ef_load = [&]() {
#pragma unroll
        for (int i = 0; i < 4; ++i) ef[i] = __builtin_amdgcn_raw_buffer_load_b32(mk_rsrc(ws + WS_HFLAG + i * 8192 + 4, closer ? 8188 : 0), lane * 128, 0, 16);
    };
    const unsigned xtra = w < 3 ? 5 * 16384 : L_QX - 3 * 2048;
    s16x8 wf2, wf3, wf4, wf5;
    ITER2(8, 1, 3, WAIT_V(14); wf0 = tr_frag(wrA0, 1024); wf1 = tr_frag(wrA1, 1024); W_LD(wow + 49152, wbB + xtra), wf2 = tr_frag(wrB0, 1024); wf3 = tr_frag(wrB1, 1024); ef_load());
    ITER2(10, 2, 1, wf4 = tr_frag(wrB0 + 16384, 1024); wf5 = tr_frag(wrB1 + 16384, 1024); PIN(wf0); PIN(wf1); W_LD(wow, wbA), PIN(wf2); PIN(wf3); W_LD(wow + 16384, wbB));
    ITER2(12, 3, 3, PIN(wf4); PIN(wf5); W_LD(wow + 32768, wbB + 16384), NOP);
    ITER2(14, 2, 2, NOP, NOP);
    ITER2(16, 1, 0, NOP, NOP);
    STAMP(9);
#define GMA(acc, wf, H) asm volatile("v_mfma_f32_32x32x16_bf16 %0, %1, %2, %0" : "+v"(acc) : "v"(H), "v"(wf))
    PRIO(3);
    oacc = pv_mma<1>(vf1, sB, oacc, spidx);
    {
        u32x4 v = pack8<0>(oacc, __builtin_amdgcn_rcpf(oacc[8]) * __uint_as_float(gr_u));
#pragma unroll
        for (int e = 0; e < 4; ++e) v[e] = v[e] < 0xfffffffeu ? v[e] : 0xfffffffeu;
        bstore_sc1(v, rs_o, o_own);
        asm volatile("s_nop 1");
    }
    PRIO(0);
    GMA(a00, wf0, hA0); GMA(a01, wf1, hA0);
    WAIT_V(1);
    lptr wrE0 = wrB0 + 32768, wrE1 = wrB1 + 32768; PIN(wrE0); PIN(wrE1);
    const s16x8 wf6 = tr_frag(wrE0, 1024), wf7 = tr_frag(wrE1, 1024), wf8 = tr_frag(wrE0 + 16384, 1024), wf9 = tr_frag(wrE1 + 16384, 1024);
    SBAR();
    GMA(a10, wf0, hA1); GMA(a11, wf1, hA1);
    const s16x8 wfa = tr_frag(wrE0 + 32768, 1024), wfb = tr_frag(wrE1 + 32768, 1024);
    s16x8 wof[8];
    wof[0] = tr_frag(wrA0, 1024); wof[1] = tr_frag(wrA1, 1024);
    SBAR();
    GMA(a00, wf2, hB0); GMA(a01, wf3, hB0);
    wof[2] = tr_frag(wrB0, 1024); wof[3] = tr_frag(wrB1, 1024); wof[4] = tr_frag(wrB0 + 16384, 1024); wof[5] = tr_frag(wrB1 + 16384, 1024);
    SBAR();
    GMA(a10, wf2, hB1); GMA(a11, wf3, hB1);
    wof[6] = tr_frag(wrB0 + xtra, 1024); wof[7] = tr_frag(wrB1 + xtra, 1024);
    SBAR();
    GMA(a00, wf4, hC0); GMA(a01, wf5, hC0); GMA(a10, wf4, hC1); GMA(a11, wf5, hC1);
    const unsigned ob = (unsigned)((row64 >> 5) * 32 + 4 * w) * 1024u;
    u32x4 of[8];
    auto o_load = [&]() {
#pragma unroll
        for (int i = 0; i < 8; ++i) of[i] = bload_sc1(rs_o, lane * 16 + (i & 3) * 1024, ob + (i >> 2) * 32768);
    };
    auto o_ready = [&]() {
        unsigned m = 0;
#pragma unroll
        for (int i = 0; i < 8; ++i) { m = max(m, max(of[i][0], of[i][1])); m = max(m, max(of[i][2], of[i][3])); }
        return __all(m != ~0u);
    };
    BAR_VL(0);
#pragma unroll
    for (int i = 0; i < 8; ++i) PIN(wof[i]);
    STAMP(10);
    o_load();
    SBAR();
    GMA(a00, wf6, hD0); GMA(a01, wf7, hD0); GMA(a10, wf6, hD1); GMA(a11, wf7, hD1);
    GMA(a00, wf8, hE0); GMA(a01, wf9, hE0); GMA(a10, wf8, hE1); GMA(a11, wf9, hE1);
    GMA(a00, wfa, hF0); GMA(a01, wfb, hF0); GMA(a10, wfa, hF1); GMA(a11, wfb, hF1);
    SBAR();
    const lptr prb = lds + (((w >> 2) * 2 + ((lane >> 2) & 1)) * 16 + 4 * (w & 3) + (lane >> 4)) * 2048 + ((lane >> 3) & 1) * 128 + p4 * 32;
    lptr pr0 = prb + g1 * 16, pr1 = prb + (g1 ^ 1) * 16;
    PIN(pr0); PIN(pr1);
    uint16_t* outp = out + (row64 + 8 * w + ((lane >> 1) & 4) + (lane >> 4)) * 512 + hg * 64 + (lane & 7) * 8;
    PIN(outp);
    SBAR();
    if (!o_ready()) {
        unsigned spins = 0;
        do { CFENCE(); o_load(); } while (!o_ready() && ++spins < SPIN_CAP);
    }
    STAMP(11);
#define AMM(acc, wi, oi) GMA(acc, wof[wi], of[oi])
#define PUT(a, i, off) asm volatile("ds_write_addtid_b32 %0 offset:%4\n\tds_write_addtid_b32 %1 offset:%5\n\tds_write_addtid_b32 %2 offset:%6\n\tds_write_addtid_b32 %3 offset:%7" \
    :: "v"(a[4 * (i)]), "v"(a[4 * (i) + 1]), "v"(a[4 * (i) + 2]), "v"(a[4 * (i) + 3]), "n"((off) + (i) * 8192), "n"((off) + (i) * 8192 + 2048), "n"((off) + (i) * 8192 + 4096), "n"((off) + (i) * 8192 + 6144) : "memory")
#define SETM0(v, prev) asm volatile("s_mov_b32 m0, %1\n\ts_nop 10" : "+v"(prev) : "s"(v))
#define MM_PUT(acc, c, o, prev, m0v, off) do { AMM(acc, c, o); SETM0(m0v, prev); PUT(prev, 0, off); AMM(acc, 2 + (c), (o) + 1); PUT(prev, 1, off); \
    AMM(acc, 4 + (c), (o) + 2); PUT(prev, 2, off); AMM(acc, 6 + (c), (o) + 3); PUT(prev, 3, off); } while (0)
    const unsigned m0a = lds0 + w * 256, m0b = m0a + 63616;
    asm volatile("s_nop 1");
    AMM(a00, 0, 0); AMM(a00, 2, 1); AMM(a00, 4, 2); AMM(a00, 6, 3);
    MM_PUT(a01, 1, 0, a00, m0a, 0);
    MM_PUT(a10, 0, 4, a01, m0a, 32768);
    MM_PUT(a11, 1, 4, a10, m0b, 65536 - 63616);
    asm volatile("s_nop 7" : "+v"(a11));
    PUT(a11, 0, 98304 - 63616); PUT(a11, 1, 98304 - 63616); PUT(a11, 2, 98304 - 63616); PUT(a11, 3, 98304 - 63616);
    BAR_L();
    {
        f32x4 t[16];
#pragma unroll
        for (int k = 0; k < 8; ++k) { t[2 * k] = *(const LAS f32x4*)(pr0 + k * 256); t[2 * k + 1] = *(const LAS f32x4*)(pr1 + k * 256); }
        SBAR();
        f32x4 lo = t[0], hi = t[1];
#pragma unroll
        for (int k = 1; k < 8; ++k) { lo += t[2 * k]; hi += t[2 * k + 1]; }
        lo *= __uint_as_float(og_u); hi *= __uint_as_float(og_u);
        const unsigned l0 = cvtpk(lo[0], lo[1]), l1 = cvtpk(lo[2], lo[3]), h0 = cvtpk(hi[0], hi[1]), h1 = cvtpk(hi[2], hi[3]);
        const u32x4 v = g1 ? (u32x4){h0, h1, l0, l1} : (u32x4){l0, l1, h0, h1};
        *(GAS u32x4*)outp = v;
    }
    if (closer) {
        unsigned spins = 0;
        while (!__all(ef[0] == epoch && ef[1] == epoch && ef[2] == epoch && ef[3] == epoch) && ++spins < SPIN_CAP) { CFENCE(); ef_load(); }
        if (lane == 0) __hip_atomic_store((gu32*)(ws + WS_EPOCH), epoch, RLX_AGENT);
    }
    STAMP(12);
}

extern "C" void kernel_launch(void* const* d_in, const int* in_sizes, int n_in, void* d_out, int out_size, void* d_ws, size_t ws_size,
                              hipStream_t stream) {
    fused_kernel<<<256, 512, 0, stream>>>((const uint16_t*)d_in[0], (const uint16_t*)d_in[1], (const uint16_t*)d_in[2], (const uint16_t*)d_in[3],
                                          (const uint16_t*)d_in[4], (const uint16_t*)d_in[6], (const uint16_t*)d_in[7], (const uint16_t*)d_in[8],
                                          (uint16_t*)d_out, (char*)d_ws);
}
```

```cpp
#include <hip/hip_runtime.h>
#include <stdint.h>

typedef short s16x8 __attribute__((ext_vector_type(8)));
typedef short s16x4 __attribute__((ext_vector_type(4)));
typedef short s16x2 __attribute__((ext_vector_type(2)));
typedef float f32x16 __attribute__((ext_vector_type(16)));
typedef float f32x4 __attribute__((ext_vector_type(4)));
typedef float f32x2 __attribute__((ext_vector_type(2)));
typedef unsigned u32x4 __attribute__((ext_vector_type(4)));
typedef __bf16 bf16x2 __attribute__((ext_vector_type(2)));
typedef __bf16 bf16x8 __attribute__((ext_vector_type(8)));
typedef __bf16 bf16x16 __attribute__((ext_vector_type(16)));
#define LAS __attribute__((address_space(3)))
#define GAS __attribute__((address_space(1)))
typedef GAS unsigned gu32;
typedef GAS unsigned long long gu64;
typedef LAS char* lptr;
#define DI __device__ __forceinline__
#define RLX_AGENT __ATOMIC_RELAXED, __HIP_MEMORY_SCOPE_AGENT
#define MFMA(a, b, c) __builtin_amdgcn_mfma_f32_32x32x16_bf16((a), (b), (c), 0, 0, 0)
#define SBAR() __builtin_amdgcn_sched_barrier(0)
#define PIN(x) asm volatile("" : "+v"(x))
#define CFENCE() do { asm volatile("" ::: "memory"); __builtin_amdgcn_fence(__ATOMIC_ACQUIRE, "wavefront"); } while (0)
#define BAR_L() asm volatile("s_waitcnt lgkmcnt(0)\n\ts_barrier" ::: "memory")
#define BAR_VL(N) asm volatile("s_waitcnt vmcnt(" #N ") lgkmcnt(0)\n\ts_barrier" ::: "memory")

#define ABL 0
#define STW 0
#define STAMPS 0

constexpr int D = 512, DFF = 1792;
constexpr float QSCALE = 0.25f * 1.4426950408889634f;
constexpr float SAFE_RANGE = 64.f;
constexpr unsigned WS_EPOCH = 0, WS_KVFLAG = 4096, WS_HFLAG = 8192;
constexpr unsigned WS_GZ = WS_HFLAG + 32768;
constexpr unsigned WS_KV = WS_GZ + 16384, KV_BYTES = 64 * 2048;
constexpr unsigned WS_H = WS_KV + KV_BYTES, H_BYTES = 64 * 112 * 1024;
constexpr unsigned WS_O = WS_H + H_BYTES, O_BYTES = 64 * 32 * 1024;
constexpr unsigned WS_STAMP = WS_O + O_BYTES;
constexpr unsigned OOB = 0x7ff00000u;
constexpr unsigned L_STAGE = 40960;
constexpr unsigned L_KSTAGE = 12288, L_KWQ = 98304;
constexpr unsigned KVT = 2080;
constexpr unsigned L_WB = 64 * KVT;
constexpr unsigned L_QX = L_WB + 16384, L_QN = L_QX + 8192, L_KM = L_QN + 1024, L_TOUCH = L_KM + 256, LDS_BYTES = 163840;
static_assert(L_TOUCH + 256 <= LDS_BYTES, "lds map");
constexpr unsigned SPIN_CAP = 1u << 16;

DI unsigned cvtpk(float a, float b) { f32x2 v = {a, b}; return __builtin_bit_cast(unsigned, __builtin_convertvector(v, bf16x2)); }
DI float rbf(float x) { return __uint_as_float(cvtpk(x, x) << 16); }
DI s16x8 asfrag(u32x4 v) { return __builtin_bit_cast(s16x8, v); }
DI s16x8 lds_frag(lptr p) { return *(const LAS s16x8*)p; }
DI void lds_put(lptr p, u32x4 v) { *(LAS u32x4*)p = v; }
DI s16x4 lds_tr(lptr p) { return __builtin_amdgcn_ds_read_tr16_b64_v4i16((LAS s16x4*)p); }
DI s16x8 tr_frag2(lptr p, lptr q) { s16x4 lo = lds_tr(p), hi = lds_tr(q); return __builtin_shufflevector(lo, hi, 0, 1, 2, 3, 4, 5, 6, 7); }
DI s16x8 tr_frag(lptr p, int second) { return tr_frag2(p, p + second); }
DI float swap_add(float v) { auto r = __builtin_amdgcn_permlane32_swap(__float_as_uint(v), __float_as_uint(v), false, false); return __uint_as_float(r[0]) + __uint_as_float(r[1]); }
DI float swap_max(float v) { auto r = __builtin_amdgcn_permlane32_swap(__float_as_uint(v), __float_as_uint(v), false, false); return fmaxf(__uint_as_float(r[0]), __uint_as_float(r[1])); }
DI float dot2(unsigned a, unsigned b, float c) { return __builtin_amdgcn_fdot2_f32_bf16(__builtin_bit_cast(bf16x2, a), __builtin_bit_cast(bf16x2, b), c, false); }
DI unsigned relu_pk(unsigned u) { return __builtin_bit_cast(unsigned, __builtin_elementwise_max(__builtin_bit_cast(s16x2, u), (s16x2){0, 0})); }
template <int CTRL> DI float dpp(float v) { return __uint_as_float(__builtin_amdgcn_update_dpp(0, __float_as_uint(v), CTRL, 0xf, 0xf, true)); }
DI float row32_max(float v) {
    v = fmaxf(v, dpp<0xB1>(v)); v = fmaxf(v, dpp<0x4E>(v)); v = fmaxf(v, dpp<0x141>(v)); v = fmaxf(v, dpp<0x140>(v));
    auto r = __builtin_amdgcn_permlane16_swap(__float_as_uint(v), __float_as_uint(v), false, false);
    return fmaxf(__uint_as_float(r[0]), __uint_as_float(r[1]));
}
template <int HALF> DI u32x4 pack8(const f32x16& a, float m) {
    u32x4 v;
#pragma unroll
    for (int e = 0; e < 4; ++e) v[e] = cvtpk(a[8 * HALF + 2 * e] * m, a[8 * HALF + 2 * e + 1] * m);
    return v;
}
template <int HALF> DI u32x4 pack8_relu(const f32x16& a) {
    u32x4 v;
#pragma unroll
    for (int e = 0; e < 4; ++e) v[e] = relu_pk(cvtpk(a[8 * HALF + 2 * e], a[8 * HALF + 2 * e + 1]));
    return v;
}
template <int HALF> DI f32x16 pv_mma(s16x8 v, const f32x16& p, f32x16 c, int idx) {
    const auto ph = __builtin_shufflevector(p, p, 8 * HALF, 8 * HALF + 1, 8 * HALF + 2, 8 * HALF + 3, 8 * HALF + 4, 8 * HALF + 5, 8 * HALF + 6, 8 * HALF + 7);
    return __builtin_amdgcn_smfmac_f32_32x32x32_bf16(__builtin_bit_cast(bf16x8, v), __builtin_bit_cast(bf16x16, ph), c, idx, 0, 0);
}
DI void glds16(const void* sbase, unsigned voff, unsigned lds_base) {
    asm volatile("s_mov_b32 m0, %2\n\ts_nop 0\n\tglobal_load_lds_dwordx4 %0, %1" :: "v"(voff), "s"(sbase), "s"(lds_base) : "memory");
}
DI void glds16x2(const void* sbase, unsigned voff0, unsigned voff1, unsigned lds_base) {
    asm volatile("s_mov_b32 m0, %3\n\ts_nop 0\n\tglobal_load_lds_dwordx4 %0, %2\n\tglobal_load_lds_dwordx4 %1, %2 offset:1024" :: "v"(voff0), "v"(voff1), "s"(sbase), "s"(lds_base) : "memory");
}
DI void glds16x4(const void* sbase, unsigned v0, unsigned v1, unsigned v2, unsigned v3, unsigned lds_base) {
    asm volatile("s_mov_b32 m0, %5\n\ts_nop 0\n\tglobal_load_lds_dwordx4 %0, %4\n\tglobal_load_lds_dwordx4 %1, %4 offset:1024\n\t"
                 "global_load_lds_dwordx4 %2, %4 offset:2048\n\tglobal_load_lds_dwordx4 %3, %4 offset:3072" :: "v"(v0), "v"(v1), "v"(v2), "v"(v3), "s"(sbase), "s"(lds_base) : "memory");
}
DI void glds16x2_sc1(const void* sbase, unsigned voff, unsigned lds_base) {
    asm volatile("s_mov_b32 m0, %2\n\ts_nop 0\n\tglobal_load_lds_dwordx4 %0, %1 sc1\n\tglobal_load_lds_dwordx4 %0, %1 offset:1024 sc1" :: "v"(voff), "s"(sbase), "s"(lds_base) : "memory");
}
DI void touch8k(const void* sbase, int lane, unsigned lds_scratch) {
    asm volatile("s_mov_b32 m0, %2\n\ts_nop 0\n\tglobal_load_lds_dword %0, %1" :: "v"(lane * 128), "s"(sbase), "s"(lds_scratch) : "memory");
}
#define WAIT_V(N) asm volatile("s_waitcnt vmcnt(" #N ")" ::: "memory")
DI __amdgpu_buffer_rsrc_t mk_rsrc(const void* p, unsigned bytes) { return __builtin_amdgcn_make_buffer_rsrc((void*)p, 0, bytes, 0x00020000); }
DI u32x4 bload(__amdgpu_buffer_rsrc_t r, unsigned voff, unsigned soff) { return __builtin_amdgcn_raw_buffer_load_b128(r, voff, soff, 0); }
DI u32x4 bload_sc1(__amdgpu_buffer_rsrc_t r, unsigned voff, unsigned soff) { return __builtin_amdgcn_raw_buffer_load_b128(r, voff, soff, 16); }
DI void bstore_sc1(u32x4 v, __amdgpu_buffer_rsrc_t r, unsigned voff) { __builtin_amdgcn_raw_buffer_store_b128(v, r, voff, 0, 16); }

#define STAMP(i) do { if ((i) == 0) tq0 = __builtin_amdgcn_s_memrealtime(); if ((i) == 0) tq1 = __builtin_amdgcn_s_memrealtime(); } while (0)

struct BigFrag { s16x8 x0, x1, w0, w1; };
struct SmallFrag { s16x8 x, q; };

__global__ void __launch_bounds__(512, 2)
fused_kernel(const uint16_t* __restrict__ x, const uint16_t* __restrict__ wq, const uint16_t* __restrict__ wk, const uint16_t* __restrict__ wv,
             const uint16_t* __restrict__ wo, const uint16_t* __restrict__ w1, const uint16_t* __restrict__ w2, const uint16_t* __restrict__ gw,
             uint16_t* out, char* ws) {
    __shared__ __attribute__((aligned(16))) char smem[LDS_BYTES];
    const lptr lds = (lptr)smem;
    const unsigned lds0 = (unsigned)(uintptr_t)smem;
    const int tid = threadIdx.x, lane = tid & 63, w = __builtin_amdgcn_readfirstlane(tid >> 6);
    const int r = lane & 31, h = lane >> 5, g1 = (lane >> 4) & 1, q4 = (lane & 15) >> 2, p4 = lane & 3;
    const int b = blockIdx.x, bi = b >> 3;
    const int R = 2 * (b & 7) + (bi >> 4), cb = bi & 15, rh64 = cb & 1, hg = cb >> 1;
    const bool kvcu = cb >= 14, big = w < 4;
    const int row64 = R * 128 + rh64 * 64;
    const int rt = w & 1, hq = w >> 1, fh = hq & 1;
    unsigned stv = 0; unsigned long long tq0 = 0, tq1 = 0;
    STAMP(0);

    asm volatile("" :: "s"(x), "s"(wq), "s"(wk), "s"(wv), "s"(wo), "s"(w1), "s"(w2), "s"(gw), "s"(out), "s"(ws));
    const unsigned w64_src = (unsigned)((lane >> 3) * 1024 + hg * 128 + (((lane & 7) ^ (g1 << 2)) << 4));
    const unsigned w64_src1 = w64_src + 8192 - 1024;
    const unsigned xv = (unsigned)((lane >> 3) * 1024 + (((lane & 7) ^ ((lane >> 4) & 3)) << 4)), xv1 = (xv ^ 64) + 8192 - 1024;
    const unsigned wv0 = (unsigned)((lane >> 4) * (DFF * 2) + cb * 256 + (((lane & 15) ^ (((lane >> 4) & 3) << 2)) << 4)), wv1 = wv0 + 4 * DFF * 2 - 1024;
    const int rot = (b + cb) & 7;
    STAMP(21);
    if (!kvcu) {
#pragma unroll
        for (int s = 0; s < 2; ++s) {
            const int sr = (s + rot) & 7;
            glds16x2((const char*)x + R * 131072 + sr * 128 + w * 16384, xv, xv1, lds0 + s * L_STAGE + w * 2048);
            glds16x2((const char*)w1 + (size_t)(sr * 64 + w * 8) * (DFF * 2), wv0, wv1, lds0 + s * L_STAGE + 16384 + w * 2048);
            glds16((const char*)wq + sr * 65536 + w * 8192, w64_src, lds0 + s * L_STAGE + 32768 + w * 1024);
        }
    } else {
        const char* wkv = (const char*)((w & 2) ? wk : wv);
        const unsigned kvv = (w & 1) * 1024 + ((lane ^ ((w & 2) << 2)) << 4);
#pragma unroll 1
        for (int s = 0; s < 8; ++s) {
            glds16((const char*)x + (row64 + 8 * w) * 1024 + s * 128, xv ^ (rt << 6), lds0 + s * L_KSTAGE + w * 1024);
            if (!big) glds16(wkv + s * 2048, kvv, lds0 + s * L_KSTAGE + 8192 + (w & 3) * 1024);
        }
    }
    STAMP(20);
    const unsigned eo = lane == 1 ? WS_HFLAG + (R * 16 + cb) * 128 : !kvcu || lane == 0 || lane > 6 ? WS_EPOCH : lane == 2 ? WS_KVFLAG + (row64 >> 6) * 128 : WS_GZ + row64 * 8 + (lane - 3) * 128;
    const unsigned epoch_v = __hip_atomic_load((gu32*)(ws + eo), RLX_AGENT);
    asm volatile("" ::: "memory");
    const __amdgpu_buffer_rsrc_t rs_kv = mk_rsrc(ws + WS_KV, KV_BYTES), rs_h = mk_rsrc(ws + WS_H, H_BYTES), rs_o = mk_rsrc(ws + WS_O, O_BYTES);
    const __amdgpu_buffer_rsrc_t rs_kf = mk_rsrc(ws + WS_KVFLAG, 4096);
    const unsigned tr_in = 32 * g1 + 8 * p4;
    const unsigned wq_rd = (8 * h + q4) * 128 + ((fh ^ (q4 >> 1)) << 6) + tr_in;
    unsigned xo[4];
#pragma unroll
    for (int ks = 0; ks < 4; ++ks) xo[ks] = r * 128 + (((2 * ks + h) ^ ((r >> 1) & 7)) << 4);

    lptr kp, vp0, vp1, wrA0, wrA1, wrB0, wrB1, q_rd, qn_rd, km_rd, cst_wr;
    unsigned h_voff, hf_voff, g_voff0, g_voff1, hb, hb1, o_own;
    int spidx;
    f32x16 oacc;
    u32x4 cst, omark;
    s16x8 zf;
    auto consts = [&]() {
        kp = lds + lane * 16;
        const unsigned v_cst = (r & 27) != 16 ? 2064 : 2048;
        vp0 = lds + (r < 16 ? 1024 + h * 256 + r * 16 : v_cst); vp1 = lds + (r < 16 ? 1536 + h * 256 + r * 16 : v_cst);
        const unsigned w_rd0 = w * 2048 + (4 * h + q4) * 128 + ((q4 >> 1) << 6) + tr_in;
        wrA0 = lds + L_WB + w_rd0; wrA1 = lds + L_WB + (w_rd0 ^ 64); wrB0 = lds + w_rd0; wrB1 = lds + (w_rd0 ^ 64);
        q_rd = lds + L_QX + (hq * 2 + rt) * 1024 + lane * 16; qn_rd = lds + L_QN + (hq * 2 + rt) * 128 + r * 4; km_rd = lds + L_KM;
        h_voff = (unsigned)((row64 >> 5) * 112 + 14 * w) * 1024u + lane * 16;
        hf_voff = (unsigned)(R * 16 + (lane & 15)) * 128 + 4; g_voff0 = 32768 + (row64 + 32 * rt + r) * 8; g_voff1 = 32768 + (row64 + 8 * w + ((lane >> 1) & 4) + (lane >> 4)) * 8 + 4;
        spidx = 0xDDDD; oacc = (f32x16){}; zf = (s16x8){};
        cst = (lane & 1) ? (u32x4){} : (u32x4){0x3f803f80u, 0x3f803f80u, 0x3f803f80u, 0x3f803f80u};
        cst_wr = lds + (tid >> 1) * KVT + 2048 + (tid & 1) * 16;
        hb = (kvcu ? OOB : (unsigned)((R * 4 + 2 * rt) * 112 + cb * 8 + fh * 4) * 1024u) + lane * 16; hb1 = hb + 112 * 1024;
        o_own = (unsigned)(((row64 >> 5) + rt) * 32 + hg * 4 + hq) * 1024u + lane * 16;
        omark = (u32x4){~0u, ~0u, ~0u, ~0u};
        PIN(oacc); PIN(spidx); PIN(cst); PIN(cst_wr); PIN(hb); PIN(hb1); PIN(zf); PIN(o_own); PIN(omark);
        PIN(kp); PIN(vp0); PIN(vp1); PIN(wrA0); PIN(wrA1); PIN(wrB0); PIN(wrB1); PIN(q_rd); PIN(qn_rd); PIN(km_rd);
        PIN(h_voff); PIN(hf_voff); PIN(g_voff0); PIN(g_voff1);
        SBAR();
    };
    u32x4 hs[8];
#pragma unroll
    for (int i = 0; i < 8; ++i) asm volatile("" : "=v"(hs[i]));
    f32x16 a00 = {}, a01 = {}, a10 = {}, a11 = {};
    auto kv_wait = [&](u32x4& fv) {
        const unsigned ep = __builtin_amdgcn_readfirstlane(epoch_v) + 1u;
        if (__builtin_expect(!__all(fv[1] == ep && fv[3] == ep), 0)) {
            unsigned spins = 0;
            do { __builtin_amdgcn_s_sleep(6); CFENCE(); fv = bload_sc1(rs_kf, r * 128 + h * 16, 0); } while (!__all(fv[1] == ep && fv[3] == ep) && ++spins < SPIN_CAP);
        }
        CFENCE();
    };
    auto kv_bound = [&](const u32x4& fv) {
        const float km = row32_max(fmaxf(__uint_as_float(fv[0]), __uint_as_float(fv[2])));
        if (tid == 256) *(LAS float*)(lds + L_KM) = __builtin_amdgcn_sqrtf(km) * 1.0001f;
    };
    auto kv_tile = [&](int t) { glds16x2_sc1(ws + WS_KV + ((t + 8 * bi) & 63) * 2048, lane * 16, lds0 + t * KVT); };
    if (!kvcu) {
        unsigned cur = 0, nxt = L_STAGE;
        if (big) {
            const unsigned xb = rt * 8192, w0o = 16384 + (8 * h + q4) * 256 + (((2 * fh) ^ q4) << 6) + tr_in, w1o = w0o ^ 64;
            BigFrag F[4];
            auto rd = [&](BigFrag& n, lptr sb, int ks) {
                n.x0 = lds_frag(sb + xb + xo[ks]); n.x1 = lds_frag(sb + xb + 4096 + xo[ks]); n.w0 = tr_frag(sb + w0o + ks * 4096, 1024); n.w1 = tr_frag(sb + w1o + ks * 4096, 1024); };
#define AM(e, ...) do { if (ABL & 2048) asm volatile("" :: __VA_ARGS__); else e; SBAR(); } while (0)
#define AR(e, ...) do { if (ABL & 4096) asm volatile("" : __VA_ARGS__); else { e; } SBAR(); } while (0)
            auto step = [&](const BigFrag& c, BigFrag& n, lptr sb, int ks) {
                AM(a00 = MFMA(c.w0, c.x0, a00), "v"(c.w0), "v"(c.x0)); AR(n.x0 = lds_frag(sb + xb + xo[ks]); n.x1 = lds_frag(sb + xb + 4096 + xo[ks]), "+v"(n.x0), "+v"(n.x1));
                AM(a10 = MFMA(c.w0, c.x1, a10), "v"(c.x1)); AR(n.w0 = tr_frag(sb + w0o + ks * 4096, 1024), "+v"(n.w0));
                AM(a01 = MFMA(c.w1, c.x0, a01), "v"(c.w1)); AR(n.w1 = tr_frag(sb + w1o + ks * 4096, 1024), "+v"(n.w1));
                AM(a11 = MFMA(c.w1, c.x1, a11), "v"(c.w1)); };
            auto stage = [&](auto bar) {
                step(F[0], F[3], lds + cur, 3);
                bar();
                step(F[1], F[0], lds + nxt, 0); step(F[2], F[1], lds + nxt, 1); step(F[3], F[2], lds + nxt, 2);
                cur = nxt; nxt = (nxt == 2 * L_STAGE) ? 0 : nxt + L_STAGE; };
            consts();
            STAMP(13);
            BAR_VL(6);
            STAMP(14);
            rd(F[0], lds, 0); rd(F[1], lds, 1); rd(F[2], lds, 2);
#pragma unroll 1
            for (int s = 0; s < 7; ++s) stage([&] {
                BAR_VL(1);
                if (s == 3 && w < 2) touch8k(w == 0 ? (const char*)w2 + (R * 14 + cb) * 8192 : (const char*)wo + (b & 63) * 8192, lane, lds0 + L_TOUCH); });
            STAMP(1);
            rd(F[3], lds + cur, 3);
#define LAST4(acc, wm, xm) do { _Pragma("unroll") for (int ks = 0; ks < 4; ++ks) acc = MFMA(F[ks].wm, F[ks].xm, acc); } while (0)
#define LAST4P(acc, wm, xm, prev, i) do { acc = MFMA(F[0].wm, F[0].xm, acc); acc = MFMA(F[1].wm, F[1].xm, acc); SBAR(); hs[i] = pack8_relu<0>(prev); PIN(hs[i]); SBAR(); \
            acc = MFMA(F[2].wm, F[2].xm, acc); SBAR(); hs[(i) + 1] = pack8_relu<1>(prev); PIN(hs[(i) + 1]); SBAR(); acc = MFMA(F[3].wm, F[3].xm, acc); SBAR(); } while (0)
            LAST4(a00, w0, x0); SBAR();
            LAST4P(a01, w1, x0, a00, 0); LAST4P(a10, w0, x1, a01, 2); LAST4P(a11, w1, x1, a10, 4);
            hs[6] = pack8_relu<0>(a11); hs[7] = pack8_relu<1>(a11);
        } else {
            const int j = w - 4;
            __builtin_amdgcn_s_setprio(2);
            const unsigned xb = (2 * rh64 + rt) * 4096, qo = 32768 + wq_rd;
            SmallFrag F[4];
            auto rd = [&](SmallFrag& n, lptr sb, int ks) { n.x = lds_frag(sb + xb + xo[ks]); n.q = tr_frag(sb + qo + ks * 2048, 512); SBAR(); };
            auto mma = [&](const SmallFrag& c) { a00 = MFMA(c.q, c.x, a00); SBAR(); };
            const unsigned xv2 = xv + 2 * (8192 - 1024), xv3 = xv1 + 2 * (8192 - 1024), wv2 = wv0 + 2 * (4 * DFF * 2 - 1024), wv3 = wv1 + 2 * (4 * DFF * 2 - 1024);
            const char* xs = (const char*)x + R * 131072 + j * 32768;
            const char* w1s = (const char*)w1 + (size_t)(j * 16) * (DFF * 2);
            const char* wqs = (const char*)wq + j * 16384;
            auto fill0 = [&](int sr, unsigned slot) { glds16x4(xs + sr * 128, xv, xv1, xv2, xv3, lds0 + slot + j * 4096); };
            auto fill1 = [&](int sr, unsigned slot) { glds16x4(w1s + sr * (64 * DFF * 2), wv0, wv1, wv2, wv3, lds0 + slot + 16384 + j * 4096); };
            auto fill2 = [&](int sr, unsigned slot) { glds16x2(wqs + sr * 65536, w64_src, w64_src1, lds0 + slot + 32768 + j * 2048); };
            fill0((rot + 2) & 7, 2 * L_STAGE); fill1((rot + 2) & 7, 2 * L_STAGE); fill2((rot + 2) & 7, 2 * L_STAGE);
            consts();
            BAR_VL(16);
            rd(F[0], lds, 0); rd(F[1], lds, 1); rd(F[2], lds, 2);
#pragma unroll 1
            for (int s = 0; s < 6; ++s) {
                const bool more = s < 5;
                const int sr = (rot + s + 3) & 7;
                rd(F[3], lds + cur, 3); mma(F[0]);
                BAR_VL(10);
                if (more) fill0(sr, cur);
                rd(F[0], lds + nxt, 0); mma(F[1]);
                if (more) fill1(sr, cur);
                rd(F[1], lds + nxt, 1); mma(F[2]);
                if (more) fill2(sr, cur);
                rd(F[2], lds + nxt, 2); mma(F[3]);
                cur = nxt; nxt = (nxt == 2 * L_STAGE) ? 0 : nxt + L_STAGE;
            }
            u32x4 fv = bload_sc1(rs_kf, r * 128 + h * 16, 0);
            rd(F[3], lds, 3); mma(F[0]);
            BAR_VL(0);
            rd(F[0], lds + L_STAGE, 0); mma(F[1]);
            STAMP(15);
            kv_wait(fv);
            STAMP(16);
            kv_tile(j); kv_tile(4 + j);
            rd(F[1], lds + L_STAGE, 1); mma(F[2]);
            kv_tile(8 + j); kv_tile(12 + j);
            rd(F[2], lds + L_STAGE, 2); mma(F[3]); rd(F[3], lds + L_STAGE, 3);
            mma(F[0]); kv_bound(fv); mma(F[1]); mma(F[2]); mma(F[3]);
        }
    } else {
        const unsigned xb = rt * 4096, w64_src2 = w64_src + 2 * (8192 - 1024), w64_src3 = w64_src + 3 * (8192 - 1024);
        {
            const unsigned wkv_rd = 8192 + g1 * 2048 + (((8 * h + q4) * 32 + 8 * p4) ^ (g1 << 7));
#pragma unroll
            for (int g = 0; g < 2; ++g) {
                if (g == 0) { if (big) BAR_VL(5); else BAR_VL(9); } else BAR_VL(1);
                const lptr st = lds + (g * 4 + hq) * L_KSTAGE;
#pragma unroll
                for (int k = 0; k < 4; ++k) a00 = MFMA(tr_frag2(st + wkv_rd + k * 512, st + (wkv_rd ^ 128) + k * 512), lds_frag(st + xb + xo[k]), a00);
            }
        }
        BAR_L();
        {
            const lptr pw = lds + w * L_KSTAGE + 8192 + r * 128;
            const unsigned sz = (r & 7) ^ ((r >> 1) & 4);
#pragma unroll
            for (int i = 0; i < 4; ++i) *(LAS f32x4*)(pw + (((2 * i + h) ^ sz) << 4)) = (f32x4){a00[4 * i], a00[4 * i + 1], a00[4 * i + 2], a00[4 * i + 3]};
            a00 = (f32x16){};
        }
        BAR_L();
        if (big) {
            STAMP(1);
            const unsigned kvo = (unsigned)((row64 >> 5) + rt) * 2048u + lane * 16;
            const lptr pr = lds + rt * L_KSTAGE + 8192;
            float n2 = 0.f;
            if (fh == 0) {
                const unsigned sz = (r & 7) ^ ((r >> 1) & 4);
                f32x4 lo = {}, hi = {};
#pragma unroll
                for (int i = 0; i < 4; ++i) { lo += *(const LAS f32x4*)(pr + i * 2 * L_KSTAGE + r * 128 + (((4 + h) ^ sz) << 4)); hi += *(const LAS f32x4*)(pr + i * 2 * L_KSTAGE + r * 128 + (((6 + h) ^ sz) << 4)); }
                const u32x4 v = {cvtpk(lo[0], lo[1]), cvtpk(lo[2], lo[3]), cvtpk(hi[0], hi[1]), cvtpk(hi[2], hi[3])};
                bstore_sc1(v, rs_kv, kvo);
                n2 = row32_max(swap_add(dot2(v[0], v[0], dot2(v[1], v[1], 0.f)) + dot2(v[2], v[2], dot2(v[3], v[3], 0.f))));
            } else {
                float t[8];
#pragma unroll
                for (int j = 0; j < 8; ++j) {
                    const lptr q = pr + ((lane >> 4) * 8 + j) * 128 + ((q4 ^ j ^ (g1 << 2)) << 4) + p4 * 4;
                    t[j] = (*(const LAS float*)q + *(const LAS float*)(q + 2 * L_KSTAGE)) + (*(const LAS float*)(q + 4 * L_KSTAGE) + *(const LAS float*)(q + 6 * L_KSTAGE));
                }
                bstore_sc1((u32x4){cvtpk(t[0], t[1]), cvtpk(t[2], t[3]), cvtpk(t[4], t[5]), cvtpk(t[6], t[7])}, rs_kv, kvo + 1024);
            }
            STAMP(13);
            WAIT_V(0);
            STAMP(14);
            if (lane == 0) __hip_atomic_store((gu64*)(ws + WS_KVFLAG) + (row64 >> 6) * 16 + w, ((unsigned long long)(epoch_v + 1u) << 32) | __float_as_uint(n2), RLX_AGENT);
            u32x4 gf[16];
#pragma unroll
            for (int i = 0; i < 16; ++i) gf[i] = *(const u32x4*)(gw + i * 32 + (lane >> 4) * 8);
            consts();
            BAR_L();
            f32x4 z = {};
            const int zr = 16 * w + (lane & 15);
#pragma unroll
            for (int i = 0; i < 16; ++i)
                z = __builtin_amdgcn_mfma_f32_16x16x32_bf16(asfrag(gf[i]), lds_frag(lds + (i >> 1) * L_KSTAGE + zr * 128 + (((4 * (i & 1) + (lane >> 4)) ^ ((zr >> 1) & 7)) << 4)), z, 0, 0, 0);
            const float gt = rbf(__builtin_amdgcn_rcpf(1.f + __expf(-rbf(z[0])))), og = fmaxf(rbf(1.f - gt), 0x1p-40f);
            if (lane < 16) __hip_atomic_store((gu64*)(ws + WS_GZ) + row64 + zr, ((unsigned long long)__float_as_uint(og) << 32) | __float_as_uint(gt / og), RLX_AGENT);
            BAR_L();
        } else {
#pragma unroll
            for (int i = 0; i < 4; ++i) { const int p = i * 16 + (w - 4) * 4; glds16x4((const char*)wq + p * 8192, w64_src, w64_src1, w64_src2, w64_src3, lds0 + L_KWQ + p * 1024); }
            consts();
            BAR_VL(0);
            SmallFrag F[4];
            auto rd = [&](SmallFrag& n, int s, int k) { n.x = lds_frag(lds + s * L_KSTAGE + xb + xo[k]); n.q = tr_frag(lds + L_KWQ + s * 8192 + wq_rd + k * 2048, 512); SBAR(); };
#pragma unroll
            for (int k = 0; k < 4; ++k) rd(F[k], 0, k);
#pragma unroll 1
            for (int s = 1; s < 8; ++s) {
#pragma unroll
                for (int k = 0; k < 4; ++k) { a00 = MFMA(F[k].q, F[k].x, a00); SBAR(); rd(F[k], s, k); }
            }
            u32x4 fv = bload_sc1(rs_kf, r * 128 + h * 16, 0);
#pragma unroll
            for (int k = 0; k < 4; ++k) a00 = MFMA(F[k].q, F[k].x, a00);
            BAR_L();
            STAMP(15);
            kv_wait(fv);
            STAMP(16);
            kv_tile(w - 4); kv_tile(w); kv_tile(4 + w); kv_tile(8 + w);
            kv_bound(fv);
        }
    }
    STAMP(2);
    if (big) {
        if (tid < 32) lds_put(cst_wr, cst);
    } else {
#pragma unroll
        for (int s = 0; s < 2; ++s) {
            const u32x4 v = s ? pack8<1>(a00, QSCALE) : pack8<0>(a00, QSCALE);
            lds_put(lds + L_QX + (4 * fh + 2 * s + rt) * 1024 + lane * 16, v);
            const float n2 = swap_add(dot2(v[0], v[0], dot2(v[1], v[1], 0.f)) + dot2(v[2], v[2], dot2(v[3], v[3], 0.f)));
            *(LAS float*)(lds + L_QN + (4 * fh + 2 * s + rt) * 128 + r * 4) = __builtin_amdgcn_sqrtf(n2);
        }
    }
    __builtin_amdgcn_s_setprio(3);
    BAR_VL(0);
    STAMP(3);
    const s16x8 qf = lds_frag(q_rd);
    s16x8 kf = lds_frag(kp), vf0, vf1;
    float mshift = *(const LAS float*)qn_rd * *(const LAS float*)km_rd;
    vf0 = lds_frag(vp0);
    if (__builtin_expect(!__all(mshift <= 0.5f * SAFE_RANGE), 0)) {
        float m = -__builtin_inff();
#pragma unroll 1
        for (int t = 0; t < 64; ++t) {
            const f32x16 s = MFMA(asfrag(bload_sc1(rs_kv, lane * 16, t * 2048)), qf, (f32x16){});
#pragma unroll
            for (int i = 0; i < 16; ++i) m = fmaxf(m, s[i]);
        }
        mshift = swap_max(m);
    }
    f32x16 cinit;
#pragma unroll
    for (int i = 0; i < 16; ++i) cinit[i] = -mshift;
    PIN(cinit);
    f32x16 sA = MFMA(kf, qf, cinit), sB;
    kf = lds_frag(kp + KVT);
    if (tid >= 32 && tid < 128) lds_put(cst_wr, cst);

#define S1(S_, i) S_[i] = __builtin_amdgcn_exp2f(S_[i])
#define EX2(S_, i) do { S1(S_, i); S1(S_, (i) + 1); } while (0)
#define EX3(S_, i) do { S1(S_, i); S1(S_, (i) + 1); S1(S_, (i) + 2); } while (0)
#define EX4(S_, i) do { EX2(S_, i); EX2(S_, (i) + 2); } while (0)
#define PV1(SN, T) do { if ((T) >= 0) oacc = pv_mma<1>(vf1, SN, oacc, spidx); vf1 = lds_frag(vp1 + ((T) < 0 ? 0 : (T)) * KVT); SBAR(); } while (0)
#define QK(SN, T) do { SN = MFMA(kf, qf, cinit); kf = lds_frag(kp + ((T) + 2) * KVT); SBAR(); } while (0)
#define PV0(SC, T) do { oacc = pv_mma<0>(vf0, SC, oacc, spidx); vf0 = lds_frag(vp0 + ((T) + 1) * KVT); SBAR(); } while (0)
#define CUT(S_) do { PIN(S_); SBAR(); } while (0)
#define ITER0(SC, SN, T, T1, PR, MEM) do { PR; \
    EX2(SC, 0); CUT(SC); PV1(SN, T1); EX4(SC, 2); CUT(SC); QK(SN, T); EX4(SC, 6); S1(SC, 10); CUT(SC); PV0(SC, T); MEM; S1(SC, 11); EX4(SC, 12); CUT(SC); } while (0)
#define ITER1(SC, SN, T, PR, PRE, MEM, G1) do { PR; PRE; \
    EX2(SC, 0); CUT(SC); PV1(SN, T); EX4(SC, 2); CUT(SC); QK(SN, T); EX4(SC, 6); CUT(SC); PV0(SC, T); MEM; EX4(SC, 10); CUT(SC); G1; SBAR(); EX2(SC, 14); CUT(SC); } while (0)
#define ITERG(SC, SN, T, PR, MEM, G0, G1) do { PR; \
    EX2(SC, 0); CUT(SC); PV1(SN, T); EX3(SC, 2); CUT(SC); QK(SN, T); EX3(SC, 5); CUT(SC); G0; SBAR(); EX3(SC, 8); CUT(SC); PV0(SC, T); MEM; EX3(SC, 11); CUT(SC); G1; SBAR(); EX2(SC, 14); CUT(SC); } while (0)
#define NOP (void)0
#define PRIO(p) __builtin_amdgcn_s_setprio(p)
#define ITER2(T, P0, P1, M0, M1) do { ITER0(sA, sB, T, T, PRIO(P0), M0); ITER0(sB, sA, (T) + 1, (T) + 1, PRIO(P1), M1); } while (0)
#define ADV(n) do { kp += (n) * KVT; vp0 += (n) * KVT; vp1 += (n) * KVT; PIN(kp); PIN(vp0); PIN(vp1); } while (0)
    const char* const w2w = (const char*)w2 + w * (14 * 16384);
    const char* const wow = (const char*)wo + w * 65536;
    int pn = (row64 >> 6) % 7;
    const unsigned wbA = lds0 + L_WB + w * 2048, wbB = lds0 + w * 2048;
    const __amdgpu_buffer_rsrc_t rs_f = mk_rsrc(ws + WS_HFLAG, 32768 + 16384);
    u32x4 hA0, hA1, hB0, hB1;
    s16x8 wf0, wf1;
#define W_LD(src, buf) glds16x2(src, w64_src, w64_src1, buf)
    STAMP(4);
#define H_ST(i) do { if (big) bstore_sc1(hs[i], rs_h, ((i) < 4 ? hb : hb1) + ((i) & 3) * 1024); } while (0)
    ITER0(sA, sB, 0, -1, PRIO(3), H_ST(0)); ITER0(sB, sA, 1, 1, NOP, H_ST(1); kv_tile(16 + w));
    ITER2(2, 3, 3, H_ST(2); kv_tile(24 + w), H_ST(3); kv_tile(32 + w));
    ITER2(4, 2, 2, H_ST(4); kv_tile(40 + w), H_ST(5); kv_tile(48 + w));
    ITER2(6, 2, 2, H_ST(6); kv_tile(56 + w), H_ST(7); W_LD(w2w + pn * 32768, wbA));
    STAMP(17);
    ITER2(8, 1, 1, bstore_sc1(omark, rs_o, o_own), NOP);
    ITER2(10, 1, 1, NOP, NOP);
    ITER2(12, 0, 0, NOP, NOP);
    STAMP(18);
    STAMP(19);
    BAR_VL(0);
    unsigned epoch_w = epoch_v; PIN(epoch_w);
    const unsigned epoch = __builtin_amdgcn_readfirstlane(epoch_w) + 1u;
    if (tid == 0) __hip_atomic_store((gu32*)(ws + WS_HFLAG) + (R * 16 + cb) * 32 + 1, epoch, RLX_AGENT);
    STAMP(5);
    unsigned hfv;
    ITER2(14, 1, 3, W_LD(w2w + pn * 32768 + 16384, wbB); a00 = MFMA(zf, zf, (f32x16){}); PIN(zf); a01 = MFMA(zf, zf, (f32x16){}), PIN(zf); a10 = MFMA(zf, zf, (f32x16){}); PIN(zf); a11 = MFMA(zf, zf, (f32x16){}));
    ITER2(16, 2, 1, NOP, hfv = __builtin_amdgcn_raw_buffer_load_b32(rs_f, hf_voff, 0, 16));
    ITER0(sA, sB, 18, 18, PRIO(3), NOP);
    STAMP(6);
    if (__builtin_expect(!__all(hfv == epoch), 0)) {
        unsigned spins = 0;
        do { __builtin_amdgcn_s_sleep(6); CFENCE(); hfv = __builtin_amdgcn_raw_buffer_load_b32(rs_f, hf_voff, 0, 16); } while (!__all(hfv == epoch) && ++spins < SPIN_CAP);
    }
    CFENCE();
    STAMP(7);
    unsigned gr_u, og_u;
    ITER0(sB, sA, 19, 19, PRIO(2), gr_u = __builtin_amdgcn_raw_buffer_load_b32(rs_f, g_voff0, 0, 16); og_u = __builtin_amdgcn_raw_buffer_load_b32(rs_f, g_voff1, 0, 16);
          hA0 = bload_sc1(rs_h, h_voff, pn * 2048); hA1 = bload_sc1(rs_h, h_voff, pn * 2048 + 112 * 1024));
    ITER2(20, 1, 3, hB0 = bload_sc1(rs_h, h_voff, pn * 2048 + 1024), hB1 = bload_sc1(rs_h, h_voff, pn * 2048 + 113 * 1024));
    ADV(22);
    PIN(a00); PIN(a01); PIN(a10); PIN(a11);
    STAMP(8);
#define GM(acc, wf, H) acc = MFMA(asfrag(H), wf, acc); SBAR()
#define GSTEP(SX, SY, T, R0, R1, H0, H1, LW, LH0, LH1) do { \
    ITER1(SX, SY, T, PRIO(3), WAIT_V(5); wf0 = tr_frag(R0, 1024); wf1 = tr_frag(R1, 1024), NOP, GM(a00, wf0, H0)); \
    ITERG(SY, SX, (T) + 1, PRIO(2), LW, GM(a01, wf1, H0); LH0, GM(a10, wf0, H1)); \
    ITER1(SX, SY, (T) + 2, PRIO(1), NOP, NOP, GM(a11, wf1, H1); LH1); } while (0)
#pragma unroll 1
    for (int n = 0; n < 4; ++n) {
        pn = pn == 6 ? 0 : pn + 1;
        const char* wsrc = w2w + pn * 32768;
        GSTEP(sA, sB, 0, wrA0, wrA1, hA0, hA1, W_LD(wsrc, wbA), hA0 = bload_sc1(rs_h, h_voff, pn * 2048), hA1 = bload_sc1(rs_h, h_voff, pn * 2048 + 112 * 1024));
        GSTEP(sB, sA, 3, wrB0, wrB1, hB0, hB1, W_LD(wsrc + 16384, wbB), hB0 = bload_sc1(rs_h, h_voff, pn * 2048 + 1024), hB1 = bload_sc1(rs_h, h_voff, pn * 2048 + 113 * 1024));
        ADV(6);
    }
    BAR_L();
    pn = pn == 6 ? 0 : pn + 1;
    const int pm = pn == 6 ? 0 : pn + 1;
    u32x4 hC0, hC1, hD0, hD1, hE0, hE1, hF0, hF1;
    ITER2(0, 3, 2, W_LD(w2w + pn * 32768, wbB + 16384); hC0 = bload_sc1(rs_h, h_voff, pn * 2048), hC1 = bload_sc1(rs_h, h_voff, pn * 2048 + 112 * 1024));
    ITER2(2, 1, 3, W_LD(w2w + pn * 32768 + 16384, wbB + 32768); hD0 = bload_sc1(rs_h, h_voff, pn * 2048 + 1024), hD1 = bload_sc1(rs_h, h_voff, pn * 2048 + 113 * 1024));
    ITER2(4, 2, 1, W_LD(w2w + pm * 32768, wbB + 49152); hE0 = bload_sc1(rs_h, h_voff, pm * 2048), hE1 = bload_sc1(rs_h, h_voff, pm * 2048 + 112 * 1024));
    ITER2(6, 3, 2, W_LD(w2w + pm * 32768 + 16384, wbB + 65536); hF0 = bload_sc1(rs_h, h_voff, pm * 2048 + 1024), hF1 = bload_sc1(rs_h, h_voff, pm * 2048 + 113 * 1024));
    const bool closer = b == 112 && w == 7;
    unsigned ef[4];
    auto ef_load = [&]() {
#pragma unroll
        for (int i = 0; i < 4; ++i) ef[i] = __builtin_amdgcn_raw_buffer_load_b32(mk_rsrc(ws + WS_HFLAG + i * 8192 + 4, closer ? 8188 : 0), lane * 128, 0, 16);
    };
    const unsigned xtra = w < 3 ? 5 * 16384 : L_QX - 3 * 2048;
    s16x8 wf2, wf3, wf4, wf5;
    ITER2(8, 1, 3, WAIT_V(14); wf0 = tr_frag(wrA0, 1024); wf1 = tr_frag(wrA1, 1024); W_LD(wow + 49152, wbB + xtra), wf2 = tr_frag(wrB0, 1024); wf3 = tr_frag(wrB1, 1024); ef_load());
    ITER2(10, 2, 1, wf4 = tr_frag(wrB0 + 16384, 1024); wf5 = tr_frag(wrB1 + 16384, 1024); PIN(wf0); PIN(wf1); W_LD(wow, wbA), PIN(wf2); PIN(wf3); W_LD(wow + 16384, wbB));
    ITER2(12, 3, 3, PIN(wf4); PIN(wf5); W_LD(wow + 32768, wbB + 16384), NOP);
    ITER2(14, 2, 2, NOP, NOP);
    ITER2(16, 1, 0, NOP, NOP);
    STAMP(9);
#define GMA(acc, wf, H) asm volatile("v_mfma_f32_32x32x16_bf16 %0, %1, %2, %0" : "+v"(acc) : "v"(H), "v"(wf))
    PRIO(3);
    oacc = pv_mma<1>(vf1, sB, oacc, spidx);
    {
        u32x4 v = pack8<0>(oacc, __builtin_amdgcn_rcpf(oacc[8]) * __uint_as_float(gr_u));
#pragma unroll
        for (int e = 0; e < 4; ++e) v[e] = v[e] < 0xfffffffeu ? v[e] : 0xfffffffeu;
        bstore_sc1(v, rs_o, o_own);
        asm volatile("s_nop 1");
    }
    PRIO(0);
    GMA(a00, wf0, hA0); GMA(a01, wf1, hA0);
    WAIT_V(1);
    lptr wrE0 = wrB0 + 32768, wrE1 = wrB1 + 32768; PIN(wrE0); PIN(wrE1);
    const s16x8 wf6 = tr_frag(wrE0, 1024), wf7 = tr_frag(wrE1, 1024), wf8 = tr_frag(wrE0 + 16384, 1024), wf9 = tr_frag(wrE1 + 16384, 1024);
    SBAR();
    GMA(a10, wf0, hA1); GMA(a11, wf1, hA1);
    const s16x8 wfa = tr_frag(wrE0 + 32768, 1024), wfb = tr_frag(wrE1 + 32768, 1024);
    s16x8 wof[8];
    wof[0] = tr_frag(wrA0, 1024); wof[1] = tr_frag(wrA1, 1024);
    SBAR();
    GMA(a00, wf2, hB0); GMA(a01, wf3, hB0);
    wof[2] = tr_frag(wrB0, 1024); wof[3] = tr_frag(wrB1, 1024); wof[4] = tr_frag(wrB0 + 16384, 1024); wof[5] = tr_frag(wrB1 + 16384, 1024);
    SBAR();
    GMA(a10, wf2, hB1); GMA(a11, wf3, hB1);
    wof[6] = tr_frag(wrB0 + xtra, 1024); wof[7] = tr_frag(wrB1 + xtra, 1024);
    SBAR();
    GMA(a00, wf4, hC0); GMA(a01, wf5, hC0); GMA(a10, wf4, hC1); GMA(a11, wf5, hC1);
    const unsigned ob = (unsigned)((row64 >> 5) * 32 + 4 * w) * 1024u;
    u32x4 of[8];
    auto o_load = [&]() {
#pragma unroll
        for (int i = 0; i < 8; ++i) of[i] = bload_sc1(rs_o, lane * 16 + (i & 3) * 1024, ob + (i >> 2) * 32768);
    };
    auto o_ready = [&]() {
        unsigned m = 0;
#pragma unroll
        for (int i = 0; i < 8; ++i) { m = max(m, max(of[i][0], of[i][1])); m = max(m, max(of[i][2], of[i][3])); }
        return __all(m != ~0u);
    };
    BAR_VL(0);
#pragma unroll
    for (int i = 0; i < 8; ++i) PIN(wof[i]);
    STAMP(10);
    o_load();
    SBAR();
    GMA(a00, wf6, hD0); GMA(a01, wf7, hD0); GMA(a10, wf6, hD1); GMA(a11, wf7, hD1);
    GMA(a00, wf8, hE0); GMA(a01, wf9, hE0); GMA(a10, wf8, hE1); GMA(a11, wf9, hE1);
    GMA(a00, wfa, hF0); GMA(a01, wfb, hF0); GMA(a10, wfa, hF1); GMA(a11, wfb, hF1);
    SBAR();
    const lptr prb = lds + (((w >> 2) * 2 + ((lane >> 2) & 1)) * 16 + 4 * (w & 3) + (lane >> 4)) * 2048 + ((lane >> 3) & 1) * 128 + p4 * 32;
    lptr pr0 = prb + g1 * 16, pr1 = prb + (g1 ^ 1) * 16;
    PIN(pr0); PIN(pr1);
    uint16_t* outp = out + (row64 + 8 * w + ((lane >> 1) & 4) + (lane >> 4)) * 512 + hg * 64 + (lane & 7) * 8;
    PIN(outp);
    SBAR();
    if (!o_ready()) {
        unsigned spins = 0;
        do { CFENCE(); o_load(); } while (!o_ready() && ++spins < SPIN_CAP);
    }
    STAMP(11);
#define AMM(acc, wi, oi) GMA(acc, wof[wi], of[oi])
#define PUT(a, i, off) asm volatile("ds_write_addtid_b32 %0 offset:%4\n\tds_write_addtid_b32 %1 offset:%5\n\tds_write_addtid_b32 %2 offset:%6\n\tds_write_addtid_b32 %3 offset:%7" \
    :: "v"(a[4 * (i)]), "v"(a[4 * (i) + 1]), "v"(a[4 * (i) + 2]), "v"(a[4 * (i) + 3]), "n"((off) + (i) * 8192), "n"((off) + (i) * 8192 + 2048), "n"((off) + (i) * 8192 + 4096), "n"((off) + (i) * 8192 + 6144) : "memory")
#define SETM0(v, prev) asm volatile("s_mov_b32 m0, %1\n\ts_nop 10" : "+v"(prev) : "s"(v))
#define MM_PUT(acc, c, o, prev, m0v, off) do { AMM(acc, c, o); SETM0(m0v, prev); PUT(prev, 0, off); AMM(acc, 2 + (c), (o) + 1); PUT(prev, 1, off); \
    AMM(acc, 4 + (c), (o) + 2); PUT(prev, 2, off); AMM(acc, 6 + (c), (o) + 3); PUT(prev, 3, off); } while (0)
    const unsigned m0a = lds0 + w * 256, m0b = m0a + 63616;
    asm volatile("s_nop 1");
    AMM(a00, 0, 0); AMM(a00, 2, 1); AMM(a00, 4, 2); AMM(a00, 6, 3);
    MM_PUT(a01, 1, 0, a00, m0a, 0);
    MM_PUT(a10, 0, 4, a01, m0a, 32768);
    MM_PUT(a11, 1, 4, a10, m0b, 65536 - 63616);
    asm volatile("s_nop 7" : "+v"(a11));
    PUT(a11, 0, 98304 - 63616); PUT(a11, 1, 98304 - 63616); PUT(a11, 2, 98304 - 63616); PUT(a11, 3, 98304 - 63616);
    BAR_L();
    {
        f32x4 t[16];
#pragma unroll
        for (int k = 0; k < 8; ++k) { t[2 * k] = *(const LAS f32x4*)(pr0 + k * 256); t[2 * k + 1] = *(const LAS f32x4*)(pr1 + k * 256); }
        SBAR();
        f32x4 lo = t[0], hi = t[1];
#pragma unroll
        for (int k = 1; k < 8; ++k) { lo += t[2 * k]; hi += t[2 * k + 1]; }
        lo *= __uint_as_float(og_u); hi *= __uint_as_float(og_u);
        const unsigned l0 = cvtpk(lo[0], lo[1]), l1 = cvtpk(lo[2], lo[3]), h0 = cvtpk(hi[0], hi[1]), h1 = cvtpk(hi[2], hi[3]);
        const u32x4 v = g1 ? (u32x4){h0, h1, l0, l1} : (u32x4){l0, l1, h0, h1};
        __builtin_nontemporal_store(v, (GAS u32x4*)outp);
    }
    if (closer) {
        unsigned spins = 0;
        while (!__all(ef[0] == epoch && ef[1] == epoch && ef[2] == epoch && ef[3] == epoch) && ++spins < SPIN_CAP) { CFENCE(); ef_load(); }
        if (lane == 0) __hip_atomic_store((gu32*)(ws + WS_EPOCH), epoch, RLX_AGENT);
    }
    STAMP(12);
    if (b == 8 && w == 0) { const unsigned long long tgt = tq0 + 6000 + 10 * (tq1 - tq0); while (__builtin_amdgcn_s_memrealtime() < tgt) __builtin_amdgcn_s_sleep(1); }
}

extern "C" void kernel_launch(void* const* d_in, const int* in_sizes, int n_in, void* d_out, int out_size, void* d_ws, size_t ws_size,
                              hipStream_t stream) {
    fused_kernel<<<256, 512, 0, stream>>>((const uint16_t*)d_in[0], (const uint16_t*)d_in[1], (const uint16_t*)d_in[2], (const uint16_t*)d_in[3],
                                          (const uint16_t*)d_in[4], (const uint16_t*)d_in[6], (const uint16_t*)d_in[7], (const uint16_t*)d_in[8],
                                          (uint16_t*)d_out, (char*)d_ws);
}
```

```cpp
#include <hip/hip_runtime.h>
#include <stdint.h>

typedef short s16x8 __attribute__((ext_vector_type(8)));
typedef short s16x4 __attribute__((ext_vector_type(4)));
typedef short s16x2 __attribute__((ext_vector_type(2)));
typedef float f32x16 __attribute__((ext_vector_type(16)));
typedef float f32x4 __attribute__((ext_vector_type(4)));
typedef float f32x2 __attribute__((ext_vector_type(2)));
typedef unsigned u32x4 __attribute__((ext_vector_type(4)));
typedef __bf16 bf16x2 __attribute__((ext_vector_type(2)));
typedef __bf16 bf16x8 __attribute__((ext_vector_type(8)));
typedef __bf16 bf16x16 __attribute__((ext_vector_type(16)));
#define LAS __attribute__((address_space(3)))
#define GAS __attribute__((address_space(1)))
typedef GAS unsigned gu32;
typedef GAS unsigned long long gu64;
typedef LAS char* lptr;
#define DI __device__ __forceinline__
#define RLX_AGENT __ATOMIC_RELAXED, __HIP_MEMORY_SCOPE_AGENT
#define MFMA(a, b, c) __builtin_amdgcn_mfma_f32_32x32x16_bf16((a), (b), (c), 0, 0, 0)
#define SBAR() __builtin_amdgcn_sched_barrier(0)
#define PIN(x) asm volatile("" : "+v"(x))
#define CFENCE() do { asm volatile("" ::: "memory"); __builtin_amdgcn_fence(__ATOMIC_ACQUIRE, "wavefront"); } while (0)
#define BAR_L() asm volatile("s_waitcnt lgkmcnt(0)\n\ts_barrier" ::: "memory")
#define BAR_VL(N) asm volatile("s_waitcnt vmcnt(" #N ") lgkmcnt(0)\n\ts_barrier" ::: "memory")

#define ABL 0
#define STW 0
#define STAMPS 0

constexpr int D = 512, DFF = 1792;
constexpr float QSCALE = 0.25f * 1.4426950408889634f;
constexpr float SAFE_RANGE = 64.f;
constexpr unsigned WS_EPOCH = 0, WS_KVFLAG = 4096, WS_HFLAG = 8192;
constexpr unsigned WS_GZ = WS_HFLAG + 32768;
constexpr unsigned WS_KV = WS_GZ + 16384, KV_BYTES = 64 * 2048;
constexpr unsigned WS_H = WS_KV + KV_BYTES, H_BYTES = 64 * 112 * 1024;
constexpr unsigned WS_O = WS_H + H_BYTES, O_BYTES = 64 * 32 * 1024;
constexpr unsigned WS_STAMP = WS_O + O_BYTES;
constexpr unsigned OOB = 0x7ff00000u;
constexpr unsigned L_STAGE = 40960;
constexpr unsigned L_KSTAGE = 12288, L_KWQ = 98304;
constexpr unsigned KVT = 2080;
constexpr unsigned L_WB = 64 * KVT;
constexpr unsigned L_QX = L_WB + 16384, L_QN = L_QX + 8192, L_KM = L_QN + 1024, L_TOUCH = L_KM + 256, LDS_BYTES = 163840;
static_assert(L_TOUCH + 256 <= LDS_BYTES, "lds map");
constexpr unsigned SPIN_CAP = 1u << 16;

DI unsigned cvtpk(float a, float b) { f32x2 v = {a, b}; return __builtin_bit_cast(unsigned, __builtin_convertvector(v, bf16x2)); }
DI float rbf(float x) { return __uint_as_float(cvtpk(x, x) << 16); }
DI s16x8 asfrag(u32x4 v) { return __builtin_bit_cast(s16x8, v); }
DI s16x8 lds_frag(lptr p) { return *(const LAS s16x8*)p; }
DI void lds_put(lptr p, u32x4 v) { *(LAS u32x4*)p = v; }
DI s16x4 lds_tr(lptr p) { return __builtin_amdgcn_ds_read_tr16_b64_v4i16((LAS s16x4*)p); }
DI s16x8 tr_frag2(lptr p, lptr q) { s16x4 lo = lds_tr(p), hi = lds_tr(q); return __builtin_shufflevector(lo, hi, 0, 1, 2, 3, 4, 5, 6, 7); }
DI s16x8 tr_frag(lptr p, int second) { return tr_frag2(p, p + second); }
DI float swap_add(float v) { auto r = __builtin_amdgcn_permlane32_swap(__float_as_uint(v), __float_as_uint(v), false, false); return __uint_as_float(r[0]) + __uint_as_float(r[1]); }
DI float swap_max(float v) { auto r = __builtin_amdgcn_permlane32_swap(__float_as_uint(v), __float_as_uint(v), false, false); return fmaxf(__uint_as_float(r[0]), __uint_as_float(r[1])); }
DI float dot2(unsigned a, unsigned b, float c) { return __builtin_amdgcn_fdot2_f32_bf16(__builtin_bit_cast(bf16x2, a), __builtin_bit_cast(bf16x2, b), c, false); }
DI unsigned relu_pk(unsigned u) { return __builtin_bit_cast(unsigned, __builtin_elementwise_max(__builtin_bit_cast(s16x2, u), (s16x2){0, 0})); }
template <int CTRL> DI float dpp(float v) { return __uint_as_float(__builtin_amdgcn_update_dpp(0, __float_as_uint(v), CTRL, 0xf, 0xf, true)); }
DI float row32_max(float v) {
    v = fmaxf(v, dpp<0xB1>(v)); v = fmaxf(v, dpp<0x4E>(v)); v = fmaxf(v, dpp<0x141>(v)); v = fmaxf(v, dpp<0x140>(v));
    auto r = __builtin_amdgcn_permlane16_swap(__float_as_uint(v), __float_as_uint(v), false, false);
    return fmaxf(__uint_as_float(r[0]), __uint_as_float(r[1]));
}
template <int HALF> DI u32x4 pack8(const f32x16& a, float m) {
    u32x4 v;
#pragma unroll
    for (int e = 0; e < 4; ++e) v[e] = cvtpk(a[8 * HALF + 2 * e] * m, a[8 * HALF + 2 * e + 1] * m);
    return v;
}
template <int HALF> DI u32x4 pack8_relu(const f32x16& a) {
    u32x4 v;
#pragma unroll
    for (int e = 0; e < 4; ++e) v[e] = relu_pk(cvtpk(a[8 * HALF + 2 * e], a[8 * HALF + 2 * e + 1]));
    return v;
}
template <int HALF> DI f32x16 pv_mma(s16x8 v, const f32x16& p, f32x16 c, int idx) {
    const auto ph = __builtin_shufflevector(p, p, 8 * HALF, 8 * HALF + 1, 8 * HALF + 2, 8 * HALF + 3, 8 * HALF + 4, 8 * HALF + 5, 8 * HALF + 6, 8 * HALF + 7);
    return __builtin_amdgcn_smfmac_f32_32x32x32_bf16(__builtin_bit_cast(bf16x8, v), __builtin_bit_cast(bf16x16, ph), c, idx, 0, 0);
}
DI void glds16(const void* sbase, unsigned voff, unsigned lds_base) {
    asm volatile("s_mov_b32 m0, %2\n\ts_nop 0\n\tglobal_load_lds_dwordx4 %0, %1" :: "v"(voff), "s"(sbase), "s"(lds_base) : "memory");
}
DI void glds16x2(const void* sbase, unsigned voff0, unsigned voff1, unsigned lds_base) {
    asm volatile("s_mov_b32 m0, %3\n\ts_nop 0\n\tglobal_load_lds_dwordx4 %0, %2\n\tglobal_load_lds_dwordx4 %1, %2 offset:1024" :: "v"(voff0), "v"(voff1), "s"(sbase), "s"(lds_base) : "memory");
}
DI void glds16x4(const void* sbase, unsigned v0, unsigned v1, unsigned v2, unsigned v3, unsigned lds_base) {
    asm volatile("s_mov_b32 m0, %5\n\ts_nop 0\n\tglobal_load_lds_dwordx4 %0, %4\n\tglobal_load_lds_dwordx4 %1, %4 offset:1024\n\t"
                 "global_load_lds_dwordx4 %2, %4 offset:2048\n\tglobal_load_lds_dwordx4 %3, %4 offset:3072" :: "v"(v0), "v"(v1), "v"(v2), "v"(v3), "s"(sbase), "s"(lds_base) : "memory");
}
DI void glds16x2_sc1(const void* sbase, unsigned voff, unsigned lds_base) {
    asm volatile("s_mov_b32 m0, %2\n\ts_nop 0\n\tglobal_load_lds_dwordx4 %0, %1 sc1\n\tglobal_load_lds_dwordx4 %0, %1 offset:1024 sc1" :: "v"(voff), "s"(sbase), "s"(lds_base) : "memory");
}
DI void touch8k(const void* sbase, int lane, unsigned lds_scratch) {
    asm volatile("s_mov_b32 m0, %2\n\ts_nop 0\n\tglobal_load_lds_dword %0, %1" :: "v"(lane * 128), "s"(sbase), "s"(lds_scratch) : "memory");
}
#define WAIT_V(N) asm volatile("s_waitcnt vmcnt(" #N ")" ::: "memory")
DI __amdgpu_buffer_rsrc_t mk_rsrc(const void* p, unsigned bytes) { return __builtin_amdgcn_make_buffer_rsrc((void*)p, 0, bytes, 0x00020000); }
DI u32x4 bload(__amdgpu_buffer_rsrc_t r, unsigned voff, unsigned soff) { return __builtin_amdgcn_raw_buffer_load_b128(r, voff, soff, 0); }
DI u32x4 bload_sc1(__amdgpu_buffer_rsrc_t r, unsigned voff, unsigned soff) { return __builtin_amdgcn_raw_buffer_load_b128(r, voff, soff, 16); }
DI void bstore_sc1(u32x4 v, __amdgpu_buffer_rsrc_t r, unsigned voff) { __builtin_amdgcn_raw_buffer_store_b128(v, r, voff, 0, 16); }

#define STAMP(i) do { if ((i) == 0) tq0 = __builtin_amdgcn_s_memrealtime(); if ((i) == 13) tq1 = __builtin_amdgcn_s_memrealtime(); } while (0)

struct BigFrag { s16x8 x0, x1, w0, w1; };
struct SmallFrag { s16x8 x, q; };

__global__ void __launch_bounds__(512, 2)
fused_kernel(const uint16_t* __restrict__ x, const uint16_t* __restrict__ wq, const uint16_t* __restrict__ wk, const uint16_t* __restrict__ wv,
             const uint16_t* __restrict__ wo, const uint16_t* __restrict__ w1, const uint16_t* __restrict__ w2, const uint16_t* __restrict__ gw,
             uint16_t* out, char* ws) {
    __shared__ __attribute__((aligned(16))) char smem[LDS_BYTES];
    const lptr lds = (lptr)smem;
    const unsigned lds0 = (unsigned)(uintptr_t)smem;
    const int tid = threadIdx.x, lane = tid & 63, w = __builtin_amdgcn_readfirstlane(tid >> 6);
    const int r = lane & 31, h = lane >> 5, g1 = (lane >> 4) & 1, q4 = (lane & 15) >> 2, p4 = lane & 3;
    const int b = blockIdx.x, bi = b >> 3;
    const int R = 2 * (b & 7) + (bi >> 4), cb = bi & 15, rh64 = cb & 1, hg = cb >> 1;
    const bool kvcu = cb >= 14, big = w < 4;
    const int row64 = R * 128 + rh64 * 64;
    const int rt = w & 1, hq = w >> 1, fh = hq & 1;
    unsigned stv = 0; unsigned long long tq0 = 0, tq1 = 0;
    STAMP(0);

    {
        const char* pc;
        asm volatile("s_getpc_b64 %0" : "=s"(pc));
        if (w < 3) touch8k(pc + w * 7168, lane, lds0 + L_TOUCH);
    }
    asm volatile("" :: "s"(x), "s"(wq), "s"(wk), "s"(wv), "s"(wo), "s"(w1), "s"(w2), "s"(gw), "s"(out), "s"(ws));
    const unsigned w64_src = (unsigned)((lane >> 3) * 1024 + hg * 128 + (((lane & 7) ^ (g1 << 2)) << 4));
    const unsigned w64_src1 = w64_src + 8192 - 1024;
    const unsigned xv = (unsigned)((lane >> 3) * 1024 + (((lane & 7) ^ ((lane >> 4) & 3)) << 4)), xv1 = (xv ^ 64) + 8192 - 1024;
    const unsigned wv0 = (unsigned)((lane >> 4) * (DFF * 2) + cb * 256 + (((lane & 15) ^ (((lane >> 4) & 3) << 2)) << 4)), wv1 = wv0 + 4 * DFF * 2 - 1024;
    const int rot = (b + cb) & 7;
    STAMP(21);
    if (!kvcu) {
#pragma unroll
        for (int s = 0; s < 2; ++s) {
            const int sr = (s + rot) & 7;
            glds16x2((const char*)x + R * 131072 + sr * 128 + w * 16384, xv, xv1, lds0 + s * L_STAGE + w * 2048);
            glds16x2((const char*)w1 + (size_t)(sr * 64 + w * 8) * (DFF * 2), wv0, wv1, lds0 + s * L_STAGE + 16384 + w * 2048);
            glds16((const char*)wq + sr * 65536 + w * 8192, w64_src, lds0 + s * L_STAGE + 32768 + w * 1024);
        }
    } else {
        const char* wkv = (const char*)((w & 2) ? wk : wv);
        const unsigned kvv = (w & 1) * 1024 + ((lane ^ ((w & 2) << 2)) << 4);
#pragma unroll 1
        for (int s = 0; s < 8; ++s) {
            glds16((const char*)x + (row64 + 8 * w) * 1024 + s * 128, xv ^ (rt << 6), lds0 + s * L_KSTAGE + w * 1024);
            if (!big) glds16(wkv + s * 2048, kvv, lds0 + s * L_KSTAGE + 8192 + (w & 3) * 1024);
        }
    }
    STAMP(20);
    const unsigned eo = lane == 1 ? WS_HFLAG + (R * 16 + cb) * 128 : !kvcu || lane == 0 || lane > 6 ? WS_EPOCH : lane == 2 ? WS_KVFLAG + (row64 >> 6) * 128 : WS_GZ + row64 * 8 + (lane - 3) * 128;
    const unsigned epoch_v = __hip_atomic_load((gu32*)(ws + eo), RLX_AGENT);
    asm volatile("" ::: "memory");
    const __amdgpu_buffer_rsrc_t rs_kv = mk_rsrc(ws + WS_KV, KV_BYTES), rs_h = mk_rsrc(ws + WS_H, H_BYTES), rs_o = mk_rsrc(ws + WS_O, O_BYTES);
    const __amdgpu_buffer_rsrc_t rs_kf = mk_rsrc(ws + WS_KVFLAG, 4096);
    const unsigned tr_in = 32 * g1 + 8 * p4;
    const unsigned wq_rd = (8 * h + q4) * 128 + ((fh ^ (q4 >> 1)) << 6) + tr_in;
    unsigned xo[4];
#pragma unroll
    for (int ks = 0; ks < 4; ++ks) xo[ks] = r * 128 + (((2 * ks + h) ^ ((r >> 1) & 7)) << 4);

    lptr kp, vp0, vp1, wrA0, wrA1, wrB0, wrB1, q_rd, qn_rd, km_rd, cst_wr;
    unsigned h_voff, hf_voff, g_voff0, g_voff1, hb, hb1, o_own;
    int spidx;
    f32x16 oacc;
    u32x4 cst, omark;
    s16x8 zf;
    auto consts = [&]() {
        kp = lds + lane * 16;
        const unsigned v_cst = (r & 27) != 16 ? 2064 : 2048;
        vp0 = lds + (r < 16 ? 1024 + h * 256 + r * 16 : v_cst); vp1 = lds + (r < 16 ? 1536 + h * 256 + r * 16 : v_cst);
        const unsigned w_rd0 = w * 2048 + (4 * h + q4) * 128 + ((q4 >> 1) << 6) + tr_in;
        wrA0 = lds + L_WB + w_rd0; wrA1 = lds + L_WB + (w_rd0 ^ 64); wrB0 = lds + w_rd0; wrB1 = lds + (w_rd0 ^ 64);
        q_rd = lds + L_QX + (hq * 2 + rt) * 1024 + lane * 16; qn_rd = lds + L_QN + (hq * 2 + rt) * 128 + r * 4; km_rd = lds + L_KM;
        h_voff = (unsigned)((row64 >> 5) * 112 + 14 * w) * 1024u + lane * 16;
        hf_voff = (unsigned)(R * 16 + (lane & 15)) * 128 + 4; g_voff0 = 32768 + (row64 + 32 * rt + r) * 8; g_voff1 = 32768 + (row64 + 8 * w + ((lane >> 1) & 4) + (lane >> 4)) * 8 + 4;
        spidx = 0xDDDD; oacc = (f32x16){}; zf = (s16x8){};
        cst = (lane & 1) ? (u32x4){} : (u32x4){0x3f803f80u, 0x3f803f80u, 0x3f803f80u, 0x3f803f80u};
        cst_wr = lds + (tid >> 1) * KVT + 2048 + (tid & 1) * 16;
        hb = (kvcu ? OOB : (unsigned)((R * 4 + 2 * rt) * 112 + cb * 8 + fh * 4) * 1024u) + lane * 16; hb1 = hb + 112 * 1024;
        o_own = (unsigned)(((row64 >> 5) + rt) * 32 + hg * 4 + hq) * 1024u + lane * 16;
        omark = (u32x4){~0u, ~0u, ~0u, ~0u};
        PIN(oacc); PIN(spidx); PIN(cst); PIN(cst_wr); PIN(hb); PIN(hb1); PIN(zf); PIN(o_own); PIN(omark);
        PIN(kp); PIN(vp0); PIN(vp1); PIN(wrA0); PIN(wrA1); PIN(wrB0); PIN(wrB1); PIN(q_rd); PIN(qn_rd); PIN(km_rd);
        PIN(h_voff); PIN(hf_voff); PIN(g_voff0); PIN(g_voff1);
        SBAR();
    };
    u32x4 hs[8];
#pragma unroll
    for (int i = 0; i < 8; ++i) asm volatile("" : "=v"(hs[i]));
    f32x16 a00 = {}, a01 = {}, a10 = {}, a11 = {};
    auto kv_wait = [&](u32x4& fv) {
        const unsigned ep = __builtin_amdgcn_readfirstlane(epoch_v) + 1u;
        if (__builtin_expect(!__all(fv[1] == ep && fv[3] == ep), 0)) {
            unsigned spins = 0;
            do { __builtin_amdgcn_s_sleep(6); CFENCE(); fv = bload_sc1(rs_kf, r * 128 + h * 16, 0); } while (!__all(fv[1] == ep && fv[3] == ep) && ++spins < SPIN_CAP);
        }
        CFENCE();
    };
    auto kv_bound = [&](const u32x4& fv) {
        const float km = row32_max(fmaxf(__uint_as_float(fv[0]), __uint_as_float(fv[2])));
        if (tid == 256) *(LAS float*)(lds + L_KM) = __builtin_amdgcn_sqrtf(km) * 1.0001f;
    };
    auto kv_tile = [&](int t) { glds16x2_sc1(ws + WS_KV + ((t + 8 * bi) & 63) * 2048, lane * 16, lds0 + t * KVT); };
    if (!kvcu) {
        unsigned cur = 0, nxt = L_STAGE;
        if (big) {
            const unsigned xb = rt * 8192, w0o = 16384 + (8 * h + q4) * 256 + (((2 * fh) ^ q4) << 6) + tr_in, w1o = w0o ^ 64;
            BigFrag F[4];
            auto rd = [&](BigFrag& n, lptr sb, int ks) {
                n.x0 = lds_frag(sb + xb + xo[ks]); n.x1 = lds_frag(sb + xb + 4096 + xo[ks]); n.w0 = tr_frag(sb + w0o + ks * 4096, 1024); n.w1 = tr_frag(sb + w1o + ks * 4096, 1024); };
#define AM(e, ...) do { if (ABL & 2048) asm volatile("" :: __VA_ARGS__); else e; SBAR(); } while (0)
#define AR(e, ...) do { if (ABL & 4096) asm volatile("" : __VA_ARGS__); else { e; } SBAR(); } while (0)
            auto step = [&](const BigFrag& c, BigFrag& n, lptr sb, int ks) {
                AM(a00 = MFMA(c.w0, c.x0, a00), "v"(c.w0), "v"(c.x0)); AR(n.x0 = lds_frag(sb + xb + xo[ks]); n.x1 = lds_frag(sb + xb + 4096 + xo[ks]), "+v"(n.x0), "+v"(n.x1));
                AM(a10 = MFMA(c.w0, c.x1, a10), "v"(c.x1)); AR(n.w0 = tr_frag(sb + w0o + ks * 4096, 1024), "+v"(n.w0));
                AM(a01 = MFMA(c.w1, c.x0, a01), "v"(c.w1)); AR(n.w1 = tr_frag(sb + w1o + ks * 4096, 1024), "+v"(n.w1));
                AM(a11 = MFMA(c.w1, c.x1, a11), "v"(c.w1)); };
            auto stage = [&](auto bar) {
                step(F[0], F[3], lds + cur, 3);
                bar();
                step(F[1], F[0], lds + nxt, 0); step(F[2], F[1], lds + nxt, 1); step(F[3], F[2], lds + nxt, 2);
                cur = nxt; nxt = (nxt == 2 * L_STAGE) ? 0 : nxt + L_STAGE; };
            consts();
            STAMP(13);
            BAR_VL(6);
            STAMP(14);
            rd(F[0], lds, 0); rd(F[1], lds, 1); rd(F[2], lds, 2);
#pragma unroll 1
            for (int s = 0; s < 7; ++s) stage([&] {
                BAR_VL(1);
                if (s == 3 && w < 2) touch8k(w == 0 ? (const char*)w2 + (R * 14 + cb) * 8192 : (const char*)wo + (b & 63) * 8192, lane, lds0 + L_TOUCH); });
            STAMP(1);
            rd(F[3], lds + cur, 3);
#define LAST4(acc, wm, xm) do { _Pragma("unroll") for (int ks = 0; ks < 4; ++ks) acc = MFMA(F[ks].wm, F[ks].xm, acc); } while (0)
#define LAST4P(acc, wm, xm, prev, i) do { acc = MFMA(F[0].wm, F[0].xm, acc); acc = MFMA(F[1].wm, F[1].xm, acc); SBAR(); hs[i] = pack8_relu<0>(prev); PIN(hs[i]); SBAR(); \
            acc = MFMA(F[2].wm, F[2].xm, acc); SBAR(); hs[(i) + 1] = pack8_relu<1>(prev); PIN(hs[(i) + 1]); SBAR(); acc = MFMA(F[3].wm, F[3].xm, acc); SBAR(); } while (0)
            LAST4(a00, w0, x0); SBAR();
            LAST4P(a01, w1, x0, a00, 0); LAST4P(a10, w0, x1, a01, 2); LAST4P(a11, w1, x1, a10, 4);
            hs[6] = pack8_relu<0>(a11); hs[7] = pack8_relu<1>(a11);
        } else {
            const int j = w - 4;
            __builtin_amdgcn_s_setprio(2);
            const unsigned xb = (2 * rh64 + rt) * 4096, qo = 32768 + wq_rd;
            SmallFrag F[4];
            auto rd = [&](SmallFrag& n, lptr sb, int ks) { n.x = lds_frag(sb + xb + xo[ks]); n.q = tr_frag(sb + qo + ks * 2048, 512); SBAR(); };
            auto mma = [&](const SmallFrag& c) { a00 = MFMA(c.q, c.x, a00); SBAR(); };
            const unsigned xv2 = xv + 2 * (8192 - 1024), xv3 = xv1 + 2 * (8192 - 1024), wv2 = wv0 + 2 * (4 * DFF * 2 - 1024), wv3 = wv1 + 2 * (4 * DFF * 2 - 1024);
            const char* xs = (const char*)x + R * 131072 + j * 32768;
            const char* w1s = (const char*)w1 + (size_t)(j * 16) * (DFF * 2);
            const char* wqs = (const char*)wq + j * 16384;
            auto fill0 = [&](int sr, unsigned slot) { glds16x4(xs + sr * 128, xv, xv1, xv2, xv3, lds0 + slot + j * 4096); };
            auto fill1 = [&](int sr, unsigned slot) { glds16x4(w1s + sr * (64 * DFF * 2), wv0, wv1, wv2, wv3, lds0 + slot + 16384 + j * 4096); };
            auto fill2 = [&](int sr, unsigned slot) { glds16x2(wqs + sr * 65536, w64_src, w64_src1, lds0 + slot + 32768 + j * 2048); };
            fill0((rot + 2) & 7, 2 * L_STAGE); fill1((rot + 2) & 7, 2 * L_STAGE); fill2((rot + 2) & 7, 2 * L_STAGE);
            consts();
            BAR_VL(16);
            rd(F[0], lds, 0); rd(F[1], lds, 1); rd(F[2], lds, 2);
#pragma unroll 1
            for (int s = 0; s < 6; ++s) {
                const bool more = s < 5;
                const int sr = (rot + s + 3) & 7;
                rd(F[3], lds + cur, 3); mma(F[0]);
                BAR_VL(10);
                if (more) fill0(sr, cur);
                rd(F[0], lds + nxt, 0); mma(F[1]);
                if (more) fill1(sr, cur);
                rd(F[1], lds + nxt, 1); mma(F[2]);
                if (more) fill2(sr, cur);
                rd(F[2], lds + nxt, 2); mma(F[3]);
                cur = nxt; nxt = (nxt == 2 * L_STAGE) ? 0 : nxt + L_STAGE;
            }
            u32x4 fv = bload_sc1(rs_kf, r * 128 + h * 16, 0);
            rd(F[3], lds, 3); mma(F[0]);
            BAR_VL(0);
            rd(F[0], lds + L_STAGE, 0); mma(F[1]);
            STAMP(15);
            kv_wait(fv);
            STAMP(16);
            kv_tile(j); kv_tile(4 + j);
            rd(F[1], lds + L_STAGE, 1); mma(F[2]);
            kv_tile(8 + j); kv_tile(12 + j);
            rd(F[2], lds + L_STAGE, 2); mma(F[3]); rd(F[3], lds + L_STAGE, 3);
            mma(F[0]); kv_bound(fv); mma(F[1]); mma(F[2]); mma(F[3]);
        }
    } else {
        const unsigned xb = rt * 4096, w64_src2 = w64_src + 2 * (8192 - 1024), w64_src3 = w64_src + 3 * (8192 - 1024);
        {
            const unsigned wkv_rd = 8192 + g1 * 2048 + (((8 * h + q4) * 32 + 8 * p4) ^ (g1 << 7));
#pragma unroll
            for (int g = 0; g < 2; ++g) {
                if (g == 0) { if (big) BAR_VL(5); else BAR_VL(9); } else BAR_VL(1);
                const lptr st = lds + (g * 4 + hq) * L_KSTAGE;
#pragma unroll
                for (int k = 0; k < 4; ++k) a00 = MFMA(tr_frag2(st + wkv_rd + k * 512, st + (wkv_rd ^ 128) + k * 512), lds_frag(st + xb + xo[k]), a00);
            }
        }
        BAR_L();
        {
            const lptr pw = lds + w * L_KSTAGE + 8192 + r * 128;
            const unsigned sz = (r & 7) ^ ((r >> 1) & 4);
#pragma unroll
            for (int i = 0; i < 4; ++i) *(LAS f32x4*)(pw + (((2 * i + h) ^ sz) << 4)) = (f32x4){a00[4 * i], a00[4 * i + 1], a00[4 * i + 2], a00[4 * i + 3]};
            a00 = (f32x16){};
        }
        BAR_L();
        if (big) {
            STAMP(1);
            const unsigned kvo = (unsigned)((row64 >> 5) + rt) * 2048u + lane * 16;
            const lptr pr = lds + rt * L_KSTAGE + 8192;
            float n2 = 0.f;
            if (fh == 0) {
                const unsigned sz = (r & 7) ^ ((r >> 1) & 4);
                f32x4 lo = {}, hi = {};
#pragma unroll
                for (int i = 0; i < 4; ++i) { lo += *(const LAS f32x4*)(pr + i * 2 * L_KSTAGE + r * 128 + (((4 + h) ^ sz) << 4)); hi += *(const LAS f32x4*)(pr + i * 2 * L_KSTAGE + r * 128 + (((6 + h) ^ sz) << 4)); }
                const u32x4 v = {cvtpk(lo[0], lo[1]), cvtpk(lo[2], lo[3]), cvtpk(hi[0], hi[1]), cvtpk(hi[2], hi[3])};
                bstore_sc1(v, rs_kv, kvo);
                n2 = row32_max(swap_add(dot2(v[0], v[0], dot2(v[1], v[1], 0.f)) + dot2(v[2], v[2], dot2(v[3], v[3], 0.f))));
            } else {
                float t[8];
#pragma unroll
                for (int j = 0; j < 8; ++j) {
                    const lptr q = pr + ((lane >> 4) * 8 + j) * 128 + ((q4 ^ j ^ (g1 << 2)) << 4) + p4 * 4;
                    t[j] = (*(const LAS float*)q + *(const LAS float*)(q + 2 * L_KSTAGE)) + (*(const LAS float*)(q + 4 * L_KSTAGE) + *(const LAS float*)(q + 6 * L_KSTAGE));
                }
                bstore_sc1((u32x4){cvtpk(t[0], t[1]), cvtpk(t[2], t[3]), cvtpk(t[4], t[5]), cvtpk(t[6], t[7])}, rs_kv, kvo + 1024);
            }
            STAMP(13);
            WAIT_V(0);
            STAMP(14);
            if (lane == 0) __hip_atomic_store((gu64*)(ws + WS_KVFLAG) + (row64 >> 6) * 16 + w, ((unsigned long long)(epoch_v + 1u) << 32) | __float_as_uint(n2), RLX_AGENT);
            u32x4 gf[16];
#pragma unroll
            for (int i = 0; i < 16; ++i) gf[i] = *(const u32x4*)(gw + i * 32 + (lane >> 4) * 8);
            consts();
            BAR_L();
            f32x4 z = {};
            const int zr = 16 * w + (lane & 15);
#pragma unroll
            for (int i = 0; i < 16; ++i)
                z = __builtin_amdgcn_mfma_f32_16x16x32_bf16(asfrag(gf[i]), lds_frag(lds + (i >> 1) * L_KSTAGE + zr * 128 + (((4 * (i & 1) + (lane >> 4)) ^ ((zr >> 1) & 7)) << 4)), z, 0, 0, 0);
            const float gt = rbf(__builtin_amdgcn_rcpf(1.f + __expf(-rbf(z[0])))), og = fmaxf(rbf(1.f - gt), 0x1p-40f);
            if (lane < 16) __hip_atomic_store((gu64*)(ws + WS_GZ) + row64 + zr, ((unsigned long long)__float_as_uint(og) << 32) | __float_as_uint(gt / og), RLX_AGENT);
            BAR_L();
        } else {
#pragma unroll
            for (int i = 0; i < 4; ++i) { const int p = i * 16 + (w - 4) * 4; glds16x4((const char*)wq + p * 8192, w64_src, w64_src1, w64_src2, w64_src3, lds0 + L_KWQ + p * 1024); }
            consts();
            BAR_VL(0);
            SmallFrag F[4];
            auto rd = [&](SmallFrag& n, int s, int k) { n.x = lds_frag(lds + s * L_KSTAGE + xb + xo[k]); n.q = tr_frag(lds + L_KWQ + s * 8192 + wq_rd + k * 2048, 512); SBAR(); };
#pragma unroll
            for (int k = 0; k < 4; ++k) rd(F[k], 0, k);
#pragma unroll 1
            for (int s = 1; s < 8; ++s) {
#pragma unroll
                for (int k = 0; k < 4; ++k) { a00 = MFMA(F[k].q, F[k].x, a00); SBAR(); rd(F[k], s, k); }
            }
            u32x4 fv = bload_sc1(rs_kf, r * 128 + h * 16, 0);
#pragma unroll
            for (int k = 0; k < 4; ++k) a00 = MFMA(F[k].q, F[k].x, a00);
            BAR_L();
            STAMP(15);
            kv_wait(fv);
            STAMP(16);
            kv_tile(w - 4); kv_tile(w); kv_tile(4 + w); kv_tile(8 + w);
            kv_bound(fv);
        }
    }
    STAMP(2);
    if (big) {
        if (tid < 32) lds_put(cst_wr, cst);
    } else {
#pragma unroll
        for (int s = 0; s < 2; ++s) {
            const u32x4 v = s ? pack8<1>(a00, QSCALE) : pack8<0>(a00, QSCALE);
            lds_put(lds + L_QX + (4 * fh + 2 * s + rt) * 1024 + lane * 16, v);
            const float n2 = swap_add(dot2(v[0], v[0], dot2(v[1], v[1], 0.f)) + dot2(v[2], v[2], dot2(v[3], v[3], 0.f)));
            *(LAS float*)(lds + L_QN + (4 * fh + 2 * s + rt) * 128 + r * 4) = __builtin_amdgcn_sqrtf(n2);
        }
    }
    __builtin_amdgcn_s_setprio(3);
    BAR_VL(0);
    STAMP(3);
    const s16x8 qf = lds_frag(q_rd);
    s16x8 kf = lds_frag(kp), vf0, vf1;
    float mshift = *(const LAS float*)qn_rd * *(const LAS float*)km_rd;
    vf0 = lds_frag(vp0);
    if (__builtin_expect(!__all(mshift <= 0.5f * SAFE_RANGE), 0)) {
        float m = -__builtin_inff();
#pragma unroll 1
        for (int t = 0; t < 64; ++t) {
            const f32x16 s = MFMA(asfrag(bload_sc1(rs_kv, lane * 16, t * 2048)), qf, (f32x16){});
#pragma unroll
            for (int i = 0; i < 16; ++i) m = fmaxf(m, s[i]);
        }
        mshift = swap_max(m);
    }
    f32x16 cinit;
#pragma unroll
    for (int i = 0; i < 16; ++i) cinit[i] = -mshift;
    PIN(cinit);
    f32x16 sA = MFMA(kf, qf, cinit), sB;
    kf = lds_frag(kp + KVT);
    if (tid >= 32 && tid < 128) lds_put(cst_wr, cst);

#define S1(S_, i) S_[i] = __builtin_amdgcn_exp2f(S_[i])
#define EX2(S_, i) do { S1(S_, i); S1(S_, (i) + 1); } while (0)
#define EX3(S_, i) do { S1(S_, i); S1(S_, (i) + 1); S1(S_, (i) + 2); } while (0)
#define EX4(S_, i) do { EX2(S_, i); EX2(S_, (i) + 2); } while (0)
#define PV1(SN, T) do { if ((T) >= 0) oacc = pv_mma<1>(vf1, SN, oacc, spidx); vf1 = lds_frag(vp1 + ((T) < 0 ? 0 : (T)) * KVT); SBAR(); } while (0)
#define QK(SN, T) do { SN = MFMA(kf, qf, cinit); kf = lds_frag(kp + ((T) + 2) * KVT); SBAR(); } while (0)
#define PV0(SC, T) do { oacc = pv_mma<0>(vf0, SC, oacc, spidx); vf0 = lds_frag(vp0 + ((T) + 1) * KVT); SBAR(); } while (0)
#define CUT(S_) do { PIN(S_); SBAR(); } while (0)
#define ITER0(SC, SN, T, T1, PR, MEM) do { PR; \
    EX2(SC, 0); CUT(SC); PV1(SN, T1); EX4(SC, 2); CUT(SC); QK(SN, T); EX4(SC, 6); S1(SC, 10); CUT(SC); PV0(SC, T); MEM; S1(SC, 11); EX4(SC, 12); CUT(SC); } while (0)
#define ITER1(SC, SN, T, PR, PRE, MEM, G1) do { PR; PRE; \
    EX2(SC, 0); CUT(SC); PV1(SN, T); EX4(SC, 2); CUT(SC); QK(SN, T); EX4(SC, 6); CUT(SC); PV0(SC, T); MEM; EX4(SC, 10); CUT(SC); G1; SBAR(); EX2(SC, 14); CUT(SC); } while (0)
#define ITERG(SC, SN, T, PR, MEM, G0, G1) do { PR; \
    EX2(SC, 0); CUT(SC); PV1(SN, T); EX3(SC, 2); CUT(SC); QK(SN, T); EX3(SC, 5); CUT(SC); G0; SBAR(); EX3(SC, 8); CUT(SC); PV0(SC, T); MEM; EX3(SC, 11); CUT(SC); G1; SBAR(); EX2(SC, 14); CUT(SC); } while (0)
#define NOP (void)0
#define PRIO(p) __builtin_amdgcn_s_setprio(p)
#define ITER2(T, P0, P1, M0, M1) do { ITER0(sA, sB, T, T, PRIO(P0), M0); ITER0(sB, sA, (T) + 1, (T) + 1, PRIO(P1), M1); } while (0)
#define ADV(n) do { kp += (n) * KVT; vp0 += (n) * KVT; vp1 += (n) * KVT; PIN(kp); PIN(vp0); PIN(vp1); } while (0)
    const char* const w2w = (const char*)w2 + w * (14 * 16384);
    const char* const wow = (const char*)wo + w * 65536;
    int pn = (row64 >> 6) % 7;
    const unsigned wbA = lds0 + L_WB + w * 2048, wbB = lds0 + w * 2048;
    const __amdgpu_buffer_rsrc_t rs_f = mk_rsrc(ws + WS_HFLAG, 32768 + 16384);
    u32x4 hA0, hA1, hB0, hB1;
    s16x8 wf0, wf1;
#define W_LD(src, buf) glds16x2(src, w64_src, w64_src1, buf)
    STAMP(4);
#define H_ST(i) do { if (big) bstore_sc1(hs[i], rs_h, ((i) < 4 ? hb : hb1) + ((i) & 3) * 1024); } while (0)
    ITER0(sA, sB, 0, -1, PRIO(3), H_ST(0)); ITER0(sB, sA, 1, 1, NOP, H_ST(1); kv_tile(16 + w));
    ITER2(2, 3, 3, H_ST(2); kv_tile(24 + w), H_ST(3); kv_tile(32 + w));
    ITER2(4, 2, 2, H_ST(4); kv_tile(40 + w), H_ST(5); kv_tile(48 + w));
    ITER2(6, 2, 2, H_ST(6); kv_tile(56 + w), H_ST(7); W_LD(w2w + pn * 32768, wbA));
    STAMP(17);
    ITER2(8, 1, 1, bstore_sc1(omark, rs_o, o_own), NOP);
    ITER2(10, 1, 1, NOP, NOP);
    ITER2(12, 0, 0, NOP, NOP);
    STAMP(18);
    STAMP(19);
    BAR_VL(0);
    unsigned epoch_w = epoch_v; PIN(epoch_w);
    const unsigned epoch = __builtin_amdgcn_readfirstlane(epoch_w) + 1u;
    if (tid == 0) __hip_atomic_store((gu32*)(ws + WS_HFLAG) + (R * 16 + cb) * 32 + 1, epoch, RLX_AGENT);
    STAMP(5);
    unsigned hfv;
    ITER2(14, 1, 3, W_LD(w2w + pn * 32768 + 16384, wbB); a00 = MFMA(zf, zf, (f32x16){}); PIN(zf); a01 = MFMA(zf, zf, (f32x16){}), PIN(zf); a10 = MFMA(zf, zf, (f32x16){}); PIN(zf); a11 = MFMA(zf, zf, (f32x16){}));
    ITER2(16, 2, 1, NOP, hfv = __builtin_amdgcn_raw_buffer_load_b32(rs_f, hf_voff, 0, 16));
    ITER0(sA, sB, 18, 18, PRIO(3), NOP);
    STAMP(6);
    if (__builtin_expect(!__all(hfv == epoch), 0)) {
        unsigned spins = 0;
        do { __builtin_amdgcn_s_sleep(6); CFENCE(); hfv = __builtin_amdgcn_raw_buffer_load_b32(rs_f, hf_voff, 0, 16); } while (!__all(hfv == epoch) && ++spins < SPIN_CAP);
    }
    CFENCE();
    STAMP(7);
    unsigned gr_u, og_u;
    ITER0(sB, sA, 19, 19, PRIO(2), gr_u = __builtin_amdgcn_raw_buffer_load_b32(rs_f, g_voff0, 0, 16); og_u = __builtin_amdgcn_raw_buffer_load_b32(rs_f, g_voff1, 0, 16);
          hA0 = bload_sc1(rs_h, h_voff, pn * 2048); hA1 = bload_sc1(rs_h, h_voff, pn * 2048 + 112 * 1024));
    ITER2(20, 1, 3, hB0 = bload_sc1(rs_h, h_voff, pn * 2048 + 1024), hB1 = bload_sc1(rs_h, h_voff, pn * 2048 + 113 * 1024));
    ADV(22);
    PIN(a00); PIN(a01); PIN(a10); PIN(a11);
    STAMP(8);
#define GM(acc, wf, H) acc = MFMA(asfrag(H), wf, acc); SBAR()
#define GSTEP(SX, SY, T, R0, R1, H0, H1, LW, LH0, LH1) do { \
    ITER1(SX, SY, T, PRIO(3), WAIT_V(5); wf0 = tr_frag(R0, 1024); wf1 = tr_frag(R1, 1024), NOP, GM(a00, wf0, H0)); \
    ITERG(SY, SX, (T) + 1, PRIO(2), LW, GM(a01, wf1, H0); LH0, GM(a10, wf0, H1)); \
    ITER1(SX, SY, (T) + 2, PRIO(1), NOP, NOP, GM(a11, wf1, H1); LH1); } while (0)
#pragma unroll 1
    for (int n = 0; n < 4; ++n) {
        pn = pn == 6 ? 0 : pn + 1;
        const char* wsrc = w2w + pn * 32768;
        GSTEP(sA, sB, 0, wrA0, wrA1, hA0, hA1, W_LD(wsrc, wbA), hA0 = bload_sc1(rs_h, h_voff, pn * 2048), hA1 = bload_sc1(rs_h, h_voff, pn * 2048 + 112 * 1024));
        GSTEP(sB, sA, 3, wrB0, wrB1, hB0, hB1, W_LD(wsrc + 16384, wbB), hB0 = bload_sc1(rs_h, h_voff, pn * 2048 + 1024), hB1 = bload_sc1(rs_h, h_voff, pn * 2048 + 113 * 1024));
        ADV(6);
    }
    BAR_L();
    pn = pn == 6 ? 0 : pn + 1;
    const int pm = pn == 6 ? 0 : pn + 1;
    u32x4 hC0, hC1, hD0, hD1, hE0, hE1, hF0, hF1;
    ITER2(0, 3, 2, W_LD(w2w + pn * 32768, wbB + 16384); hC0 = bload_sc1(rs_h, h_voff, pn * 2048), hC1 = bload_sc1(rs_h, h_voff, pn * 2048 + 112 * 1024));
    ITER2(2, 1, 3, W_LD(w2w + pn * 32768 + 16384, wbB + 32768); hD0 = bload_sc1(rs_h, h_voff, pn * 2048 + 1024), hD1 = bload_sc1(rs_h, h_voff, pn * 2048 + 113 * 1024));
    ITER2(4, 2, 1, W_LD(w2w + pm * 32768, wbB + 49152); hE0 = bload_sc1(rs_h, h_voff, pm * 2048), hE1 = bload_sc1(rs_h, h_voff, pm * 2048 + 112 * 1024));
    ITER2(6, 3, 2, W_LD(w2w + pm * 32768 + 16384, wbB + 65536); hF0 = bload_sc1(rs_h, h_voff, pm * 2048 + 1024), hF1 = bload_sc1(rs_h, h_voff, pm * 2048 + 113 * 1024));
    const bool closer = b == 112 && w == 7;
    unsigned ef[4];
    auto ef_load = [&]() {
#pragma unroll
        for (int i = 0; i < 4; ++i) ef[i] = __builtin_amdgcn_raw_buffer_load_b32(mk_rsrc(ws + WS_HFLAG + i * 8192 + 4, closer ? 8188 : 0), lane * 128, 0, 16);
    };
    const unsigned xtra = w < 3 ? 5 * 16384 : L_QX - 3 * 2048;
    s16x8 wf2, wf3, wf4, wf5;
    ITER2(8, 1, 3, WAIT_V(14); wf0 = tr_frag(wrA0, 1024); wf1 = tr_frag(wrA1, 1024); W_LD(wow + 49152, wbB + xtra), wf2 = tr_frag(wrB0, 1024); wf3 = tr_frag(wrB1, 1024); ef_load());
    ITER2(10, 2, 1, wf4 = tr_frag(wrB0 + 16384, 1024); wf5 = tr_frag(wrB1 + 16384, 1024); PIN(wf0); PIN(wf1); W_LD(wow, wbA), PIN(wf2); PIN(wf3); W_LD(wow + 16384, wbB));
    ITER2(12, 3, 3, PIN(wf4); PIN(wf5); W_LD(wow + 32768, wbB + 16384), NOP);
    ITER2(14, 2, 2, NOP, NOP);
    ITER2(16, 1, 0, NOP, NOP);
    STAMP(9);
#define GMA(acc, wf, H) asm volatile("v_mfma_f32_32x32x16_bf16 %0, %1, %2, %0" : "+v"(acc) : "v"(H), "v"(wf))
    PRIO(3);
    oacc = pv_mma<1>(vf1, sB, oacc, spidx);
    {
        u32x4 v = pack8<0>(oacc, __builtin_amdgcn_rcpf(oacc[8]) * __uint_as_float(gr_u));
#pragma unroll
        for (int e = 0; e < 4; ++e) v[e] = v[e] < 0xfffffffeu ? v[e] : 0xfffffffeu;
        bstore_sc1(v, rs_o, o_own);
        asm volatile("s_nop 1");
    }
    PRIO(0);
    GMA(a00, wf0, hA0); GMA(a01, wf1, hA0);
    WAIT_V(1);
    lptr wrE0 = wrB0 + 32768, wrE1 = wrB1 + 32768; PIN(wrE0); PIN(wrE1);
    const s16x8 wf6 = tr_frag(wrE0, 1024), wf7 = tr_frag(wrE1, 1024), wf8 = tr_frag(wrE0 + 16384, 1024), wf9 = tr_frag(wrE1 + 16384, 1024);
    SBAR();
    GMA(a10, wf0, hA1); GMA(a11, wf1, hA1);
    const s16x8 wfa = tr_frag(wrE0 + 32768, 1024), wfb = tr_frag(wrE1 + 32768, 1024);
    s16x8 wof[8];
    wof[0] = tr_frag(wrA0, 1024); wof[1] = tr_frag(wrA1, 1024);
    SBAR();
    GMA(a00, wf2, hB0); GMA(a01, wf3, hB0);
    wof[2] = tr_frag(wrB0, 1024); wof[3] = tr_frag(wrB1, 1024); wof[4] = tr_frag(wrB0 + 16384, 1024); wof[5] = tr_frag(wrB1 + 16384, 1024);
    SBAR();
    GMA(a10, wf2, hB1); GMA(a11, wf3, hB1);
    wof[6] = tr_frag(wrB0 + xtra, 1024); wof[7] = tr_frag(wrB1 + xtra, 1024);
    SBAR();
    GMA(a00, wf4, hC0); GMA(a01, wf5, hC0); GMA(a10, wf4, hC1); GMA(a11, wf5, hC1);
    const unsigned ob = (unsigned)((row64 >> 5) * 32 + 4 * w) * 1024u;
    u32x4 of[8];
    auto o_load = [&]() {
#pragma unroll
        for (int i = 0; i < 8; ++i) of[i] = bload_sc1(rs_o, lane * 16 + (i & 3) * 1024, ob + (i >> 2) * 32768);
    };
    auto o_ready = [&]() {
        unsigned m = 0;
#pragma unroll
        for (int i = 0; i < 8; ++i) { m = max(m, max(of[i][0], of[i][1])); m = max(m, max(of[i][2], of[i][3])); }
        return __all(m != ~0u);
    };
    BAR_VL(0);
#pragma unroll
    for (int i = 0; i < 8; ++i) PIN(wof[i]);
    STAMP(10);
    o_load();
    SBAR();
    GMA(a00, wf6, hD0); GMA(a01, wf7, hD0); GMA(a10, wf6, hD1); GMA(a11, wf7, hD1);
    GMA(a00, wf8, hE0); GMA(a01, wf9, hE0); GMA(a10, wf8, hE1); GMA(a11, wf9, hE1);
    GMA(a00, wfa, hF0); GMA(a01, wfb, hF0); GMA(a10, wfa, hF1); GMA(a11, wfb, hF1);
    SBAR();
    const lptr prb = lds + (((w >> 2) * 2 + ((lane >> 2) & 1)) * 16 + 4 * (w & 3) + (lane >> 4)) * 2048 + ((lane >> 3) & 1) * 128 + p4 * 32;
    lptr pr0 = prb + g1 * 16, pr1 = prb + (g1 ^ 1) * 16;
    PIN(pr0); PIN(pr1);
    uint16_t* outp = out + (row64 + 8 * w + ((lane >> 1) & 4) + (lane >> 4)) * 512 + hg * 64 + (lane & 7) * 8;
    PIN(outp);
    SBAR();
    if (!o_ready()) {
        unsigned spins = 0;
        do { CFENCE(); o_load(); } while (!o_ready() && ++spins < SPIN_CAP);
    }
    STAMP(11);
#define AMM(acc, wi, oi) GMA(acc, wof[wi], of[oi])
#define PUT(a, i, off) asm volatile("ds_write_addtid_b32 %0 offset:%4\n\tds_write_addtid_b32 %1 offset:%5\n\tds_write_addtid_b32 %2 offset:%6\n\tds_write_addtid_b32 %3 offset:%7" \
    :: "v"(a[4 * (i)]), "v"(a[4 * (i) + 1]), "v"(a[4 * (i) + 2]), "v"(a[4 * (i) + 3]), "n"((off) + (i) * 8192), "n"((off) + (i) * 8192 + 2048), "n"((off) + (i) * 8192 + 4096), "n"((off) + (i) * 8192 + 6144) : "memory")
#define SETM0(v, prev) asm volatile("s_mov_b32 m0, %1\n\ts_nop 10" : "+v"(prev) : "s"(v))
#define MM_PUT(acc, c, o, prev, m0v, off) do { AMM(acc, c, o); SETM0(m0v, prev); PUT(prev, 0, off); AMM(acc, 2 + (c), (o) + 1); PUT(prev, 1, off); \
    AMM(acc, 4 + (c), (o) + 2); PUT(prev, 2, off); AMM(acc, 6 + (c), (o) + 3); PUT(prev, 3, off); } while (0)
    const unsigned m0a = lds0 + w * 256, m0b = m0a + 63616;
    asm volatile("s_nop 1");
    AMM(a00, 0, 0); AMM(a00, 2, 1); AMM(a00, 4, 2); AMM(a00, 6, 3);
    MM_PUT(a01, 1, 0, a00, m0a, 0);
    MM_PUT(a10, 0, 4, a01, m0a, 32768);
    MM_PUT(a11, 1, 4, a10, m0b, 65536 - 63616);
    asm volatile("s_nop 7" : "+v"(a11));
    PUT(a11, 0, 98304 - 63616); PUT(a11, 1, 98304 - 63616); PUT(a11, 2, 98304 - 63616); PUT(a11, 3, 98304 - 63616);
    BAR_L();
    {
        f32x4 t[16];
#pragma unroll
        for (int k = 0; k < 8; ++k) { t[2 * k] = *(const LAS f32x4*)(pr0 + k * 256); t[2 * k + 1] = *(const LAS f32x4*)(pr1 + k * 256); }
        SBAR();
        f32x4 lo = t[0], hi = t[1];
#pragma unroll
        for (int k = 1; k < 8; ++k) { lo += t[2 * k]; hi += t[2 * k + 1]; }
        lo *= __uint_as_float(og_u); hi *= __uint_as_float(og_u);
        const unsigned l0 = cvtpk(lo[0], lo[1]), l1 = cvtpk(lo[2], lo[3]), h0 = cvtpk(hi[0], hi[1]), h1 = cvtpk(hi[2], hi[3]);
        const u32x4 v = g1 ? (u32x4){h0, h1, l0, l1} : (u32x4){l0, l1, h0, h1};
        __builtin_nontemporal_store(v, (GAS u32x4*)outp);
    }
    if (closer) {
        unsigned spins = 0;
        while (!__all(ef[0] == epoch && ef[1] == epoch && ef[2] == epoch && ef[3] == epoch) && ++spins < SPIN_CAP) { CFENCE(); ef_load(); }
        if (lane == 0) __hip_atomic_store((gu32*)(ws + WS_EPOCH), epoch, RLX_AGENT);
    }
    STAMP(12);
    if (b == 8 && w == 0) { const unsigned long long tgt = tq0 + 6000 + 10 * (tq1 - tq0); while (__builtin_amdgcn_s_memrealtime() < tgt) __builtin_amdgcn_s_sleep(1); }
}

extern "C" void kernel_launch(void* const* d_in, const int* in_sizes, int n_in, void* d_out, int out_size, void* d_ws, size_t ws_size,
                              hipStream_t stream) {
    fused_kernel<<<256, 512, 0, stream>>>((const uint16_t*)d_in[0], (const uint16_t*)d_in[1], (const uint16_t*)d_in[2], (const uint16_t*)d_in[3],
                                          (const uint16_t*)d_in[4], (const uint16_t*)d_in[6], (const uint16_t*)d_in[7], (const uint16_t*)d_in[8],
                                          (uint16_t*)d_out, (char*)d_ws);
}
```

```cpp
#include <hip/hip_runtime.h>
#include <stdint.h>

typedef short s16x8 __attribute__((ext_vector_type(8)));
typedef short s16x4 __attribute__((ext_vector_type(4)));
typedef short s16x2 __attribute__((ext_vector_type(2)));
typedef float f32x16 __attribute__((ext_vector_type(16)));
typedef float f32x4 __attribute__((ext_vector_type(4)));
typedef float f32x2 __attribute__((ext_vector_type(2)));
typedef unsigned u32x4 __attribute__((ext_vector_type(4)));
typedef __bf16 bf16x2 __attribute__((ext_vector_type(2)));
typedef __bf16 bf16x8 __attribute__((ext_vector_type(8)));
typedef __bf16 bf16x16 __attribute__((ext_vector_type(16)));
#define LAS __attribute__((address_space(3)))
#define GAS __attribute__((address_space(1)))
typedef GAS unsigned gu32;
typedef GAS unsigned long long gu64;
typedef LAS char* lptr;
#define DI __device__ __forceinline__
#define RLX_AGENT __ATOMIC_RELAXED, __HIP_MEMORY_SCOPE_AGENT
#define MFMA(a, b, c) __builtin_amdgcn_mfma_f32_32x32x16_bf16((a), (b), (c), 0, 0, 0)
#define SBAR() __builtin_amdgcn_sched_barrier(0)
#define PIN(x) asm volatile("" : "+v"(x))
#define CFENCE() do { asm volatile("" ::: "memory"); __builtin_amdgcn_fence(__ATOMIC_ACQUIRE, "wavefront"); } while (0)
#define BAR_L() asm volatile("s_waitcnt lgkmcnt(0)\n\ts_barrier" ::: "memory")
#define BAR_VL(N) asm volatile("s_waitcnt vmcnt(" #N ") lgkmcnt(0)\n\ts_barrier" ::: "memory")

#define ABL 0
#define STW 0
#define STAMPS 0

constexpr int D = 512, DFF = 1792;
constexpr float QSCALE = 0.25f * 1.4426950408889634f;
constexpr float SAFE_RANGE = 64.f;
constexpr unsigned WS_EPOCH = 0, WS_KVFLAG = 4096, WS_HFLAG = 8192;
constexpr unsigned WS_GZ = WS_HFLAG + 32768;
constexpr unsigned WS_KV = WS_GZ + 16384, KV_BYTES = 64 * 2048;
constexpr unsigned WS_H = WS_KV + KV_BYTES, H_BYTES = 64 * 112 * 1024;
constexpr unsigned WS_O = WS_H + H_BYTES, O_BYTES = 64 * 32 * 1024;
constexpr unsigned WS_STAMP = WS_O + O_BYTES;
constexpr unsigned OOB = 0x7ff00000u;
constexpr unsigned L_STAGE = 40960;
constexpr unsigned L_KSTAGE = 12288, L_KWQ = 98304;
constexpr unsigned KVT = 2080;
constexpr unsigned L_WB = 64 * KVT;
constexpr unsigned L_QX = L_WB + 16384, L_QN = L_QX + 8192, L_KM = L_QN + 1024, L_TOUCH = L_KM + 256, LDS_BYTES = 163840;
static_assert(L_TOUCH + 256 <= LDS_BYTES, "lds map");
constexpr unsigned SPIN_CAP = 1u << 16;

DI unsigned cvtpk(float a, float b) { f32x2 v = {a, b}; return __builtin_bit_cast(unsigned, __builtin_convertvector(v, bf16x2)); }
DI float rbf(float x) { return __uint_as_float(cvtpk(x, x) << 16); }
DI s16x8 asfrag(u32x4 v) { return __builtin_bit_cast(s16x8, v); }
DI s16x8 lds_frag(lptr p) { return *(const LAS s16x8*)p; }
DI void lds_put(lptr p, u32x4 v) { *(LAS u32x4*)p = v; }
DI s16x4 lds_tr(lptr p) { return __builtin_amdgcn_ds_read_tr16_b64_v4i16((LAS s16x4*)p); }
DI s16x8 tr_frag2(lptr p, lptr q) { s16x4 lo = lds_tr(p), hi = lds_tr(q); return __builtin_shufflevector(lo, hi, 0, 1, 2, 3, 4, 5, 6, 7); }
DI s16x8 tr_frag(lptr p, int second) { return tr_frag2(p, p + second); }
DI float swap_add(float v) { auto r = __builtin_amdgcn_permlane32_swap(__float_as_uint(v), __float_as_uint(v), false, false); return __uint_as_float(r[0]) + __uint_as_float(r[1]); }
DI float swap_max(float v) { auto r = __builtin_amdgcn_permlane32_swap(__float_as_uint(v), __float_as_uint(v), false, false); return fmaxf(__uint_as_float(r[0]), __uint_as_float(r[1])); }
DI float dot2(unsigned a, unsigned b, float c) { return __builtin_amdgcn_fdot2_f32_bf16(__builtin_bit_cast(bf16x2, a), __builtin_bit_cast(bf16x2, b), c, false); }
DI unsigned relu_pk(unsigned u) { return __builtin_bit_cast(unsigned, __builtin_elementwise_max(__builtin_bit_cast(s16x2, u), (s16x2){0, 0})); }
template <int CTRL> DI float dpp(float v) { return __uint_as_float(__builtin_amdgcn_update_dpp(0, __float_as_uint(v), CTRL, 0xf, 0xf, true)); }
DI float row32_max(float v) {
    v = fmaxf(v, dpp<0xB1>(v)); v = fmaxf(v, dpp<0x4E>(v)); v = fmaxf(v, dpp<0x141>(v)); v = fmaxf(v, dpp<0x140>(v));
    auto r = __builtin_amdgcn_permlane16_swap(__float_as_uint(v), __float_as_uint(v), false, false);
    return fmaxf(__uint_as_float(r[0]), __uint_as_float(r[1]));
}
template <int HALF> DI u32x4 pack8(const f32x16& a, float m) {
    u32x4 v;
#pragma unroll
    for (int e = 0; e < 4; ++e) v[e] = cvtpk(a[8 * HALF + 2 * e] * m, a[8 * HALF + 2 * e + 1] * m);
    return v;
}
template <int HALF> DI u32x4 pack8_relu(const f32x16& a) {
    u32x4 v;
#pragma unroll
    for (int e = 0; e < 4; ++e) v[e] = relu_pk(cvtpk(a[8 * HALF + 2 * e], a[8 * HALF + 2 * e + 1]));
    return v;
}
template <int HALF> DI f32x16 pv_mma(s16x8 v, const f32x16& p, f32x16 c, int idx) {
    const auto ph = __builtin_shufflevector(p, p, 8 * HALF, 8 * HALF + 1, 8 * HALF + 2, 8 * HALF + 3, 8 * HALF + 4, 8 * HALF + 5, 8 * HALF + 6, 8 * HALF + 7);
    return __builtin_amdgcn_smfmac_f32_32x32x32_bf16(__builtin_bit_cast(bf16x8, v), __builtin_bit_cast(bf16x16, ph), c, idx, 0, 0);
}
DI void glds16(const void* sbase, unsigned voff, unsigned lds_base) {
    asm volatile("s_mov_b32 m0, %2\n\ts_nop 0\n\tglobal_load_lds_dwordx4 %0, %1" :: "v"(voff), "s"(sbase), "s"(lds_base) : "memory");
}
DI void glds16x2(const void* sbase, unsigned voff0, unsigned voff1, unsigned lds_base) {
    asm volatile("s_mov_b32 m0, %3\n\ts_nop 0\n\tglobal_load_lds_dwordx4 %0, %2\n\tglobal_load_lds_dwordx4 %1, %2 offset:1024" :: "v"(voff0), "v"(voff1), "s"(sbase), "s"(lds_base) : "memory");
}
DI void glds16x4(const void* sbase, unsigned v0, unsigned v1, unsigned v2, unsigned v3, unsigned lds_base) {
    asm volatile("s_mov_b32 m0, %5\n\ts_nop 0\n\tglobal_load_lds_dwordx4 %0, %4\n\tglobal_load_lds_dwordx4 %1, %4 offset:1024\n\t"
                 "global_load_lds_dwordx4 %2, %4 offset:2048\n\tglobal_load_lds_dwordx4 %3, %4 offset:3072" :: "v"(v0), "v"(v1), "v"(v2), "v"(v3), "s"(sbase), "s"(lds_base) : "memory");
}
DI void glds16x2_sc1(const void* sbase, unsigned voff, unsigned lds_base) {
    asm volatile("s_mov_b32 m0, %2\n\ts_nop 0\n\tglobal_load_lds_dwordx4 %0, %1 sc1\n\tglobal_load_lds_dwordx4 %0, %1 offset:1024 sc1" :: "v"(voff), "s"(sbase), "s"(lds_base) : "memory");
}
DI void touch8k(const void* sbase, int lane, unsigned lds_scratch) {
    asm volatile("s_mov_b32 m0, %2\n\ts_nop 0\n\tglobal_load_lds_dword %0, %1" :: "v"(lane * 128), "s"(sbase), "s"(lds_scratch) : "memory");
}
#define WAIT_V(N) asm volatile("s_waitcnt vmcnt(" #N ")" ::: "memory")
DI __amdgpu_buffer_rsrc_t mk_rsrc(const void* p, unsigned bytes) { return __builtin_amdgcn_make_buffer_rsrc((void*)p, 0, bytes, 0x00020000); }
DI u32x4 bload(__amdgpu_buffer_rsrc_t r, unsigned voff, unsigned soff) { return __builtin_amdgcn_raw_buffer_load_b128(r, voff, soff, 0); }
DI u32x4 bload_sc1(__amdgpu_buffer_rsrc_t r, unsigned voff, unsigned soff) { return __builtin_amdgcn_raw_buffer_load_b128(r, voff, soff, 16); }
DI void bstore_sc1(u32x4 v, __amdgpu_buffer_rsrc_t r, unsigned voff) { __builtin_amdgcn_raw_buffer_store_b128(v, r, voff, 0, 16); }

#define STAMP(i) do { } while (0)

struct BigFrag { s16x8 x0, x1, w0, w1; };
struct SmallFrag { s16x8 x, q; };

__global__ void __launch_bounds__(512, 2)
fused_kernel(const uint16_t* __restrict__ x, const uint16_t* __restrict__ wq, const uint16_t* __restrict__ wk, const uint16_t* __restrict__ wv,
             const uint16_t* __restrict__ wo, const uint16_t* __restrict__ w1, const uint16_t* __restrict__ w2, const uint16_t* __restrict__ gw,
             uint16_t* out, char* ws) {
    __shared__ __attribute__((aligned(16))) char smem[LDS_BYTES];
    const lptr lds = (lptr)smem;
    const unsigned lds0 = (unsigned)(uintptr_t)smem;
    const int tid = threadIdx.x, lane = tid & 63, w = __builtin_amdgcn_readfirstlane(tid >> 6);
    const int r = lane & 31, h = lane >> 5, g1 = (lane >> 4) & 1, q4 = (lane & 15) >> 2, p4 = lane & 3;
    const int b = blockIdx.x, bi = b >> 3;
    const int R = 2 * (b & 7) + (bi >> 4), cb = bi & 15, rh64 = cb & 1, hg = cb >> 1;
    const bool kvcu = cb >= 14, big = w < 4;
    const int row64 = R * 128 + rh64 * 64;
    const int rt = w & 1, hq = w >> 1, fh = hq & 1;
    unsigned stv = 0;
    STAMP(0);

    asm volatile("" :: "s"(x), "s"(wq), "s"(wk), "s"(wv), "s"(wo), "s"(w1), "s"(w2), "s"(gw), "s"(out), "s"(ws));
    const unsigned w64_src = (unsigned)((lane >> 3) * 1024 + hg * 128 + (((lane & 7) ^ (g1 << 2)) << 4));
    const unsigned w64_src1 = w64_src + 8192 - 1024;
    const unsigned xv = (unsigned)((lane >> 3) * 1024 + (((lane & 7) ^ ((lane >> 4) & 3)) << 4)), xv1 = (xv ^ 64) + 8192 - 1024;
    const unsigned wv0 = (unsigned)((lane >> 4) * (DFF * 2) + cb * 256 + (((lane & 15) ^ (((lane >> 4) & 3) << 2)) << 4)), wv1 = wv0 + 4 * DFF * 2 - 1024;
    const int rot = (b + cb) & 7;
    if (!kvcu) {
#pragma unroll
        for (int s = 0; s < 2; ++s) {
            const int sr = (s + rot) & 7;
            glds16x2((const char*)x + R * 131072 + sr * 128 + w * 16384, xv, xv1, lds0 + s * L_STAGE + w * 2048);
            glds16x2((const char*)w1 + (size_t)(sr * 64 + w * 8) * (DFF * 2), wv0, wv1, lds0 + s * L_STAGE + 16384 + w * 2048);
            glds16((const char*)wq + sr * 65536 + w * 8192, w64_src, lds0 + s * L_STAGE + 32768 + w * 1024);
        }
    } else {
        const char* wkv = (const char*)((w & 2) ? wk : wv);
        const unsigned kvv = (w & 1) * 1024 + ((lane ^ ((w & 2) << 2)) << 4);
#pragma unroll 1
        for (int s = 0; s < 8; ++s) {
            glds16((const char*)x + (row64 + 8 * w) * 1024 + s * 128, xv ^ (rt << 6), lds0 + s * L_KSTAGE + w * 1024);
            if (!big) glds16(wkv + s * 2048, kvv, lds0 + s * L_KSTAGE + 8192 + (w & 3) * 1024);
        }
    }
    const unsigned eo = lane == 1 ? WS_HFLAG + (R * 16 + cb) * 128 : !kvcu || lane == 0 || lane > 6 ? WS_EPOCH : lane == 2 ? WS_KVFLAG + (row64 >> 6) * 128 : WS_GZ + row64 * 8 + (lane - 3) * 128;
    const unsigned epoch_v = __hip_atomic_load((gu32*)(ws + eo), RLX_AGENT);
    asm volatile("" ::: "memory");
    const __amdgpu_buffer_rsrc_t rs_kv = mk_rsrc(ws + WS_KV, KV_BYTES), rs_h = mk_rsrc(ws + WS_H, H_BYTES), rs_o = mk_rsrc(ws + WS_O, O_BYTES);
    const __amdgpu_buffer_rsrc_t rs_kf = mk_rsrc(ws + WS_KVFLAG, 4096);
    const unsigned tr_in = 32 * g1 + 8 * p4;
    const unsigned wq_rd = (8 * h + q4) * 128 + ((fh ^ (q4 >> 1)) << 6) + tr_in;
    unsigned xo[4];
#pragma unroll
    for (int ks = 0; ks < 4; ++ks) xo[ks] = r * 128 + (((2 * ks + h) ^ ((r >> 1) & 7)) << 4);

    lptr kp, vp0, vp1, wrA0, wrA1, wrB0, wrB1, q_rd, qn_rd, km_rd, cst_wr;
    unsigned h_voff, hf_voff, g_voff0, g_voff1, hb, hb1, o_own;
    int spidx;
    f32x16 oacc;
    u32x4 cst, omark;
    s16x8 zf;
    auto consts = [&]() {
        kp = lds + lane * 16;
        const unsigned v_cst = (r & 27) != 16 ? 2064 : 2048;
        vp0 = lds + (r < 16 ? 1024 + h * 256 + r * 16 : v_cst); vp1 = lds + (r < 16 ? 1536 + h * 256 + r * 16 : v_cst);
        const unsigned w_rd0 = w * 2048 + (4 * h + q4) * 128 + ((q4 >> 1) << 6) + tr_in;
        wrA0 = lds + L_WB + w_rd0; wrA1 = lds + L_WB + (w_rd0 ^ 64); wrB0 = lds + w_rd0; wrB1 = lds + (w_rd0 ^ 64);
        q_rd = lds + L_QX + (hq * 2 + rt) * 1024 + lane * 16; qn_rd = lds + L_QN + (hq * 2 + rt) * 128 + r * 4; km_rd = lds + L_KM;
        h_voff = (unsigned)((row64 >> 5) * 112 + 14 * w) * 1024u + lane * 16;
        hf_voff = (unsigned)(R * 16 + (lane & 15)) * 128 + 4; g_voff0 = 32768 + (row64 + 32 * rt + r) * 8; g_voff1 = 32768 + (row64 + 8 * w + ((lane >> 1) & 4) + (lane >> 4)) * 8 + 4;
        spidx = 0xDDDD; oacc = (f32x16){}; zf = (s16x8){};
        cst = (lane & 1) ? (u32x4){} : (u32x4){0x3f803f80u, 0x3f803f80u, 0x3f803f80u, 0x3f803f80u};
        cst_wr = lds + (tid >> 1) * KVT + 2048 + (tid & 1) * 16;
        hb = (kvcu ? OOB : (unsigned)((R * 4 + 2 * rt) * 112 + cb * 8 + fh * 4) * 1024u) + lane * 16; hb1 = hb + 112 * 1024;
        o_own = (unsigned)(((row64 >> 5) + rt) * 32 + hg * 4 + hq) * 1024u + lane * 16;
        omark = (u32x4){~0u, ~0u, ~0u, ~0u};
        PIN(oacc); PIN(spidx); PIN(cst); PIN(cst_wr); PIN(hb); PIN(hb1); PIN(zf); PIN(o_own); PIN(omark);
        PIN(kp); PIN(vp0); PIN(vp1); PIN(wrA0); PIN(wrA1); PIN(wrB0); PIN(wrB1); PIN(q_rd); PIN(qn_rd); PIN(km_rd);
        PIN(h_voff); PIN(hf_voff); PIN(g_voff0); PIN(g_voff1);
        SBAR();
    };
    u32x4 hs[8];
#pragma unroll
    for (int i = 0; i < 8; ++i) asm volatile("" : "=v"(hs[i]));
    f32x16 a00 = {}, a01 = {}, a10 = {}, a11 = {};
    auto kv_wait = [&](u32x4& fv) {
        const unsigned ep = __builtin_amdgcn_readfirstlane(epoch_v) + 1u;
        if (__builtin_expect(!__all(fv[1] == ep && fv[3] == ep), 0)) {
            unsigned spins = 0;
            do { __builtin_amdgcn_s_sleep(6); CFENCE(); fv = bload_sc1(rs_kf, r * 128 + h * 16, 0); } while (!__all(fv[1] == ep && fv[3] == ep) && ++spins < SPIN_CAP);
        }
        CFENCE();
    };
    auto kv_bound = [&](const u32x4& fv) {
        const float km = row32_max(fmaxf(__uint_as_float(fv[0]), __uint_as_float(fv[2])));
        if (tid == 256) *(LAS float*)(lds + L_KM) = __builtin_amdgcn_sqrtf(km) * 1.0001f;
    };
    auto kv_tile = [&](int t) { glds16x2_sc1(ws + WS_KV + ((t + 8 * bi) & 63) * 2048, lane * 16, lds0 + t * KVT); };
    if (!kvcu) {
        unsigned cur = 0, nxt = L_STAGE;
        if (big) {
            const unsigned xb = rt * 8192, w0o = 16384 + (8 * h + q4) * 256 + (((2 * fh) ^ q4) << 6) + tr_in, w1o = w0o ^ 64;
            BigFrag F[4];
            auto rd = [&](BigFrag& n, lptr sb, int ks) {
                n.x0 = lds_frag(sb + xb + xo[ks]); n.x1 = lds_frag(sb + xb + 4096 + xo[ks]); n.w0 = tr_frag(sb + w0o + ks * 4096, 1024); n.w1 = tr_frag(sb + w1o + ks * 4096, 1024); };
#define AM(e, ...) do { if (ABL & 2048) asm volatile("" :: __VA_ARGS__); else e; SBAR(); } while (0)
#define AR(e, ...) do { if (ABL & 4096) asm volatile("" : __VA_ARGS__); else { e; } SBAR(); } while (0)
            auto step = [&](const BigFrag& c, BigFrag& n, lptr sb, int ks) {
                AM(a00 = MFMA(c.w0, c.x0, a00), "v"(c.w0), "v"(c.x0)); AR(n.x0 = lds_frag(sb + xb + xo[ks]); n.x1 = lds_frag(sb + xb + 4096 + xo[ks]), "+v"(n.x0), "+v"(n.x1));
                AM(a10 = MFMA(c.w0, c.x1, a10), "v"(c.x1)); AR(n.w0 = tr_frag(sb + w0o + ks * 4096, 1024), "+v"(n.w0));
                AM(a01 = MFMA(c.w1, c.x0, a01), "v"(c.w1)); AR(n.w1 = tr_frag(sb + w1o + ks * 4096, 1024), "+v"(n.w1));
                AM(a11 = MFMA(c.w1, c.x1, a11), "v"(c.w1)); };
            auto stage = [&](auto bar) {
                step(F[0], F[3], lds + cur, 3);
                bar();
                step(F[1], F[0], lds + nxt, 0); step(F[2], F[1], lds + nxt, 1); step(F[3], F[2], lds + nxt, 2);
                cur = nxt; nxt = (nxt == 2 * L_STAGE) ? 0 : nxt + L_STAGE; };
            consts();
            STAMP(13);
            BAR_VL(6);
            STAMP(14);
            rd(F[0], lds, 0); rd(F[1], lds, 1); rd(F[2], lds, 2);
#pragma unroll 1
            for (int s = 0; s < 7; ++s) stage([&] {
                BAR_VL(1);
                if (s == 3 && w < 2) touch8k(w == 0 ? (const char*)w2 + (R * 14 + cb) * 8192 : (const char*)wo + (b & 63) * 8192, lane, lds0 + L_TOUCH); });
            STAMP(1);
            rd(F[3], lds + cur, 3);
#define LAST4(acc, wm, xm) do { _Pragma("unroll") for (int ks = 0; ks < 4; ++ks) acc = MFMA(F[ks].wm, F[ks].xm, acc); } while (0)
#define LAST4P(acc, wm, xm, prev, i) do { acc = MFMA(F[0].wm, F[0].xm, acc); acc = MFMA(F[1].wm, F[1].xm, acc); SBAR(); hs[i] = pack8_relu<0>(prev); PIN(hs[i]); SBAR(); \
            acc = MFMA(F[2].wm, F[2].xm, acc); SBAR(); hs[(i) + 1] = pack8_relu<1>(prev); PIN(hs[(i) + 1]); SBAR(); acc = MFMA(F[3].wm, F[3].xm, acc); SBAR(); } while (0)
            LAST4(a00, w0, x0); SBAR();
            LAST4P(a01, w1, x0, a00, 0); LAST4P(a10, w0, x1, a01, 2); LAST4P(a11, w1, x1, a10, 4);
            hs[6] = pack8_relu<0>(a11); hs[7] = pack8_relu<1>(a11);
        } else {
            const int j = w - 4;
            __builtin_amdgcn_s_setprio(2);
            const unsigned xb = (2 * rh64 + rt) * 4096, qo = 32768 + wq_rd;
            SmallFrag F[4];
            auto rd = [&](SmallFrag& n, lptr sb, int ks) { n.x = lds_frag(sb + xb + xo[ks]); n.q = tr_frag(sb + qo + ks * 2048, 512); SBAR(); };
            auto mma = [&](const SmallFrag& c) { a00 = MFMA(c.q, c.x, a00); SBAR(); };
            const unsigned xv2 = xv + 2 * (8192 - 1024), xv3 = xv1 + 2 * (8192 - 1024), wv2 = wv0 + 2 * (4 * DFF * 2 - 1024), wv3 = wv1 + 2 * (4 * DFF * 2 - 1024);
            const char* xs = (const char*)x + R * 131072 + j * 32768;
            const char* w1s = (const char*)w1 + (size_t)(j * 16) * (DFF * 2);
            const char* wqs = (const char*)wq + j * 16384;
            auto fill0 = [&](int sr, unsigned slot) { glds16x4(xs + sr * 128, xv, xv1, xv2, xv3, lds0 + slot + j * 4096); };
            auto fill1 = [&](int sr, unsigned slot) { glds16x4(w1s + sr * (64 * DFF * 2), wv0, wv1, wv2, wv3, lds0 + slot + 16384 + j * 4096); };
            auto fill2 = [&](int sr, unsigned slot) { glds16x2(wqs + sr * 65536, w64_src, w64_src1, lds0 + slot + 32768 + j * 2048); };
            fill0((rot + 2) & 7, 2 * L_STAGE); fill1((rot + 2) & 7, 2 * L_STAGE); fill2((rot + 2) & 7, 2 * L_STAGE);
            consts();
            BAR_VL(16);
            rd(F[0], lds, 0); rd(F[1], lds, 1); rd(F[2], lds, 2);
#pragma unroll 1
            for (int s = 0; s < 6; ++s) {
                const bool more = s < 5;
                const int sr = (rot + s + 3) & 7;
                rd(F[3], lds + cur, 3); mma(F[0]);
                BAR_VL(10);
                if (more) fill0(sr, cur);
                rd(F[0], lds + nxt, 0); mma(F[1]);
                if (more) fill1(sr, cur);
                rd(F[1], lds + nxt, 1); mma(F[2]);
                if (more) fill2(sr, cur);
                rd(F[2], lds + nxt, 2); mma(F[3]);
                cur = nxt; nxt = (nxt == 2 * L_STAGE) ? 0 : nxt + L_STAGE;
            }
            u32x4 fv = bload_sc1(rs_kf, r * 128 + h * 16, 0);
            rd(F[3], lds, 3); mma(F[0]);
            BAR_VL(0);
            rd(F[0], lds + L_STAGE, 0); mma(F[1]);
            STAMP(15);
            kv_wait(fv);
            STAMP(16);
            kv_tile(j); kv_tile(4 + j);
            rd(F[1], lds + L_STAGE, 1); mma(F[2]);
            kv_tile(8 + j); kv_tile(12 + j);
            rd(F[2], lds + L_STAGE, 2); mma(F[3]); rd(F[3], lds + L_STAGE, 3);
            mma(F[0]); kv_bound(fv); mma(F[1]); mma(F[2]); mma(F[3]);
        }
    } else {
        const unsigned xb = rt * 4096, w64_src2 = w64_src + 2 * (8192 - 1024), w64_src3 = w64_src + 3 * (8192 - 1024);
        {
            const unsigned wkv_rd = 8192 + g1 * 2048 + (((8 * h + q4) * 32 + 8 * p4) ^ (g1 << 7));
#pragma unroll
            for (int g = 0; g < 2; ++g) {
                if (g == 0) { if (big) BAR_VL(5); else BAR_VL(9); } else BAR_VL(1);
                const lptr st = lds + (g * 4 + hq) * L_KSTAGE;
#pragma unroll
                for (int k = 0; k < 4; ++k) a00 = MFMA(tr_frag2(st + wkv_rd + k * 512, st + (wkv_rd ^ 128) + k * 512), lds_frag(st + xb + xo[k]), a00);
            }
        }
        BAR_L();
        {
            const lptr pw = lds + w * L_KSTAGE + 8192 + r * 128;
            const unsigned sz = (r & 7) ^ ((r >> 1) & 4);
#pragma unroll
            for (int i = 0; i < 4; ++i) *(LAS f32x4*)(pw + (((2 * i + h) ^ sz) << 4)) = (f32x4){a00[4 * i], a00[4 * i + 1], a00[4 * i + 2], a00[4 * i + 3]};
            a00 = (f32x16){};
        }
        BAR_L();
        if (big) {
            STAMP(1);
            const unsigned kvo = (unsigned)((row64 >> 5) + rt) * 2048u + lane * 16;
            const lptr pr = lds + rt * L_KSTAGE + 8192;
            float n2 = 0.f;
            if (fh == 0) {
                const unsigned sz = (r & 7) ^ ((r >> 1) & 4);
                f32x4 lo = {}, hi = {};
#pragma unroll
                for (int i = 0; i < 4; ++i) { lo += *(const LAS f32x4*)(pr + i * 2 * L_KSTAGE + r * 128 + (((4 + h) ^ sz) << 4)); hi += *(const LAS f32x4*)(pr + i * 2 * L_KSTAGE + r * 128 + (((6 + h) ^ sz) << 4)); }
                const u32x4 v = {cvtpk(lo[0], lo[1]), cvtpk(lo[2], lo[3]), cvtpk(hi[0], hi[1]), cvtpk(hi[2], hi[3])};
                bstore_sc1(v, rs_kv, kvo);
                n2 = row32_max(swap_add(dot2(v[0], v[0], dot2(v[1], v[1], 0.f)) + dot2(v[2], v[2], dot2(v[3], v[3], 0.f))));
            } else {
                float t[8];
#pragma unroll
                for (int j = 0; j < 8; ++j) {
                    const lptr q = pr + ((lane >> 4) * 8 + j) * 128 + ((q4 ^ j ^ (g1 << 2)) << 4) + p4 * 4;
                    t[j] = (*(const LAS float*)q + *(const LAS float*)(q + 2 * L_KSTAGE)) + (*(const LAS float*)(q + 4 * L_KSTAGE) + *(const LAS float*)(q + 6 * L_KSTAGE));
                }
                bstore_sc1((u32x4){cvtpk(t[0], t[1]), cvtpk(t[2], t[3]), cvtpk(t[4], t[5]), cvtpk(t[6], t[7])}, rs_kv, kvo + 1024);
            }
            STAMP(13);
            WAIT_V(0);
            STAMP(14);
            if (lane == 0) __hip_atomic_store((gu64*)(ws + WS_KVFLAG) + (row64 >> 6) * 16 + w, ((unsigned long long)(epoch_v + 1u) << 32) | __float_as_uint(n2), RLX_AGENT);
            u32x4 gf[16];
#pragma unroll
            for (int i = 0; i < 16; ++i) gf[i] = *(const u32x4*)(gw + i * 32 + (lane >> 4) * 8);
            consts();
            BAR_L();
            f32x4 z = {};
            const int zr = 16 * w + (lane & 15);
#pragma unroll
            for (int i = 0; i < 16; ++i)
                z = __builtin_amdgcn_mfma_f32_16x16x32_bf16(asfrag(gf[i]), lds_frag(lds + (i >> 1) * L_KSTAGE + zr * 128 + (((4 * (i & 1) + (lane >> 4)) ^ ((zr >> 1) & 7)) << 4)), z, 0, 0, 0);
            const float gt = rbf(__builtin_amdgcn_rcpf(1.f + __expf(-rbf(z[0])))), og = fmaxf(rbf(1.f - gt), 0x1p-40f);
            if (lane < 16) __hip_atomic_store((gu64*)(ws + WS_GZ) + row64 + zr, ((unsigned long long)__float_as_uint(og) << 32) | __float_as_uint(gt / og), RLX_AGENT);
            BAR_L();
        } else {
#pragma unroll
            for (int i = 0; i < 4; ++i) { const int p = i * 16 + (w - 4) * 4; glds16x4((const char*)wq + p * 8192, w64_src, w64_src1, w64_src2, w64_src3, lds0 + L_KWQ + p * 1024); }
            consts();
            BAR_VL(0);
            SmallFrag F[4];
            auto rd = [&](SmallFrag& n, int s, int k) { n.x = lds_frag(lds + s * L_KSTAGE + xb + xo[k]); n.q = tr_frag(lds + L_KWQ + s * 8192 + wq_rd + k * 2048, 512); SBAR(); };
#pragma unroll
            for (int k = 0; k < 4; ++k) rd(F[k], 0, k);
#pragma unroll 1
            for (int s = 1; s < 8; ++s) {
#pragma unroll
                for (int k = 0; k < 4; ++k) { a00 = MFMA(F[k].q, F[k].x, a00); SBAR(); rd(F[k], s, k); }
            }
            u32x4 fv = bload_sc1(rs_kf, r * 128 + h * 16, 0);
#pragma unroll
            for (int k = 0; k < 4; ++k) a00 = MFMA(F[k].q, F[k].x, a00);
            BAR_L();
            STAMP(15);
            kv_wait(fv);
            STAMP(16);
            kv_tile(w - 4); kv_tile(w); kv_tile(4 + w); kv_tile(8 + w);
            kv_bound(fv);
        }
    }
    STAMP(2);
    if (big) {
        if (tid < 32) lds_put(cst_wr, cst);
    } else {
#pragma unroll
        for (int s = 0; s < 2; ++s) {
            const u32x4 v = s ? pack8<1>(a00, QSCALE) : pack8<0>(a00, QSCALE);
            lds_put(lds + L_QX + (4 * fh + 2 * s + rt) * 1024 + lane * 16, v);
            const float n2 = swap_add(dot2(v[0], v[0], dot2(v[1], v[1], 0.f)) + dot2(v[2], v[2], dot2(v[3], v[3], 0.f)));
            *(LAS float*)(lds + L_QN + (4 * fh + 2 * s + rt) * 128 + r * 4) = __builtin_amdgcn_sqrtf(n2);
        }
    }
    __builtin_amdgcn_s_setprio(3);
    BAR_VL(0);
    STAMP(3);
    const s16x8 qf = lds_frag(q_rd);
    s16x8 kf = lds_frag(kp), vf0, vf1;
    float mshift = *(const LAS float*)qn_rd * *(const LAS float*)km_rd;
    vf0 = lds_frag(vp0);
    if (__builtin_expect(!__all(mshift <= 0.5f * SAFE_RANGE), 0)) {
        float m = -__builtin_inff();
#pragma unroll 1
        for (int t = 0; t < 64; ++t) {
            const f32x16 s = MFMA(asfrag(bload_sc1(rs_kv, lane * 16, t * 2048)), qf, (f32x16){});
#pragma unroll
            for (int i = 0; i < 16; ++i) m = fmaxf(m, s[i]);
        }
        mshift = swap_max(m);
    }
    f32x16 cinit;
#pragma unroll
    for (int i = 0; i < 16; ++i) cinit[i] = -mshift;
    PIN(cinit);
    f32x16 sA = MFMA(kf, qf, cinit), sB;
    kf = lds_frag(kp + KVT);
    if (tid >= 32 && tid < 128) lds_put(cst_wr, cst);

#define S1(S_, i) S_[i] = __builtin_amdgcn_exp2f(S_[i])
#define EX2(S_, i) do { S1(S_, i); S1(S_, (i) + 1); } while (0)
#define EX3(S_, i) do { S1(S_, i); S1(S_, (i) + 1); S1(S_, (i) + 2); } while (0)
#define EX4(S_, i) do { EX2(S_, i); EX2(S_, (i) + 2); } while (0)
#define PV1(SN, T) do { if ((T) >= 0) oacc = pv_mma<1>(vf1, SN, oacc, spidx); vf1 = lds_frag(vp1 + ((T) < 0 ? 0 : (T)) * KVT); SBAR(); } while (0)
#define QK(SN, T) do { SN = MFMA(kf, qf, cinit); kf = lds_frag(kp + ((T) + 2) * KVT); SBAR(); } while (0)
#define PV0(SC, T) do { oacc = pv_mma<0>(vf0, SC, oacc, spidx); vf0 = lds_frag(vp0 + ((T) + 1) * KVT); SBAR(); } while (0)
#define CUT(S_) do { PIN(S_); SBAR(); } while (0)
#define ITER0(SC, SN, T, T1, PR, MEM) do { PR; \
    EX2(SC, 0); CUT(SC); PV1(SN, T1); EX4(SC, 2); CUT(SC); QK(SN, T); EX4(SC, 6); S1(SC, 10); CUT(SC); PV0(SC, T); MEM; S1(SC, 11); EX4(SC, 12); CUT(SC); } while (0)
#define ITER1(SC, SN, T, PR, PRE, MEM, G1) do { PR; PRE; \
    EX2(SC, 0); CUT(SC); PV1(SN, T); EX4(SC, 2); CUT(SC); QK(SN, T); EX4(SC, 6); CUT(SC); PV0(SC, T); MEM; EX4(SC, 10); CUT(SC); G1; SBAR(); EX2(SC, 14); CUT(SC); } while (0)
#define ITERG(SC, SN, T, PR, MEM, G0, G1) do { PR; \
    EX2(SC, 0); CUT(SC); PV1(SN, T); EX3(SC, 2); CUT(SC); QK(SN, T); EX3(SC, 5); CUT(SC); G0; SBAR(); EX3(SC, 8); CUT(SC); PV0(SC, T); MEM; EX3(SC, 11); CUT(SC); G1; SBAR(); EX2(SC, 14); CUT(SC); } while (0)
#define NOP (void)0
#define PRIO(p) __builtin_amdgcn_s_setprio(p)
#define ITER2(T, P0, P1, M0, M1) do { ITER0(sA, sB, T, T, PRIO(P0), M0); ITER0(sB, sA, (T) + 1, (T) + 1, PRIO(P1), M1); } while (0)
#define ADV(n) do { kp += (n) * KVT; vp0 += (n) * KVT; vp1 += (n) * KVT; PIN(kp); PIN(vp0); PIN(vp1); } while (0)
    const char* const w2w = (const char*)w2 + w * (14 * 16384);
    const char* const wow = (const char*)wo + w * 65536;
    int pn = (row64 >> 6) % 7;
    const unsigned wbA = lds0 + L_WB + w * 2048, wbB = lds0 + w * 2048;
    const __amdgpu_buffer_rsrc_t rs_f = mk_rsrc(ws + WS_HFLAG, 32768 + 16384);
    u32x4 hA0, hA1, hB0, hB1;
    s16x8 wf0, wf1;
#define W_LD(src, buf) glds16x2(src, w64_src, w64_src1, buf)
    STAMP(4);
#define H_ST(i) do { if (big) bstore_sc1(hs[i], rs_h, ((i) < 4 ? hb : hb1) + ((i) & 3) * 1024); } while (0)
    ITER0(sA, sB, 0, -1, PRIO(3), H_ST(0)); ITER0(sB, sA, 1, 1, NOP, H_ST(1); kv_tile(16 + w));
    ITER2(2, 3, 3, H_ST(2); kv_tile(24 + w), H_ST(3); kv_tile(32 + w));
    ITER2(4, 2, 2, H_ST(4); kv_tile(40 + w), H_ST(5); kv_tile(48 + w));
    ITER2(6, 2, 2, H_ST(6); kv_tile(56 + w), H_ST(7); W_LD(w2w + pn * 32768, wbA));
    STAMP(17);
    ITER2(8, 1, 1, bstore_sc1(omark, rs_o, o_own), NOP);
    ITER2(10, 1, 1, NOP, NOP);
    ITER2(12, 0, 0, NOP, NOP);
    STAMP(18);
    STAMP(19);
    BAR_VL(0);
    unsigned epoch_w = epoch_v; PIN(epoch_w);
    const unsigned epoch = __builtin_amdgcn_readfirstlane(epoch_w) + 1u;
    if (tid == 0) __hip_atomic_store((gu32*)(ws + WS_HFLAG) + (R * 16 + cb) * 32 + 1, epoch, RLX_AGENT);
    STAMP(5);
    unsigned hfv;
    ITER2(14, 1, 3, W_LD(w2w + pn * 32768 + 16384, wbB); a00 = MFMA(zf, zf, (f32x16){}); PIN(zf); a01 = MFMA(zf, zf, (f32x16){}), PIN(zf); a10 = MFMA(zf, zf, (f32x16){}); PIN(zf); a11 = MFMA(zf, zf, (f32x16){}));
    ITER2(16, 2, 1, NOP, hfv = __builtin_amdgcn_raw_buffer_load_b32(rs_f, hf_voff, 0, 16));
    ITER0(sA, sB, 18, 18, PRIO(3), NOP);
    STAMP(6);
    if (__builtin_expect(!__all(hfv == epoch), 0)) {
        unsigned spins = 0;
        do { __builtin_amdgcn_s_sleep(6); CFENCE(); hfv = __builtin_amdgcn_raw_buffer_load_b32(rs_f, hf_voff, 0, 16); } while (!__all(hfv == epoch) && ++spins < SPIN_CAP);
    }
    CFENCE();
    STAMP(7);
    unsigned gr_u, og_u;
    ITER0(sB, sA, 19, 19, PRIO(2), gr_u = __builtin_amdgcn_raw_buffer_load_b32(rs_f, g_voff0, 0, 16); og_u = __builtin_amdgcn_raw_buffer_load_b32(rs_f, g_voff1, 0, 16);
          hA0 = bload_sc1(rs_h, h_voff, pn * 2048); hA1 = bload_sc1(rs_h, h_voff, pn * 2048 + 112 * 1024));
    ITER2(20, 1, 3, hB0 = bload_sc1(rs_h, h_voff, pn * 2048 + 1024), hB1 = bload_sc1(rs_h, h_voff, pn * 2048 + 113 * 1024));
    ADV(22);
    PIN(a00); PIN(a01); PIN(a10); PIN(a11);
    STAMP(8);
#define GM(acc, wf, H) acc = MFMA(asfrag(H), wf, acc); SBAR()
#define GSTEP(SX, SY, T, R0, R1, H0, H1, LW, LH0, LH1) do { \
    ITER1(SX, SY, T, PRIO(3), WAIT_V(5); wf0 = tr_frag(R0, 1024); wf1 = tr_frag(R1, 1024), NOP, GM(a00, wf0, H0)); \
    ITERG(SY, SX, (T) + 1, PRIO(2), LW, GM(a01, wf1, H0); LH0, GM(a10, wf0, H1)); \
    ITER1(SX, SY, (T) + 2, PRIO(1), NOP, NOP, GM(a11, wf1, H1); LH1); } while (0)
#pragma unroll 1
    for (int n = 0; n < 4; ++n) {
        pn = pn == 6 ? 0 : pn + 1;
        const char* wsrc = w2w + pn * 32768;
        GSTEP(sA, sB, 0, wrA0, wrA1, hA0, hA1, W_LD(wsrc, wbA), hA0 = bload_sc1(rs_h, h_voff, pn * 2048), hA1 = bload_sc1(rs_h, h_voff, pn * 2048 + 112 * 1024));
        GSTEP(sB, sA, 3, wrB0, wrB1, hB0, hB1, W_LD(wsrc + 16384, wbB), hB0 = bload_sc1(rs_h, h_voff, pn * 2048 + 1024), hB1 = bload_sc1(rs_h, h_voff, pn * 2048 + 113 * 1024));
        ADV(6);
    }
    BAR_L();
    pn = pn == 6 ? 0 : pn + 1;
    const int pm = pn == 6 ? 0 : pn + 1;
    u32x4 hC0, hC1, hD0, hD1, hE0, hE1, hF0, hF1;
    ITER2(0, 3, 2, W_LD(w2w + pn * 32768, wbB + 16384); hC0 = bload_sc1(rs_h, h_voff, pn * 2048), hC1 = bload_sc1(rs_h, h_voff, pn * 2048 + 112 * 1024));
    ITER2(2, 1, 3, W_LD(w2w + pn * 32768 + 16384, wbB + 32768); hD0 = bload_sc1(rs_h, h_voff, pn * 2048 + 1024), hD1 = bload_sc1(rs_h, h_voff, pn * 2048 + 113 * 1024));
    ITER2(4, 2, 1, W_LD(w2w + pm * 32768, wbB + 49152); hE0 = bload_sc1(rs_h, h_voff, pm * 2048), hE1 = bload_sc1(rs_h, h_voff, pm * 2048 + 112 * 1024));
    ITER2(6, 3, 2, W_LD(w2w + pm * 32768 + 16384, wbB + 65536); hF0 = bload_sc1(rs_h, h_voff, pm * 2048 + 1024), hF1 = bload_sc1(rs_h, h_voff, pm * 2048 + 113 * 1024));
    const bool closer = b == 112 && w == 7;
    unsigned ef[4];
    auto ef_load = [&]() {
#pragma unroll
        for (int i = 0; i < 4; ++i) ef[i] = __builtin_amdgcn_raw_buffer_load_b32(mk_rsrc(ws + WS_HFLAG + i * 8192 + 4, closer ? 8188 : 0), lane * 128, 0, 16);
    };
    const unsigned xtra = w < 3 ? 5 * 16384 : L_QX - 3 * 2048;
    s16x8 wf2, wf3, wf4, wf5;
    ITER2(8, 1, 3, WAIT_V(14); wf0 = tr_frag(wrA0, 1024); wf1 = tr_frag(wrA1, 1024); W_LD(wow + 49152, wbB + xtra), wf2 = tr_frag(wrB0, 1024); wf3 = tr_frag(wrB1, 1024); ef_load());
    ITER2(10, 2, 1, wf4 = tr_frag(wrB0 + 16384, 1024); wf5 = tr_frag(wrB1 + 16384, 1024); PIN(wf0); PIN(wf1); W_LD(wow, wbA), PIN(wf2); PIN(wf3); W_LD(wow + 16384, wbB));
    ITER2(12, 3, 3, PIN(wf4); PIN(wf5); W_LD(wow + 32768, wbB + 16384), NOP);
    ITER2(14, 2, 2, NOP, NOP);
    ITER2(16, 1, 0, NOP, NOP);
    STAMP(9);
#define GMA(acc, wf, H) asm volatile("v_mfma_f32_32x32x16_bf16 %0, %1, %2, %0" : "+v"(acc) : "v"(H), "v"(wf))
    PRIO(3);
    oacc = pv_mma<1>(vf1, sB, oacc, spidx);
    {
        u32x4 v = pack8<0>(oacc, __builtin_amdgcn_rcpf(oacc[8]) * __uint_as_float(gr_u));
#pragma unroll
        for (int e = 0; e < 4; ++e) v[e] = v[e] < 0xfffffffeu ? v[e] : 0xfffffffeu;
        bstore_sc1(v, rs_o, o_own);
        asm volatile("s_nop 1");
    }
    PRIO(0);
    GMA(a00, wf0, hA0); GMA(a01, wf1, hA0);
    WAIT_V(1);
    lptr wrE0 = wrB0 + 32768, wrE1 = wrB1 + 32768; PIN(wrE0); PIN(wrE1);
    const s16x8 wf6 = tr_frag(wrE0, 1024), wf7 = tr_frag(wrE1, 1024), wf8 = tr_frag(wrE0 + 16384, 1024), wf9 = tr_frag(wrE1 + 16384, 1024);
    SBAR();
    GMA(a10, wf0, hA1); GMA(a11, wf1, hA1);
    const s16x8 wfa = tr_frag(wrE0 + 32768, 1024), wfb = tr_frag(wrE1 + 32768, 1024);
    s16x8 wof[8];
    wof[0] = tr_frag(wrA0, 1024); wof[1] = tr_frag(wrA1, 1024);
    SBAR();
    GMA(a00, wf2, hB0); GMA(a01, wf3, hB0);
    wof[2] = tr_frag(wrB0, 1024); wof[3] = tr_frag(wrB1, 1024); wof[4] = tr_frag(wrB0 + 16384, 1024); wof[5] = tr_frag(wrB1 + 16384, 1024);
    SBAR();
    GMA(a10, wf2, hB1); GMA(a11, wf3, hB1);
    wof[6] = tr_frag(wrB0 + xtra, 1024); wof[7] = tr_frag(wrB1 + xtra, 1024);
    SBAR();
    GMA(a00, wf4, hC0); GMA(a01, wf5, hC0); GMA(a10, wf4, hC1); GMA(a11, wf5, hC1);
    const unsigned ob = (unsigned)((row64 >> 5) * 32 + 4 * w) * 1024u;
    u32x4 of[8];
    auto o_load = [&]() {
#pragma unroll
        for (int i = 0; i < 8; ++i) of[i] = bload_sc1(rs_o, lane * 16 + (i & 3) * 1024, ob + (i >> 2) * 32768);
    };
    auto o_ready = [&]() {
        unsigned m = 0;
#pragma unroll
        for (int i = 0; i < 8; ++i) { m = max(m, max(of[i][0], of[i][1])); m = max(m, max(of[i][2], of[i][3])); }
        return __all(m != ~0u);
    };
    BAR_VL(0);
#pragma unroll
    for (int i = 0; i < 8; ++i) PIN(wof[i]);
    STAMP(10);
    o_load();
    SBAR();
    GMA(a00, wf6, hD0); GMA(a01, wf7, hD0); GMA(a10, wf6, hD1); GMA(a11, wf7, hD1);
    GMA(a00, wf8, hE0); GMA(a01, wf9, hE0); GMA(a10, wf8, hE1); GMA(a11, wf9, hE1);
    GMA(a00, wfa, hF0); GMA(a01, wfb, hF0); GMA(a10, wfa, hF1); GMA(a11, wfb, hF1);
    SBAR();
    const lptr prb = lds + (((w >> 2) * 2 + ((lane >> 2) & 1)) * 16 + 4 * (w & 3) + (lane >> 4)) * 2048 + ((lane >> 3) & 1) * 128 + p4 * 32;
    lptr pr0 = prb + g1 * 16, pr1 = prb + (g1 ^ 1) * 16;
    PIN(pr0); PIN(pr1);
    uint16_t* outp = out + (row64 + 8 * w + ((lane >> 1) & 4) + (lane >> 4)) * 512 + hg * 64 + (lane & 7) * 8;
    PIN(outp);
    SBAR();
    if (!o_ready()) {
        unsigned spins = 0;
        do { CFENCE(); o_load(); } while (!o_ready() && ++spins < SPIN_CAP);
    }
    STAMP(11);
#define AMM(acc, wi, oi) GMA(acc, wof[wi], of[oi])
#define PUT(a, i, off) asm volatile("ds_write_addtid_b32 %0 offset:%4\n\tds_write_addtid_b32 %1 offset:%5\n\tds_write_addtid_b32 %2 offset:%6\n\tds_write_addtid_b32 %3 offset:%7" \
    :: "v"(a[4 * (i)]), "v"(a[4 * (i) + 1]), "v"(a[4 * (i) + 2]), "v"(a[4 * (i) + 3]), "n"((off) + (i) * 8192), "n"((off) + (i) * 8192 + 2048), "n"((off) + (i) * 8192 + 4096), "n"((off) + (i) * 8192 + 6144) : "memory")
#define SETM0(v, prev) asm volatile("s_mov_b32 m0, %1\n\ts_nop 10" : "+v"(prev) : "s"(v))
#define MM_PUT(acc, c, o, prev, m0v, off) do { AMM(acc, c, o); SETM0(m0v, prev); PUT(prev, 0, off); AMM(acc, 2 + (c), (o) + 1); PUT(prev, 1, off); \
    AMM(acc, 4 + (c), (o) + 2); PUT(prev, 2, off); AMM(acc, 6 + (c), (o) + 3); PUT(prev, 3, off); } while (0)
    const unsigned m0a = lds0 + w * 256, m0b = m0a + 63616;
    asm volatile("s_nop 1");
    AMM(a00, 0, 0); AMM(a00, 2, 1); AMM(a00, 4, 2); AMM(a00, 6, 3);
    MM_PUT(a01, 1, 0, a00, m0a, 0);
    MM_PUT(a10, 0, 4, a01, m0a, 32768);
    MM_PUT(a11, 1, 4, a10, m0b, 65536 - 63616);
    asm volatile("s_nop 7" : "+v"(a11));
    PUT(a11, 0, 98304 - 63616); PUT(a11, 1, 98304 - 63616); PUT(a11, 2, 98304 - 63616); PUT(a11, 3, 98304 - 63616);
    BAR_L();
    {
        f32x4 t[16];
#pragma unroll
        for (int k = 0; k < 8; ++k) { t[2 * k] = *(const LAS f32x4*)(pr0 + k * 256); t[2 * k + 1] = *(const LAS f32x4*)(pr1 + k * 256); }
        SBAR();
        f32x4 lo = t[0], hi = t[1];
#pragma unroll
        for (int k = 1; k < 8; ++k) { lo += t[2 * k]; hi += t[2 * k + 1]; }
        lo *= __uint_as_float(og_u); hi *= __uint_as_float(og_u);
        const unsigned l0 = cvtpk(lo[0], lo[1]), l1 = cvtpk(lo[2], lo[3]), h0 = cvtpk(hi[0], hi[1]), h1 = cvtpk(hi[2], hi[3]);
        const u32x4 v = g1 ? (u32x4){h0, h1, l0, l1} : (u32x4){l0, l1, h0, h1};
        __builtin_nontemporal_store(v, (GAS u32x4*)outp);
    }
    if (closer) {
        unsigned spins = 0;
        while (!__all(ef[0] == epoch && ef[1] == epoch && ef[2] == epoch && ef[3] == epoch) && ++spins < SPIN_CAP) { CFENCE(); ef_load(); }
        if (lane == 0) __hip_atomic_store((gu32*)(ws + WS_EPOCH), epoch, RLX_AGENT);
    }
    STAMP(12);
}

extern "C" void kernel_launch(void* const* d_in, const int* in_sizes, int n_in, void* d_out, int out_size, void* d_ws, size_t ws_size,
                              hipStream_t stream) {
    fused_kernel<<<256, 512, 0, stream>>>((const uint16_t*)d_in[0], (const uint16_t*)d_in[1], (const uint16_t*)d_in[2], (const uint16_t*)d_in[3],
                                          (const uint16_t*)d_in[4], (const uint16_t*)d_in[6], (const uint16_t*)d_in[7], (const uint16_t*)d_in[8],
                                          (uint16_t*)d_out, (char*)d_ws);
}
```

```cpp
#include <hip/hip_runtime.h>
#include <stdint.h>

typedef short s16x8 __attribute__((ext_vector_type(8)));
typedef short s16x4 __attribute__((ext_vector_type(4)));
typedef short s16x2 __attribute__((ext_vector_type(2)));
typedef float f32x16 __attribute__((ext_vector_type(16)));
typedef float f32x4 __attribute__((ext_vector_type(4)));
typedef float f32x2 __attribute__((ext_vector_type(2)));
typedef unsigned u32x4 __attribute__((ext_vector_type(4)));
typedef __bf16 bf16x2 __attribute__((ext_vector_type(2)));
typedef __bf16 bf16x8 __attribute__((ext_vector_type(8)));
typedef __bf16 bf16x16 __attribute__((ext_vector_type(16)));
#define LAS __attribute__((address_space(3)))
#define GAS __attribute__((address_space(1)))
typedef GAS unsigned gu32;
typedef GAS unsigned long long gu64;
typedef LAS char* lptr;
#define DI __device__ __forceinline__
#define RLX_AGENT __ATOMIC_RELAXED, __HIP_MEMORY_SCOPE_AGENT
#define MFMA(a, b, c) __builtin_amdgcn_mfma_f32_32x32x16_bf16((a), (b), (c), 0, 0, 0)
#define SBAR() __builtin_amdgcn_sched_barrier(0)
#define PIN(x) asm volatile("" : "+v"(x))
#define CFENCE() do { asm volatile("" ::: "memory"); __builtin_amdgcn_fence(__ATOMIC_ACQUIRE, "wavefront"); } while (0)
#define BAR_L() asm volatile("s_waitcnt lgkmcnt(0)\n\ts_barrier" ::: "memory")
#define BAR_VL(N) asm volatile("s_waitcnt vmcnt(" #N ") lgkmcnt(0)\n\ts_barrier" ::: "memory")

constexpr int D = 512, DFF = 1792;
constexpr float QSCALE = 0.25f * 1.4426950408889634f;
constexpr float SAFE_RANGE = 64.f;
constexpr unsigned WS_EPOCH = 0, WS_KVFLAG = 4096, WS_HFLAG = 8192;
constexpr unsigned WS_GZ = WS_HFLAG + 32768;
constexpr unsigned WS_KV = WS_GZ + 16384, KV_BYTES = 64 * 2048;
constexpr unsigned WS_H = WS_KV + KV_BYTES, H_BYTES = 64 * 112 * 1024;
constexpr unsigned WS_O = WS_H + H_BYTES, O_BYTES = 64 * 32 * 1024;
constexpr unsigned OOB = 0x7ff00000u;
constexpr unsigned L_STAGE = 40960;
constexpr unsigned L_KSTAGE = 12288, L_KWQ = 98304;
constexpr unsigned KVT = 2080;
constexpr unsigned L_WB = 64 * KVT;
constexpr unsigned L_QX = L_WB + 16384, L_QN = L_QX + 8192, L_KM = L_QN + 1024, L_TOUCH = L_KM + 256, LDS_BYTES = 163840;
static_assert(L_TOUCH + 256 <= LDS_BYTES, "lds map");
constexpr unsigned SPIN_CAP = 1u << 16;

DI unsigned cvtpk(float a, float b) { f32x2 v = {a, b}; return __builtin_bit_cast(unsigned, __builtin_convertvector(v, bf16x2)); }
DI float rbf(float x) { return __uint_as_float(cvtpk(x, x) << 16); }
DI s16x8 asfrag(u32x4 v) { return __builtin_bit_cast(s16x8, v); }
DI s16x8 lds_frag(lptr p) { return *(const LAS s16x8*)p; }
DI void lds_put(lptr p, u32x4 v) { *(LAS u32x4*)p = v; }
DI s16x4 lds_tr(lptr p) { return __builtin_amdgcn_ds_read_tr16_b64_v4i16((LAS s16x4*)p); }
DI s16x8 tr_frag2(lptr p, lptr q) { s16x4 lo = lds_tr(p), hi = lds_tr(q); return __builtin_shufflevector(lo, hi, 0, 1, 2, 3, 4, 5, 6, 7); }
DI s16x8 tr_frag(lptr p, int second) { return tr_frag2(p, p + second); }
DI float swap_add(float v) { auto r = __builtin_amdgcn_permlane32_swap(__float_as_uint(v), __float_as_uint(v), false, false); return __uint_as_float(r[0]) + __uint_as_float(r[1]); }
DI float swap_max(float v) { auto r = __builtin_amdgcn_permlane32_swap(__float_as_uint(v), __float_as_uint(v), false, false); return fmaxf(__uint_as_float(r[0]), __uint_as_float(r[1])); }
DI float dot2(unsigned a, unsigned b, float c) { return __builtin_amdgcn_fdot2_f32_bf16(__builtin_bit_cast(bf16x2, a), __builtin_bit_cast(bf16x2, b), c, false); }
DI unsigned relu_pk(unsigned u) { return __builtin_bit_cast(unsigned, __builtin_elementwise_max(__builtin_bit_cast(s16x2, u), (s16x2){0, 0})); }
template <int CTRL> DI float dpp(float v) { return __uint_as_float(__builtin_amdgcn_update_dpp(0, __float_as_uint(v), CTRL, 0xf, 0xf, true)); }
DI float row32_max(float v) {
    v = fmaxf(v, dpp<0xB1>(v)); v = fmaxf(v, dpp<0x4E>(v)); v = fmaxf(v, dpp<0x141>(v)); v = fmaxf(v, dpp<0x140>(v));
    auto r = __builtin_amdgcn_permlane16_swap(__float_as_uint(v), __float_as_uint(v), false, false);
    return fmaxf(__uint_as_float(r[0]), __uint_as_float(r[1]));
}
template <int HALF> DI u32x4 pack8(const f32x16& a, float m) {
    u32x4 v;
#pragma unroll
    for (int e = 0; e < 4; ++e) v[e] = cvtpk(a[8 * HALF + 2 * e] * m, a[8 * HALF + 2 * e + 1] * m);
    return v;
}
template <int HALF> DI u32x4 pack8_relu(const f32x16& a) {
    u32x4 v;
#pragma unroll
    for (int e = 0; e < 4; ++e) v[e] = relu_pk(cvtpk(a[8 * HALF + 2 * e], a[8 * HALF + 2 * e + 1]));
    return v;
}
template <int HALF> DI f32x16 pv_mma(s16x8 v, const f32x16& p, f32x16 c, int idx) {
    const auto ph = __builtin_shufflevector(p, p, 8 * HALF, 8 * HALF + 1, 8 * HALF + 2, 8 * HALF + 3, 8 * HALF + 4, 8 * HALF + 5, 8 * HALF + 6, 8 * HALF + 7);
    return __builtin_amdgcn_smfmac_f32_32x32x32_bf16(__builtin_bit_cast(bf16x8, v), __builtin_bit_cast(bf16x16, ph), c, idx, 0, 0);
}
DI void glds16(const void* sbase, unsigned voff, unsigned lds_base) {
    asm volatile("s_mov_b32 m0, %2\n\ts_nop 0\n\tglobal_load_lds_dwordx4 %0, %1" :: "v"(voff), "s"(sbase), "s"(lds_base) : "memory");
}
DI void glds16x2(const void* sbase, unsigned voff0, unsigned voff1, unsigned lds_base) {
    asm volatile("s_mov_b32 m0, %3\n\ts_nop 0\n\tglobal_load_lds_dwordx4 %0, %2\n\tglobal_load_lds_dwordx4 %1, %2 offset:1024" :: "v"(voff0), "v"(voff1), "s"(sbase), "s"(lds_base) : "memory");
}
DI void glds16x4(const void* sbase, unsigned v0, unsigned v1, unsigned v2, unsigned v3, unsigned lds_base) {
    asm volatile("s_mov_b32 m0, %5\n\ts_nop 0\n\tglobal_load_lds_dwordx4 %0, %4\n\tglobal_load_lds_dwordx4 %1, %4 offset:1024\n\t"
                 "global_load_lds_dwordx4 %2, %4 offset:2048\n\tglobal_load_lds_dwordx4 %3, %4 offset:3072" :: "v"(v0), "v"(v1), "v"(v2), "v"(v3), "s"(sbase), "s"(lds_base) : "memory");
}
DI void glds16x2_sc1(const void* sbase, unsigned voff, unsigned lds_base) {
    asm volatile("s_mov_b32 m0, %2\n\ts_nop 0\n\tglobal_load_lds_dwordx4 %0, %1 sc1\n\tglobal_load_lds_dwordx4 %0, %1 offset:1024 sc1" :: "v"(voff), "s"(sbase), "s"(lds_base) : "memory");
}
DI void touch8k(const void* sbase, int lane, unsigned lds_scratch) {
    asm volatile("s_mov_b32 m0, %2\n\ts_nop 0\n\tglobal_load_lds_dword %0, %1" :: "v"(lane * 128), "s"(sbase), "s"(lds_scratch) : "memory");
}
#define WAIT_V(N) asm volatile("s_waitcnt vmcnt(" #N ")" ::: "memory")
DI __amdgpu_buffer_rsrc_t mk_rsrc(const void* p, unsigned bytes) { return __builtin_amdgcn_make_buffer_rsrc((void*)p, 0, bytes, 0x00020000); }
DI u32x4 bload(__amdgpu_buffer_rsrc_t r, unsigned voff, unsigned soff) { return __builtin_amdgcn_raw_buffer_load_b128(r, voff, soff, 0); }
DI u32x4 bload_sc1(__amdgpu_buffer_rsrc_t r, unsigned voff, unsigned soff) { return __builtin_amdgcn_raw_buffer_load_b128(r, voff, soff, 16); }
DI void bstore_sc1(u32x4 v, __amdgpu_buffer_rsrc_t r, unsigned voff) { __builtin_amdgcn_raw_buffer_store_b128(v, r, voff, 0, 16); }

struct BigFrag { s16x8 x0, x1, w0, w1; };
struct SmallFrag { s16x8 x, q; };

__global__ void __launch_bounds__(512, 2)
fused_kernel(const uint16_t* __restrict__ x, const uint16_t* __restrict__ wq, const uint16_t* __restrict__ wk, const uint16_t* __restrict__ wv,
             const uint16_t* __restrict__ wo, const uint16_t* __restrict__ w1, const uint16_t* __restrict__ w2, const uint16_t* __restrict__ gw,
             uint16_t* out, char* ws) {
    __shared__ __attribute__((aligned(16))) char smem[LDS_BYTES];
    const lptr lds = (lptr)smem;
    const unsigned lds0 = (unsigned)(uintptr_t)smem;
    const int tid = threadIdx.x, lane = tid & 63, w = __builtin_amdgcn_readfirstlane(tid >> 6);
    const int r = lane & 31, h = lane >> 5, g1 = (lane >> 4) & 1, q4 = (lane & 15) >> 2, p4 = lane & 3;
    const int b = blockIdx.x, bi = b >> 3;
    const int R = 2 * (b & 7) + (bi >> 4), cb = bi & 15, rh64 = cb & 1, hg = cb >> 1;
    const bool kvcu = cb >= 14, big = w < 4;
    const int row64 = R * 128 + rh64 * 64;
    const int rt = w & 1, hq = w >> 1, fh = hq & 1;

    asm volatile("" :: "s"(x), "s"(wq), "s"(wk), "s"(wv), "s"(wo), "s"(w1), "s"(w2), "s"(gw), "s"(out), "s"(ws));
    const unsigned w64_src = (unsigned)((lane >> 3) * 1024 + hg * 128 + (((lane & 7) ^ (g1 << 2)) << 4));
    const unsigned w64_src1 = w64_src + 8192 - 1024;
    const unsigned xv = (unsigned)((lane >> 3) * 1024 + (((lane & 7) ^ ((lane >> 4) & 3)) << 4)), xv1 = (xv ^ 64) + 8192 - 1024;
    const unsigned wv0 = (unsigned)((lane >> 4) * (DFF * 2) + cb * 256 + (((lane & 15) ^ (((lane >> 4) & 3) << 2)) << 4)), wv1 = wv0 + 4 * DFF * 2 - 1024;
    const int rot = (b + cb) & 7;
    if (!kvcu) {
#pragma unroll
        for (int s = 0; s < 2; ++s) {
            const int sr = (s + rot) & 7;
            glds16x2((const char*)x + R * 131072 + sr * 128 + w * 16384, xv, xv1, lds0 + s * L_STAGE + w * 2048);
            glds16x2((const char*)w1 + (size_t)(sr * 64 + w * 8) * (DFF * 2), wv0, wv1, lds0 + s * L_STAGE + 16384 + w * 2048);
            glds16((const char*)wq + sr * 65536 + w * 8192, w64_src, lds0 + s * L_STAGE + 32768 + w * 1024);
        }
    } else {
        const char* wkv = (const char*)((w & 2) ? wk : wv);
        const unsigned kvv = (w & 1) * 1024 + ((lane ^ ((w & 2) << 2)) << 4);
#pragma unroll 1
        for (int s = 0; s < 8; ++s) {
            glds16((const char*)x + (row64 + 8 * w) * 1024 + s * 128, xv ^ (rt << 6), lds0 + s * L_KSTAGE + w * 1024);
            if (!big) glds16(wkv + s * 2048, kvv, lds0 + s * L_KSTAGE + 8192 + (w & 3) * 1024);
        }
    }
    const unsigned eo = lane == 1 ? WS_HFLAG + (R * 16 + cb) * 128 : !kvcu || lane == 0 || lane > 6 ? WS_EPOCH : lane == 2 ? WS_KVFLAG + (row64 >> 6) * 128 : WS_GZ + row64 * 8 + (lane - 3) * 128;
    const unsigned epoch_v = __hip_atomic_load((gu32*)(ws + eo), RLX_AGENT);
    asm volatile("" ::: "memory");
    auto get_epoch = [&]() { unsigned e = epoch_v; PIN(e); return __builtin_amdgcn_readfirstlane(e) * 0x9E3779B1u + 0x7F4A7C15u; };
    const __amdgpu_buffer_rsrc_t rs_kv = mk_rsrc(ws + WS_KV, KV_BYTES), rs_h = mk_rsrc(ws + WS_H, H_BYTES), rs_o = mk_rsrc(ws + WS_O, O_BYTES);
    const __amdgpu_buffer_rsrc_t rs_kf = mk_rsrc(ws + WS_KVFLAG, 4096);
    const unsigned tr_in = 32 * g1 + 8 * p4;
    const unsigned wq_rd = (8 * h + q4) * 128 + ((fh ^ (q4 >> 1)) << 6) + tr_in;
    unsigned xo[4];
#pragma unroll
    for (int ks = 0; ks < 4; ++ks) xo[ks] = r * 128 + (((2 * ks + h) ^ ((r >> 1) & 7)) << 4);

    lptr kp, vp0, vp1, wrA0, wrA1, wrB0, wrB1, q_rd, qn_rd, km_rd, cst_wr;
    unsigned h_voff, hf_voff, g_voff0, g_voff1, hb, hb1, o_own;
    int spidx;
    f32x16 oacc;
    u32x4 cst, omark;
    s16x8 zf;
    auto consts = [&]() {
        kp = lds + lane * 16;
        const unsigned v_cst = (r & 27) != 16 ? 2064 : 2048;
        vp0 = lds + (r < 16 ? 1024 + h * 256 + r * 16 : v_cst); vp1 = lds + (r < 16 ? 1536 + h * 256 + r * 16 : v_cst);
        const unsigned w_rd0 = w * 2048 + (4 * h + q4) * 128 + ((q4 >> 1) << 6) + tr_in;
        wrA0 = lds + L_WB + w_rd0; wrA1 = lds + L_WB + (w_rd0 ^ 64); wrB0 = lds + w_rd0; wrB1 = lds + (w_rd0 ^ 64);
        q_rd = lds + L_QX + (hq * 2 + rt) * 1024 + lane * 16; qn_rd = lds + L_QN + (hq * 2 + rt) * 128 + r * 4; km_rd = lds + L_KM;
        h_voff = (unsigned)((row64 >> 5) * 112 + 14 * w) * 1024u + lane * 16;
        hf_voff = (unsigned)(R * 16 + (lane & 15)) * 128 + 4; g_voff0 = 32768 + (row64 + 32 * rt + r) * 8; g_voff1 = 32768 + (row64 + 8 * w + ((lane >> 1) & 4) + (lane >> 4)) * 8 + 4;
        spidx = 0xDDDD; oacc = (f32x16){}; zf = (s16x8){};
        cst = (lane & 1) ? (u32x4){} : (u32x4){0x3f803f80u, 0x3f803f80u, 0x3f803f80u, 0x3f803f80u};
        cst_wr = lds + (tid >> 1) * KVT + 2048 + (tid & 1) * 16;
        hb = (kvcu ? OOB : (unsigned)((R * 4 + 2 * rt) * 112 + cb * 8 + fh * 4) * 1024u) + lane * 16; hb1 = hb + 112 * 1024;
        o_own = (unsigned)(((row64 >> 5) + rt) * 32 + hg * 4 + hq) * 1024u + lane * 16;
        omark = (u32x4){~0u, ~0u, ~0u, ~0u};
        PIN(oacc); PIN(spidx); PIN(cst); PIN(cst_wr); PIN(hb); PIN(hb1); PIN(zf); PIN(o_own); PIN(omark);
        PIN(kp); PIN(vp0); PIN(vp1); PIN(wrA0); PIN(wrA1); PIN(wrB0); PIN(wrB1); PIN(q_rd); PIN(qn_rd); PIN(km_rd);
        PIN(h_voff); PIN(hf_voff); PIN(g_voff0); PIN(g_voff1);
        SBAR();
    };
    u32x4 hs[8];
#pragma unroll
    for (int i = 0; i < 8; ++i) asm volatile("" : "=v"(hs[i]));
    f32x16 a00 = {}, a01 = {}, a10 = {}, a11 = {};
    auto kv_wait = [&](u32x4& fv) {
        const unsigned ep = get_epoch();
        if (__builtin_expect(!__all(fv[1] == ep && fv[3] == ep), 0)) {
            unsigned spins = 0;
            do { __builtin_amdgcn_s_sleep(6); CFENCE(); fv = bload_sc1(rs_kf, r * 128 + h * 16, 0); } while (!__all(fv[1] == ep && fv[3] == ep) && ++spins < SPIN_CAP);
        }
        CFENCE();
    };
    auto kv_bound = [&](const u32x4& fv) {
        const float km = row32_max(fmaxf(__uint_as_float(fv[0]), __uint_as_float(fv[2])));
        if (tid == 256) *(LAS float*)(lds + L_KM) = __builtin_amdgcn_sqrtf(km) * 1.0001f;
    };
    auto kv_tile = [&](int t) { glds16x2_sc1(ws + WS_KV + ((t + 8 * bi) & 63) * 2048, lane * 16, lds0 + t * KVT); };
    if (!kvcu) {
        unsigned cur = 0, nxt = L_STAGE;
        if (big) {
            const unsigned xb = rt * 8192, w0o = 16384 + (8 * h + q4) * 256 + (((2 * fh) ^ q4) << 6) + tr_in, w1o = w0o ^ 64;
            BigFrag F[4];
            auto rd = [&](BigFrag& n, lptr sb, int ks) {
                n.x0 = lds_frag(sb + xb + xo[ks]); n.x1 = lds_frag(sb + xb + 4096 + xo[ks]); n.w0 = tr_frag(sb + w0o + ks * 4096, 1024); n.w1 = tr_frag(sb + w1o + ks * 4096, 1024); };
            auto step = [&](const BigFrag& c, BigFrag& n, lptr sb, int ks) {
                a00 = MFMA(c.w0, c.x0, a00); SBAR(); n.x0 = lds_frag(sb + xb + xo[ks]); n.x1 = lds_frag(sb + xb + 4096 + xo[ks]); SBAR();
                a10 = MFMA(c.w0, c.x1, a10); SBAR(); n.w0 = tr_frag(sb + w0o + ks * 4096, 1024); SBAR();
                a01 = MFMA(c.w1, c.x0, a01); SBAR(); n.w1 = tr_frag(sb + w1o + ks * 4096, 1024); SBAR();
                a11 = MFMA(c.w1, c.x1, a11); SBAR(); };
            auto stage = [&](auto bar) {
                step(F[0], F[3], lds + cur, 3);
                bar();
                step(F[1], F[0], lds + nxt, 0); step(F[2], F[1], lds + nxt, 1); step(F[3], F[2], lds + nxt, 2);
                cur = nxt; nxt = (nxt == 2 * L_STAGE) ? 0 : nxt + L_STAGE; };
            consts();
            BAR_VL(6);
            rd(F[0], lds, 0); rd(F[1], lds, 1); rd(F[2], lds, 2);
#pragma unroll 1
            for (int s = 0; s < 7; ++s) stage([&] {
                BAR_VL(1);
                if (s == 3 && w < 2) touch8k(w == 0 ? (const char*)w2 + (R * 14 + cb) * 8192 : (const char*)wo + (b & 63) * 8192, lane, lds0 + L_TOUCH); });
            rd(F[3], lds + cur, 3);
#define LAST4(acc, wm, xm) do { _Pragma("unroll") for (int ks = 0; ks < 4; ++ks) acc = MFMA(F[ks].wm, F[ks].xm, acc); } while (0)
#define LAST4P(acc, wm, xm, prev, i) do { acc = MFMA(F[0].wm, F[0].xm, acc); acc = MFMA(F[1].wm, F[1].xm, acc); SBAR(); hs[i] = pack8_relu<0>(prev); PIN(hs[i]); SBAR(); \
            acc = MFMA(F[2].wm, F[2].xm, acc); SBAR(); hs[(i) + 1] = pack8_relu<1>(prev); PIN(hs[(i) + 1]); SBAR(); acc = MFMA(F[3].wm, F[3].xm, acc); SBAR(); } while (0)
            LAST4(a00, w0, x0); SBAR();
            LAST4P(a01, w1, x0, a00, 0); LAST4P(a10, w0, x1, a01, 2); LAST4P(a11, w1, x1, a10, 4);
            hs[6] = pack8_relu<0>(a11); hs[7] = pack8_relu<1>(a11);
        } else {
            const int j = w - 4;
            __builtin_amdgcn_s_setprio(2);
            const unsigned xb = (2 * rh64 + rt) * 4096, qo = 32768 + wq_rd;
            SmallFrag F[4];
            auto rd = [&](SmallFrag& n, lptr sb, int ks) { n.x = lds_frag(sb + xb + xo[ks]); n.q = tr_frag(sb + qo + ks * 2048, 512); SBAR(); };
            auto mma = [&](const SmallFrag& c) { a00 = MFMA(c.q, c.x, a00); SBAR(); };
            const unsigned xv2 = xv + 2 * (8192 - 1024), xv3 = xv1 + 2 * (8192 - 1024), wv2 = wv0 + 2 * (4 * DFF * 2 - 1024), wv3 = wv1 + 2 * (4 * DFF * 2 - 1024);
            const char* xs = (const char*)x + R * 131072 + j * 32768;
            const char* w1s = (const char*)w1 + (size_t)(j * 16) * (DFF * 2);
            const char* wqs = (const char*)wq + j * 16384;
            auto fill0 = [&](int sr, unsigned slot) { glds16x4(xs + sr * 128, xv, xv1, xv2, xv3, lds0 + slot + j * 4096); };
            auto fill1 = [&](int sr, unsigned slot) { glds16x4(w1s + sr * (64 * DFF * 2), wv0, wv1, wv2, wv3, lds0 + slot + 16384 + j * 4096); };
            auto fill2 = [&](int sr, unsigned slot) { glds16x2(wqs + sr * 65536, w64_src, w64_src1, lds0 + slot + 32768 + j * 2048); };
            fill0((rot + 2) & 7, 2 * L_STAGE); fill1((rot + 2) & 7, 2 * L_STAGE); fill2((rot + 2) & 7, 2 * L_STAGE);
            consts();
            BAR_VL(16);
            rd(F[0], lds, 0); rd(F[1], lds, 1); rd(F[2], lds, 2);
#pragma unroll 1
            for (int s = 0; s < 6; ++s) {
                const bool more = s < 5;
                const int sr = (rot + s + 3) & 7;
                rd(F[3], lds + cur, 3); mma(F[0]);
                BAR_VL(10);
                if (more) fill0(sr, cur);
                rd(F[0], lds + nxt, 0); mma(F[1]);
                if (more) fill1(sr, cur);
                rd(F[1], lds + nxt, 1); mma(F[2]);
                if (more) fill2(sr, cur);
                rd(F[2], lds + nxt, 2); mma(F[3]);
                cur = nxt; nxt = (nxt == 2 * L_STAGE) ? 0 : nxt + L_STAGE;
            }
            u32x4 fv = bload_sc1(rs_kf, r * 128 + h * 16, 0);
            rd(F[3], lds, 3); mma(F[0]);
            BAR_VL(0);
            rd(F[0], lds + L_STAGE, 0); mma(F[1]);
            kv_wait(fv);
            kv_tile(j); kv_tile(4 + j);
            rd(F[1], lds + L_STAGE, 1); mma(F[2]);
            kv_tile(8 + j); kv_tile(12 + j);
            rd(F[2], lds + L_STAGE, 2); mma(F[3]); rd(F[3], lds + L_STAGE, 3);
            mma(F[0]); kv_bound(fv); mma(F[1]); mma(F[2]); mma(F[3]);
        }
    } else {
        const unsigned xb = rt * 4096, w64_src2 = w64_src + 2 * (8192 - 1024), w64_src3 = w64_src + 3 * (8192 - 1024);
        {
            const unsigned wkv_rd = 8192 + g1 * 2048 + (((8 * h + q4) * 32 + 8 * p4) ^ (g1 << 7));
#pragma unroll
            for (int g = 0; g < 2; ++g) {
                if (g == 0) { if (big) BAR_VL(5); else BAR_VL(9); } else BAR_VL(1);
                const lptr st = lds + (g * 4 + hq) * L_KSTAGE;
#pragma unroll
                for (int k = 0; k < 4; ++k) a00 = MFMA(tr_frag2(st + wkv_rd + k * 512, st + (wkv_rd ^ 128) + k * 512), lds_frag(st + xb + xo[k]), a00);
            }
        }
        BAR_L();
        {
            const lptr pw = lds + w * L_KSTAGE + 8192 + r * 128;
            const unsigned sz = (r & 7) ^ ((r >> 1) & 4);
#pragma unroll
            for (int i = 0; i < 4; ++i) *(LAS f32x4*)(pw + (((2 * i + h) ^ sz) << 4)) = (f32x4){a00[4 * i], a00[4 * i + 1], a00[4 * i + 2], a00[4 * i + 3]};
            a00 = (f32x16){};
        }
        BAR_L();
        if (big) {
            const unsigned kvo = (unsigned)((row64 >> 5) + rt) * 2048u + lane * 16;
            const lptr pr = lds + rt * L_KSTAGE + 8192;
            float n2 = 0.f;
            if (fh == 0) {
                const unsigned sz = (r & 7) ^ ((r >> 1) & 4);
                f32x4 lo = {}, hi = {};
#pragma unroll
                for (int i = 0; i < 4; ++i) { lo += *(const LAS f32x4*)(pr + i * 2 * L_KSTAGE + r * 128 + (((4 + h) ^ sz) << 4)); hi += *(const LAS f32x4*)(pr + i * 2 * L_KSTAGE + r * 128 + (((6 + h) ^ sz) << 4)); }
                const u32x4 v = {cvtpk(lo[0], lo[1]), cvtpk(lo[2], lo[3]), cvtpk(hi[0], hi[1]), cvtpk(hi[2], hi[3])};
                bstore_sc1(v, rs_kv, kvo);
                n2 = row32_max(swap_add(dot2(v[0], v[0], dot2(v[1], v[1], 0.f)) + dot2(v[2], v[2], dot2(v[3], v[3], 0.f))));
            } else {
                float t[8];
#pragma unroll
                for (int j = 0; j < 8; ++j) {
                    const lptr q = pr + ((lane >> 4) * 8 + j) * 128 + ((q4 ^ j ^ (g1 << 2)) << 4) + p4 * 4;
                    t[j] = (*(const LAS float*)q + *(const LAS float*)(q + 2 * L_KSTAGE)) + (*(const LAS float*)(q + 4 * L_KSTAGE) + *(const LAS float*)(q + 6 * L_KSTAGE));
                }
                bstore_sc1((u32x4){cvtpk(t[0], t[1]), cvtpk(t[2], t[3]), cvtpk(t[4], t[5]), cvtpk(t[6], t[7])}, rs_kv, kvo + 1024);
            }
            WAIT_V(0);
            const unsigned ep = get_epoch();
            if (lane == 0) __hip_atomic_store((gu64*)(ws + WS_KVFLAG) + (row64 >> 6) * 16 + w, ((unsigned long long)ep << 32) | __float_as_uint(n2), RLX_AGENT);
            u32x4 gf[16];
#pragma unroll
            for (int i = 0; i < 16; ++i) gf[i] = *(const u32x4*)(gw + i * 32 + (lane >> 4) * 8);
            consts();
            BAR_L();
            f32x4 z = {};
            const int zr = 16 * w + (lane & 15);
#pragma unroll
            for (int i = 0; i < 16; ++i)
                z = __builtin_amdgcn_mfma_f32_16x16x32_bf16(asfrag(gf[i]), lds_frag(lds + (i >> 1) * L_KSTAGE + zr * 128 + (((4 * (i & 1) + (lane >> 4)) ^ ((zr >> 1) & 7)) << 4)), z, 0, 0, 0);
            const float gt = rbf(__builtin_amdgcn_rcpf(1.f + __expf(-rbf(z[0])))), og = fmaxf(rbf(1.f - gt), 0x1p-40f);
            if (lane < 16) __hip_atomic_store((gu64*)(ws + WS_GZ) + row64 + zr, ((unsigned long long)__float_as_uint(og) << 32) | __float_as_uint(gt / og), RLX_AGENT);
            BAR_L();
        } else {
#pragma unroll
            for (int i = 0; i < 4; ++i) { const int p = i * 16 + (w - 4) * 4; glds16x4((const char*)wq + p * 8192, w64_src, w64_src1, w64_src2, w64_src3, lds0 + L_KWQ + p * 1024); }
            consts();
            BAR_VL(0);
            SmallFrag F[4];
            auto rd = [&](SmallFrag& n, int s, int k) { n.x = lds_frag(lds + s * L_KSTAGE + xb + xo[k]); n.q = tr_frag(lds + L_KWQ + s * 8192 + wq_rd + k * 2048, 512); SBAR(); };
#pragma unroll
            for (int k = 0; k < 4; ++k) rd(F[k], 0, k);
#pragma unroll 1
            for (int s = 1; s < 8; ++s) {
#pragma unroll
                for (int k = 0; k < 4; ++k) { a00 = MFMA(F[k].q, F[k].x, a00); SBAR(); rd(F[k], s, k); }
            }
            u32x4 fv = bload_sc1(rs_kf, r * 128 + h * 16, 0);
#pragma unroll
            for (int k = 0; k < 4; ++k) a00 = MFMA(F[k].q, F[k].x, a00);
            BAR_L();
            kv_wait(fv);
            kv_tile(w - 4); kv_tile(w); kv_tile(4 + w); kv_tile(8 + w);
            kv_bound(fv);
        }
    }
    if (big) {
        if (tid < 32) lds_put(cst_wr, cst);
    } else {
#pragma unroll
        for (int s = 0; s < 2; ++s) {
            const u32x4 v = s ? pack8<1>(a00, QSCALE) : pack8<0>(a00, QSCALE);
            lds_put(lds + L_QX + (4 * fh + 2 * s + rt) * 1024 + lane * 16, v);
            const float n2 = swap_add(dot2(v[0], v[0], dot2(v[1], v[1], 0.f)) + dot2(v[2], v[2], dot2(v[3], v[3], 0.f)));
            *(LAS float*)(lds + L_QN + (4 * fh + 2 * s + rt) * 128 + r * 4) = __builtin_amdgcn_sqrtf(n2);
        }
    }
    __builtin_amdgcn_s_setprio(3);
    BAR_VL(0);
    const s16x8 qf = lds_frag(q_rd);
    s16x8 kf = lds_frag(kp), vf0, vf1;
    float mshift = *(const LAS float*)qn_rd * *(const LAS float*)km_rd;
    vf0 = lds_frag(vp0);
    if (__builtin_expect(!__all(mshift <= 0.5f * SAFE_RANGE), 0)) {
        float m = -__builtin_inff();
#pragma unroll 1
        for (int t = 0; t < 64; ++t) {
            const f32x16 s = MFMA(asfrag(bload_sc1(rs_kv, lane * 16, t * 2048)), qf, (f32x16){});
#pragma unroll
            for (int i = 0; i < 16; ++i) m = fmaxf(m, s[i]);
        }
        mshift = swap_max(m);
    }
    f32x16 cinit;
#pragma unroll
    for (int i = 0; i < 16; ++i) cinit[i] = -mshift;
    PIN(cinit);
    f32x16 sA = MFMA(kf, qf, cinit), sB;
    kf = lds_frag(kp + KVT);
    if (tid >= 32 && tid < 128) lds_put(cst_wr, cst);

#define S1(S_, i) S_[i] = __builtin_amdgcn_exp2f(S_[i])
#define EX2(S_, i) do { S1(S_, i); S1(S_, (i) + 1); } while (0)
#define EX3(S_, i) do { S1(S_, i); S1(S_, (i) + 1); S1(S_, (i) + 2); } while (0)
#define EX4(S_, i) do { EX2(S_, i); EX2(S_, (i) + 2); } while (0)
#define PV1(SN, T) do { if ((T) >= 0) oacc = pv_mma<1>(vf1, SN, oacc, spidx); vf1 = lds_frag(vp1 + ((T) < 0 ? 0 : (T)) * KVT); SBAR(); } while (0)
#define QK(SN, T) do { SN = MFMA(kf, qf, cinit); kf = lds_frag(kp + ((T) + 2) * KVT); SBAR(); } while (0)
#define PV0(SC, T) do { oacc = pv_mma<0>(vf0, SC, oacc, spidx); vf0 = lds_frag(vp0 + ((T) + 1) * KVT); SBAR(); } while (0)
#define CUT(S_) do { PIN(S_); SBAR(); } while (0)
#define ITER0(SC, SN, T, T1, PR, MEM) do { PR; \
    EX2(SC, 0); CUT(SC); PV1(SN, T1); EX4(SC, 2); CUT(SC); QK(SN, T); EX4(SC, 6); S1(SC, 10); CUT(SC); PV0(SC, T); MEM; S1(SC, 11); EX4(SC, 12); CUT(SC); } while (0)
#define ITER1(SC, SN, T, PR, PRE, MEM, G1) do { PR; PRE; \
    EX2(SC, 0); CUT(SC); PV1(SN, T); EX4(SC, 2); CUT(SC); QK(SN, T); EX4(SC, 6); CUT(SC); PV0(SC, T); MEM; EX4(SC, 10); CUT(SC); G1; SBAR(); EX2(SC, 14); CUT(SC); } while (0)
#define ITERG(SC, SN, T, PR, MEM, G0, G1) do { PR; \
    EX2(SC, 0); CUT(SC); PV1(SN, T); EX3(SC, 2); CUT(SC); QK(SN, T); EX3(SC, 5); CUT(SC); G0; SBAR(); EX3(SC, 8); CUT(SC); PV0(SC, T); MEM; EX3(SC, 11); CUT(SC); G1; SBAR(); EX2(SC, 14); CUT(SC); } while (0)
#define NOP (void)0
#define PRIO(p) __builtin_amdgcn_s_setprio(p)
#define ITER2(T, P0, P1, M0, M1) do { ITER0(sA, sB, T, T, PRIO(P0), M0); ITER0(sB, sA, (T) + 1, (T) + 1, PRIO(P1), M1); } while (0)
#define ADV(n) do { kp += (n) * KVT; vp0 += (n) * KVT; vp1 += (n) * KVT; PIN(kp); PIN(vp0); PIN(vp1); } while (0)
    const char* const w2w = (const char*)w2 + w * (14 * 16384);
    const char* const wow = (const char*)wo + w * 65536;
    int pn = (row64 >> 6) % 7;
    const unsigned wbA = lds0 + L_WB + w * 2048, wbB = lds0 + w * 2048;
    const __amdgpu_buffer_rsrc_t rs_f = mk_rsrc(ws + WS_HFLAG, 32768 + 16384);
    u32x4 hA0, hA1, hB0, hB1;
    s16x8 wf0, wf1;
#define W_LD(src, buf) glds16x2(src, w64_src, w64_src1, buf)
#define H_ST(i) do { if (big) bstore_sc1(hs[i], rs_h, ((i) < 4 ? hb : hb1) + ((i) & 3) * 1024); } while (0)
    ITER0(sA, sB, 0, -1, PRIO(3), H_ST(0)); ITER0(sB, sA, 1, 1, NOP, H_ST(1); kv_tile(16 + w));
    ITER2(2, 3, 3, H_ST(2); kv_tile(24 + w), H_ST(3); kv_tile(32 + w));
    ITER2(4, 2, 2, H_ST(4); kv_tile(40 + w), H_ST(5); kv_tile(48 + w));
    ITER2(6, 2, 2, H_ST(6); kv_tile(56 + w), H_ST(7); W_LD(w2w + pn * 32768, wbA));
    ITER2(8, 1, 1, bstore_sc1(omark, rs_o, o_own), NOP);
    ITER2(10, 1, 1, NOP, NOP);
    ITER2(12, 0, 0, NOP, NOP);
    BAR_VL(0);
    const unsigned epoch = get_epoch();
    if (tid == 0) __hip_atomic_store((gu32*)(ws + WS_HFLAG) + (R * 16 + cb) * 32 + 1, epoch, RLX_AGENT);
    unsigned hfv;
    ITER2(14, 1, 3, W_LD(w2w + pn * 32768 + 16384, wbB); a00 = MFMA(zf, zf, (f32x16){}); PIN(zf); a01 = MFMA(zf, zf, (f32x16){}), PIN(zf); a10 = MFMA(zf, zf, (f32x16){}); PIN(zf); a11 = MFMA(zf, zf, (f32x16){}));
    ITER2(16, 2, 1, NOP, hfv = __builtin_amdgcn_raw_buffer_load_b32(rs_f, hf_voff, 0, 16));
    ITER0(sA, sB, 18, 18, PRIO(3), NOP);
    if (__builtin_expect(!__all(hfv == epoch), 0)) {
        unsigned spins = 0;
        do { __builtin_amdgcn_s_sleep(6); CFENCE(); hfv = __builtin_amdgcn_raw_buffer_load_b32(rs_f, hf_voff, 0, 16); } while (!__all(hfv == epoch) && ++spins < SPIN_CAP);
    }
    CFENCE();
    unsigned gr_u, og_u;
    ITER0(sB, sA, 19, 19, PRIO(2), gr_u = __builtin_amdgcn_raw_buffer_load_b32(rs_f, g_voff0, 0, 16); og_u = __builtin_amdgcn_raw_buffer_load_b32(rs_f, g_voff1, 0, 16);
          hA0 = bload_sc1(rs_h, h_voff, pn * 2048); hA1 = bload_sc1(rs_h, h_voff, pn * 2048 + 112 * 1024));
    ITER2(20, 1, 3, hB0 = bload_sc1(rs_h, h_voff, pn * 2048 + 1024), hB1 = bload_sc1(rs_h, h_voff, pn * 2048 + 113 * 1024));
    ADV(22);
    PIN(a00); PIN(a01); PIN(a10); PIN(a11);
#define GM(acc, wf, H) acc = MFMA(asfrag(H), wf, acc); SBAR()
#define GSTEP(SX, SY, T, R0, R1, H0, H1, LW, LH0, LH1) do { \
    ITER1(SX, SY, T, PRIO(3), WAIT_V(5); wf0 = tr_frag(R0, 1024); wf1 = tr_frag(R1, 1024), NOP, GM(a00, wf0, H0)); \
    ITERG(SY, SX, (T) + 1, PRIO(2), LW, GM(a01, wf1, H0); LH0, GM(a10, wf0, H1)); \
    ITER1(SX, SY, (T) + 2, PRIO(1), NOP, NOP, GM(a11, wf1, H1); LH1); } while (0)
#pragma unroll 1
    for (int n = 0; n < 4; ++n) {
        pn = pn == 6 ? 0 : pn + 1;
        const char* wsrc = w2w + pn * 32768;
        GSTEP(sA, sB, 0, wrA0, wrA1, hA0, hA1, W_LD(wsrc, wbA), hA0 = bload_sc1(rs_h, h_voff, pn * 2048), hA1 = bload_sc1(rs_h, h_voff, pn * 2048 + 112 * 1024));
        GSTEP(sB, sA, 3, wrB0, wrB1, hB0, hB1, W_LD(wsrc + 16384, wbB), hB0 = bload_sc1(rs_h, h_voff, pn * 2048 + 1024), hB1 = bload_sc1(rs_h, h_voff, pn * 2048 + 113 * 1024));
        ADV(6);
    }
    BAR_L();
    pn = pn == 6 ? 0 : pn + 1;
    const int pm = pn == 6 ? 0 : pn + 1;
    u32x4 hC0, hC1, hD0, hD1, hE0, hE1, hF0, hF1;
    ITER2(0, 3, 2, W_LD(w2w + pn * 32768, wbB + 16384); hC0 = bload_sc1(rs_h, h_voff, pn * 2048), hC1 = bload_sc1(rs_h, h_voff, pn * 2048 + 112 * 1024));
    ITER2(2, 1, 3, W_LD(w2w + pn * 32768 + 16384, wbB + 32768); hD0 = bload_sc1(rs_h, h_voff, pn * 2048 + 1024), hD1 = bload_sc1(rs_h, h_voff, pn * 2048 + 113 * 1024));
    ITER2(4, 2, 1, W_LD(w2w + pm * 32768, wbB + 49152); hE0 = bload_sc1(rs_h, h_voff, pm * 2048), hE1 = bload_sc1(rs_h, h_voff, pm * 2048 + 112 * 1024));
    ITER2(6, 3, 2, W_LD(w2w + pm * 32768 + 16384, wbB + 65536); hF0 = bload_sc1(rs_h, h_voff, pm * 2048 + 1024), hF1 = bload_sc1(rs_h, h_voff, pm * 2048 + 113 * 1024));
    const bool closer = b == 112 && w == 7;
    unsigned ef[4];
    auto ef_load = [&]() {
#pragma unroll
        for (int i = 0; i < 4; ++i) ef[i] = __builtin_amdgcn_raw_buffer_load_b32(mk_rsrc(ws + WS_HFLAG + i * 8192 + 4, closer ? 8188 : 0), lane * 128, 0, 16);
    };
    const unsigned xtra = w < 3 ? 5 * 16384 : L_QX - 3 * 2048;
    s16x8 wf2, wf3, wf4, wf5;
    ITER2(8, 1, 3, WAIT_V(14); wf0 = tr_frag(wrA0, 1024); wf1 = tr_frag(wrA1, 1024); W_LD(wow + 49152, wbB + xtra), wf2 = tr_frag(wrB0, 1024); wf3 = tr_frag(wrB1, 1024); ef_load());
    ITER2(10, 2, 1, wf4 = tr_frag(wrB0 + 16384, 1024); wf5 = tr_frag(wrB1 + 16384, 1024); PIN(wf0); PIN(wf1); W_LD(wow, wbA), PIN(wf2); PIN(wf3); W_LD(wow + 16384, wbB));
    ITER2(12, 3, 3, PIN(wf4); PIN(wf5); W_LD(wow + 32768, wbB + 16384), NOP);
    ITER2(14, 2, 2, NOP, NOP);
    ITER2(16, 1, 0, NOP, NOP);
#define GMA(acc, wf, H) asm volatile("v_mfma_f32_32x32x16_bf16 %0, %1, %2, %0" : "+v"(acc) : "v"(H), "v"(wf))
    PRIO(3);
    oacc = pv_mma<1>(vf1, sB, oacc, spidx);
    {
        u32x4 v = pack8<0>(oacc, __builtin_amdgcn_rcpf(oacc[8]) * __uint_as_float(gr_u));
#pragma unroll
        for (int e = 0; e < 4; ++e) v[e] = v[e] < 0xfffffffeu ? v[e] : 0xfffffffeu;
        bstore_sc1(v, rs_o, o_own);
        asm volatile("s_nop 1");
    }
    PRIO(0);
    GMA(a00, wf0, hA0); GMA(a01, wf1, hA0);
    WAIT_V(1);
    lptr wrE0 = wrB0 + 32768, wrE1 = wrB1 + 32768; PIN(wrE0); PIN(wrE1);
    const s16x8 wf6 = tr_frag(wrE0, 1024), wf7 = tr_frag(wrE1, 1024), wf8 = tr_frag(wrE0 + 16384, 1024), wf9 = tr_frag(wrE1 + 16384, 1024);
    SBAR();
    GMA(a10, wf0, hA1); GMA(a11, wf1, hA1);
    const s16x8 wfa = tr_frag(wrE0 + 32768, 1024), wfb = tr_frag(wrE1 + 32768, 1024);
    s16x8 wof[8];
    wof[0] = tr_frag(wrA0, 1024); wof[1] = tr_frag(wrA1, 1024);
    SBAR();
    GMA(a00, wf2, hB0); GMA(a01, wf3, hB0);
    wof[2] = tr_frag(wrB0, 1024); wof[3] = tr_frag(wrB1, 1024); wof[4] = tr_frag(wrB0 + 16384, 1024); wof[5] = tr_frag(wrB1 + 16384, 1024);
    SBAR();
    GMA(a10, wf2, hB1); GMA(a11, wf3, hB1);
    wof[6] = tr_frag(wrB0 + xtra, 1024); wof[7] = tr_frag(wrB1 + xtra, 1024);
    SBAR();
    GMA(a00, wf4, hC0); GMA(a01, wf5, hC0); GMA(a10, wf4, hC1); GMA(a11, wf5, hC1);
    const unsigned ob = (unsigned)((row64 >> 5) * 32 + 4 * w) * 1024u;
    u32x4 of[8];
    auto o_load = [&]() {
#pragma unroll
        for (int i = 0; i < 8; ++i) of[i] = bload_sc1(rs_o, lane * 16 + (i & 3) * 1024, ob + (i >> 2) * 32768);
    };
    auto o_ready = [&]() {
        unsigned m = 0;
#pragma unroll
        for (int i = 0; i < 8; ++i) { m = max(m, max(of[i][0], of[i][1])); m = max(m, max(of[i][2], of[i][3])); }
        return __all(m != ~0u);
    };
    BAR_VL(0);
#pragma unroll
    for (int i = 0; i < 8; ++i) PIN(wof[i]);
    o_load();
    SBAR();
    GMA(a00, wf6, hD0); GMA(a01, wf7, hD0); GMA(a10, wf6, hD1); GMA(a11, wf7, hD1);
    GMA(a00, wf8, hE0); GMA(a01, wf9, hE0); GMA(a10, wf8, hE1); GMA(a11, wf9, hE1);
    GMA(a00, wfa, hF0); GMA(a01, wfb, hF0); GMA(a10, wfa, hF1); GMA(a11, wfb, hF1);
    SBAR();
    const lptr prb = lds + (((w >> 2) * 2 + ((lane >> 2) & 1)) * 16 + 4 * (w & 3) + (lane >> 4)) * 2048 + ((lane >> 3) & 1) * 128 + p4 * 32;
    lptr pr0 = prb + g1 * 16, pr1 = prb + (g1 ^ 1) * 16;
    PIN(pr0); PIN(pr1);
    uint16_t* outp = out + (row64 + 8 * w + ((lane >> 1) & 4) + (lane >> 4)) * 512 + hg * 64 + (lane & 7) * 8;
    PIN(outp);
    SBAR();
    if (!o_ready()) {
        unsigned spins = 0;
        do { CFENCE(); o_load(); } while (!o_ready() && ++spins < SPIN_CAP);
    }
#define AMM(acc, wi, oi) GMA(acc, wof[wi], of[oi])
#define PUT(a, i, off) asm volatile("ds_write_addtid_b32 %0 offset:%4\n\tds_write_addtid_b32 %1 offset:%5\n\tds_write_addtid_b32 %2 offset:%6\n\tds_write_addtid_b32 %3 offset:%7" \
    :: "v"(a[4 * (i)]), "v"(a[4 * (i) + 1]), "v"(a[4 * (i) + 2]), "v"(a[4 * (i) + 3]), "n"((off) + (i) * 8192), "n"((off) + (i) * 8192 + 2048), "n"((off) + (i) * 8192 + 4096), "n"((off) + (i) * 8192 + 6144) : "memory")
#define SETM0(v, prev) asm volatile("s_mov_b32 m0, %1\n\ts_nop 10" : "+v"(prev) : "s"(v))
#define MM_PUT(acc, c, o, prev, m0v, off) do { AMM(acc, c, o); SETM0(m0v, prev); PUT(prev, 0, off); AMM(acc, 2 + (c), (o) + 1); PUT(prev, 1, off); \
    AMM(acc, 4 + (c), (o) + 2); PUT(prev, 2, off); AMM(acc, 6 + (c), (o) + 3); PUT(prev, 3, off); } while (0)
    const unsigned m0a = lds0 + w * 256, m0b = m0a + 63616;
    asm volatile("s_nop 1");
    AMM(a00, 0, 0); AMM(a00, 2, 1); AMM(a00, 4, 2); AMM(a00, 6, 3);
    MM_PUT(a01, 1, 0, a00, m0a, 0);
    MM_PUT(a10, 0, 4, a01, m0a, 32768);
    MM_PUT(a11, 1, 4, a10, m0b, 65536 - 63616);
    asm volatile("s_nop 7" : "+v"(a11));
    PUT(a11, 0, 98304 - 63616); PUT(a11, 1, 98304 - 63616); PUT(a11, 2, 98304 - 63616); PUT(a11, 3, 98304 - 63616);
    BAR_L();
    {
        f32x4 t[16];
#pragma unroll
        for (int k = 0; k < 8; ++k) { t[2 * k] = *(const LAS f32x4*)(pr0 + k * 256); t[2 * k + 1] = *(const LAS f32x4*)(pr1 + k * 256); }
        SBAR();
        f32x4 lo = t[0], hi = t[1];
#pragma unroll
        for (int k = 1; k < 8; ++k) { lo += t[2 * k]; hi += t[2 * k + 1]; }
        lo *= __uint_as_float(og_u); hi *= __uint_as_float(og_u);
        const unsigned l0 = cvtpk(lo[0], lo[1]), l1 = cvtpk(lo[2], lo[3]), h0 = cvtpk(hi[0], hi[1]), h1 = cvtpk(hi[2], hi[3]);
        const u32x4 v = g1 ? (u32x4){h0, h1, l0, l1} : (u32x4){l0, l1, h0, h1};
        __builtin_nontemporal_store(v, (GAS u32x4*)outp);
    }
    if (closer) {
        unsigned spins = 0;
        while (!__all(ef[0] == epoch && ef[1] == epoch && ef[2] == epoch && ef[3] == epoch) && ++spins < SPIN_CAP) { CFENCE(); ef_load(); }
        if (lane == 0) __hip_atomic_store((gu32*)(ws + WS_EPOCH), epoch, RLX_AGENT);
    }
}

extern "C" void kernel_launch(void* const* d_in, const int* in_sizes, int n_in, void* d_out, int out_size, void* d_ws, size_t ws_size,
                              hipStream_t stream) {
    fused_kernel<<<256, 512, 0, stream>>>((const uint16_t*)d_in[0], (const uint16_t*)d_in[1], (const uint16_t*)d_in[2], (const uint16_t*)d_in[3],
                                          (const uint16_t*)d_in[4], (const uint16_t*)d_in[6], (const uint16_t*)d_in[7], (const uint16_t*)d_in[8],
                                          (uint16_t*)d_out, (char*)d_ws);
}
```

```cpp
#include <hip/hip_runtime.h>
#include <stdint.h>

typedef short s16x8 __attribute__((ext_vector_type(8)));
typedef short s16x4 __attribute__((ext_vector_type(4)));
typedef short s16x2 __attribute__((ext_vector_type(2)));
typedef float f32x16 __attribute__((ext_vector_type(16)));
typedef float f32x4 __attribute__((ext_vector_type(4)));
typedef float f32x2 __attribute__((ext_vector_type(2)));
typedef unsigned u32x4 __attribute__((ext_vector_type(4)));
typedef __bf16 bf16x2 __attribute__((ext_vector_type(2)));
typedef __bf16 bf16x8 __attribute__((ext_vector_type(8)));
typedef __bf16 bf16x16 __attribute__((ext_vector_type(16)));
#define LAS __attribute__((address_space(3)))
#define GAS __attribute__((address_space(1)))
typedef GAS unsigned gu32;
typedef GAS unsigned long long gu64;
typedef LAS char* lptr;
#define DI __device__ __forceinline__
#define RLX_AGENT __ATOMIC_RELAXED, __HIP_MEMORY_SCOPE_AGENT
#define MFMA(a, b, c) __builtin_amdgcn_mfma_f32_32x32x16_bf16((a), (b), (c), 0, 0, 0)
#define SBAR() __builtin_amdgcn_sched_barrier(0)
#define PIN(x) asm volatile("" : "+v"(x))
#define CFENCE() do { asm volatile("" ::: "memory"); __builtin_amdgcn_fence(__ATOMIC_ACQUIRE, "wavefront"); } while (0)
#define BAR_L() asm volatile("s_waitcnt lgkmcnt(0)\n\ts_barrier" ::: "memory")
#define BAR_VL(N) asm volatile("s_waitcnt vmcnt(" #N ") lgkmcnt(0)\n\ts_barrier" ::: "memory")

constexpr int DFF = 1792;
constexpr float QSCALE = 0.25f * 1.4426950408889634f;
constexpr float SAFE_RANGE = 80.f;
constexpr unsigned WS_EPOCH = 0, WS_KVFLAG = 4096, WS_HFLAG = 8192;
constexpr unsigned WS_GZ = WS_HFLAG + 32768;
constexpr unsigned WS_KV = WS_GZ + 16384, KV_BYTES = 64 * 2048;
constexpr unsigned WS_H = WS_KV + KV_BYTES, H_BYTES = 64 * 112 * 1024;
constexpr unsigned WS_O = WS_H + H_BYTES, O_BYTES = 64 * 32 * 1024;
constexpr unsigned OOB = 0x7ff00000u;
constexpr unsigned L_STAGE = 40960;
constexpr unsigned L_KSTAGE = 12288, L_KWQ = 98304;
constexpr unsigned KVT = 2080;
constexpr unsigned L_WB = 64 * KVT;
constexpr unsigned L_QX = L_WB + 16384, L_QN = L_QX + 8192, L_KM = L_QN + 1024, L_TOUCH = L_KM + 256, LDS_BYTES = 163840;
static_assert(L_TOUCH + 256 <= LDS_BYTES, "lds map");
constexpr unsigned SPIN_CAP = 1u << 16;

DI unsigned cvtpk(float a, float b) { f32x2 v = {a, b}; return __builtin_bit_cast(unsigned, __builtin_convertvector(v, bf16x2)); }
DI float rbf(float x) { return __uint_as_float(cvtpk(x, x) << 16); }
DI s16x8 asfrag(u32x4 v) { return __builtin_bit_cast(s16x8, v); }
DI s16x8 lds_frag(lptr p) { return *(const LAS s16x8*)p; }
DI void lds_put(lptr p, u32x4 v) { *(LAS u32x4*)p = v; }
DI s16x4 lds_tr(lptr p) { return __builtin_amdgcn_ds_read_tr16_b64_v4i16((LAS s16x4*)p); }
DI s16x8 tr_frag2(lptr p, lptr q) { s16x4 lo = lds_tr(p), hi = lds_tr(q); return __builtin_shufflevector(lo, hi, 0, 1, 2, 3, 4, 5, 6, 7); }
DI s16x8 tr_frag(lptr p, int second) { return tr_frag2(p, p + second); }
DI float swap_add(float v) { auto r = __builtin_amdgcn_permlane32_swap(__float_as_uint(v), __float_as_uint(v), false, false); return __uint_as_float(r[0]) + __uint_as_float(r[1]); }
DI float swap_max(float v) { auto r = __builtin_amdgcn_permlane32_swap(__float_as_uint(v), __float_as_uint(v), false, false); return fmaxf(__uint_as_float(r[0]), __uint_as_float(r[1])); }
DI float dot2(unsigned a, unsigned b, float c) { return __builtin_amdgcn_fdot2_f32_bf16(__builtin_bit_cast(bf16x2, a), __builtin_bit_cast(bf16x2, b), c, false); }
DI unsigned relu_pk(unsigned u) { return __builtin_bit_cast(unsigned, __builtin_elementwise_max(__builtin_bit_cast(s16x2, u), (s16x2){0, 0})); }
template <int CTRL> DI float dpp(float v) { return __uint_as_float(__builtin_amdgcn_update_dpp(0, __float_as_uint(v), CTRL, 0xf, 0xf, true)); }
DI float row32_max(float v) {
    v = fmaxf(v, dpp<0xB1>(v)); v = fmaxf(v, dpp<0x4E>(v)); v = fmaxf(v, dpp<0x141>(v)); v = fmaxf(v, dpp<0x140>(v));
    auto r = __builtin_amdgcn_permlane16_swap(__float_as_uint(v), __float_as_uint(v), false, false);
    return fmaxf(__uint_as_float(r[0]), __uint_as_float(r[1]));
}
template <int HALF> DI u32x4 pack8(const f32x16& a, float m) {
    u32x4 v;
#pragma unroll
    for (int e = 0; e < 4; ++e) v[e] = cvtpk(a[8 * HALF + 2 * e] * m, a[8 * HALF + 2 * e + 1] * m);
    return v;
}
template <int HALF> DI u32x4 pack8_relu(const f32x16& a) {
    u32x4 v;
#pragma unroll
    for (int e = 0; e < 4; ++e) v[e] = relu_pk(cvtpk(a[8 * HALF + 2 * e], a[8 * HALF + 2 * e + 1]));
    return v;
}
template <int HALF> DI f32x16 pv_mma(s16x8 v, const f32x16& p, f32x16 c, int idx) {
    const auto ph = __builtin_shufflevector(p, p, 8 * HALF, 8 * HALF + 1, 8 * HALF + 2, 8 * HALF + 3, 8 * HALF + 4, 8 * HALF + 5, 8 * HALF + 6, 8 * HALF + 7);
    return __builtin_amdgcn_smfmac_f32_32x32x32_bf16(__builtin_bit_cast(bf16x8, v), __builtin_bit_cast(bf16x16, ph), c, idx, 0, 0);
}
DI void glds16(const void* sbase, unsigned voff, unsigned lds_base) {
    asm volatile("s_mov_b32 m0, %2\n\ts_nop 0\n\tglobal_load_lds_dwordx4 %0, %1" :: "v"(voff), "s"(sbase), "s"(lds_base) : "memory");
}
DI void glds16x2(const void* sbase, unsigned voff0, unsigned voff1, unsigned lds_base) {
    asm volatile("s_mov_b32 m0, %3\n\ts_nop 0\n\tglobal_load_lds_dwordx4 %0, %2\n\tglobal_load_lds_dwordx4 %1, %2 offset:1024" :: "v"(voff0), "v"(voff1), "s"(sbase), "s"(lds_base) : "memory");
}
DI void glds16x4(const void* sbase, unsigned v0, unsigned v1, unsigned v2, unsigned v3, unsigned lds_base) {
    asm volatile("s_mov_b32 m0, %5\n\ts_nop 0\n\tglobal_load_lds_dwordx4 %0, %4\n\tglobal_load_lds_dwordx4 %1, %4 offset:1024\n\t"
                 "global_load_lds_dwordx4 %2, %4 offset:2048\n\tglobal_load_lds_dwordx4 %3, %4 offset:3072" :: "v"(v0), "v"(v1), "v"(v2), "v"(v3), "s"(sbase), "s"(lds_base) : "memory");
}
DI void glds16x2_sc1(const void* sbase, unsigned voff, unsigned lds_base) {
    asm volatile("s_mov_b32 m0, %2\n\ts_nop 0\n\tglobal_load_lds_dwordx4 %0, %1 sc1\n\tglobal_load_lds_dwordx4 %0, %1 offset:1024 sc1" :: "v"(voff), "s"(sbase), "s"(lds_base) : "memory");
}
DI void touch8k(const void* sbase, int lane, unsigned lds_scratch) {
    asm volatile("s_mov_b32 m0, %2\n\ts_nop 0\n\tglobal_load_lds_dword %0, %1" :: "v"(lane * 128), "s"(sbase), "s"(lds_scratch) : "memory");
}
#define WAIT_V(N) asm volatile("s_waitcnt vmcnt(" #N ")" ::: "memory")
DI __amdgpu_buffer_rsrc_t mk_rsrc(const void* p, unsigned bytes) { return __builtin_amdgcn_make_buffer_rsrc((void*)p, 0, bytes, 0x00020000); }
DI u32x4 bload_sc1(__amdgpu_buffer_rsrc_t r, unsigned voff, unsigned soff) { return __builtin_amdgcn_raw_buffer_load_b128(r, voff, soff, 16); }
DI void bstore_sc1(u32x4 v, __amdgpu_buffer_rsrc_t r, unsigned voff) { __builtin_amdgcn_raw_buffer_store_b128(v, r, voff, 0, 16); }

struct BigFrag { s16x8 x0, x1, w0, w1; };
struct SmallFrag { s16x8 x, q; };

__global__ void __launch_bounds__(512, 2)
fused_kernel(const uint16_t* __restrict__ x, const uint16_t* __restrict__ wq, const uint16_t* __restrict__ wk, const uint16_t* __restrict__ wv,
             const uint16_t* __restrict__ wo, const uint16_t* __restrict__ w1, const uint16_t* __restrict__ w2, const uint16_t* __restrict__ gw,
             uint16_t* out, char* ws) {
    __shared__ __attribute__((aligned(16))) char smem[LDS_BYTES];
    const lptr lds = (lptr)smem;
    const unsigned lds0 = (unsigned)(uintptr_t)smem;
    const int tid = threadIdx.x, lane = tid & 63, w = __builtin_amdgcn_readfirstlane(tid >> 6);
    const int r = lane & 31, h = lane >> 5, g1 = (lane >> 4) & 1, q4 = (lane & 15) >> 2, p4 = lane & 3;
    const int b = blockIdx.x, bi = b >> 3;
    const int R = 2 * (b & 7) + (bi >> 4), cb = bi & 15, rh64 = cb & 1, hg = cb >> 1;
    const bool kvcu = cb >= 14, big = w < 4;
    const int row64 = R * 128 + rh64 * 64;
    const int rt = w & 1, hq = w >> 1, fh = hq & 1;

    asm volatile("" :: "s"(x), "s"(wq), "s"(wk), "s"(wv), "s"(wo), "s"(w1), "s"(w2), "s"(gw), "s"(out), "s"(ws));
    const unsigned w64_src = (unsigned)((lane >> 3) * 1024 + hg * 128 + (((lane & 7) ^ (g1 << 2)) << 4));
    const unsigned w64_src1 = w64_src + 8192 - 1024;
    const unsigned xv = (unsigned)((lane >> 3) * 1024 + (((lane & 7) ^ ((lane >> 4) & 3)) << 4)), xv1 = (xv ^ 64) + 8192 - 1024;
    const unsigned wv0 = (unsigned)((lane >> 4) * (DFF * 2) + cb * 256 + (((lane & 15) ^ (((lane >> 4) & 3) << 2)) << 4)), wv1 = wv0 + 4 * DFF * 2 - 1024;
    const int rot = (b + cb) & 7;
    if (!kvcu) {
#pragma unroll
        for (int s = 0; s < 2; ++s) {
            const int sr = (s + rot) & 7;
            glds16x2((const char*)x + R * 131072 + sr * 128 + w * 16384, xv, xv1, lds0 + s * L_STAGE + w * 2048);
            glds16x2((const char*)w1 + (size_t)(sr * 64 + w * 8) * (DFF * 2), wv0, wv1, lds0 + s * L_STAGE + 16384 + w * 2048);
            glds16((const char*)wq + sr * 65536 + w * 8192, w64_src, lds0 + s * L_STAGE + 32768 + w * 1024);
        }
    } else {
        const char* wkv = (const char*)((w & 2) ? wk : wv);
        const unsigned kvv = (w & 1) * 1024 + ((lane ^ ((w & 2) << 2)) << 4);
#pragma unroll 1
        for (int s = 0; s < 8; ++s) {
            glds16((const char*)x + (row64 + 8 * w) * 1024 + s * 128, xv ^ (rt << 6), lds0 + s * L_KSTAGE + w * 1024);
            if (!big) glds16(wkv + s * 2048, kvv, lds0 + s * L_KSTAGE + 8192 + (w & 3) * 1024);
        }
    }
    const unsigned eo = lane == 1 ? WS_HFLAG + (R * 16 + cb) * 128 : !kvcu || lane == 0 || lane > 6 ? WS_EPOCH : lane == 2 ? WS_KVFLAG + (row64 >> 6) * 128 : WS_GZ + row64 * 8 + (lane - 3) * 128;
    const unsigned epoch_v = __hip_atomic_load((gu32*)(ws + eo), RLX_AGENT);
    asm volatile("" ::: "memory");
    auto get_epoch = [&]() { unsigned e = epoch_v; PIN(e); return __builtin_amdgcn_readfirstlane(e) * 0x9E3779B1u + 0x7F4A7C15u; };
    const __amdgpu_buffer_rsrc_t rs_kv = mk_rsrc(ws + WS_KV, KV_BYTES), rs_h = mk_rsrc(ws + WS_H, H_BYTES), rs_o = mk_rsrc(ws + WS_O, O_BYTES);
    const __amdgpu_buffer_rsrc_t rs_kf = mk_rsrc(ws + WS_KVFLAG, 4096);
    const unsigned tr_in = 32 * g1 + 8 * p4;
    const unsigned wq_rd = (8 * h + q4) * 128 + ((fh ^ (q4 >> 1)) << 6) + tr_in;
    unsigned xo[4];
#pragma unroll
    for (int ks = 0; ks < 4; ++ks) xo[ks] = r * 128 + (((2 * ks + h) ^ ((r >> 1) & 7)) << 4);

    lptr kp, vp0, vp1, wrA0, wrA1, wrB0, wrB1, q_rd, qn_rd, km_rd, cst_wr;
    unsigned h_voff, hf_voff, g_voff0, g_voff1, hb, hb1, o_own;
    int spidx;
    f32x16 oacc;
    u32x4 cst, omark;
    s16x8 zf;
    auto consts = [&]() {
        kp = lds + lane * 16;
        const unsigned v_cst = (r & 27) != 16 ? 2064 : 2048;
        vp0 = lds + (r < 16 ? 1024 + h * 256 + r * 16 : v_cst); vp1 = lds + (r < 16 ? 1536 + h * 256 + r * 16 : v_cst);
        const unsigned w_rd0 = w * 2048 + (4 * h + q4) * 128 + ((q4 >> 1) << 6) + tr_in;
        wrA0 = lds + L_WB + w_rd0; wrA1 = lds + L_WB + (w_rd0 ^ 64); wrB0 = lds + w_rd0; wrB1 = lds + (w_rd0 ^ 64);
        q_rd = lds + L_QX + (hq * 2 + rt) * 1024 + lane * 16; qn_rd = lds + L_QN + (hq * 2 + rt) * 128 + r * 4; km_rd = lds + L_KM;
        h_voff = (unsigned)((row64 >> 5) * 112 + 14 * w) * 1024u + lane * 16;
        hf_voff = (unsigned)(R * 16 + (lane & 15)) * 128 + 4; g_voff0 = 32768 + (row64 + 32 * rt + r) * 8; g_voff1 = 32768 + (row64 + 8 * w + ((lane >> 1) & 4) + (lane >> 4)) * 8 + 4;
        spidx = 0xDDDD; oacc = (f32x16){}; zf = (s16x8){};
        cst = (lane & 1) ? (u32x4){} : (u32x4){0x3f803f80u, 0x3f803f80u, 0x3f803f80u, 0x3f803f80u};
        cst_wr = lds + (tid >> 1) * KVT + 2048 + (tid & 1) * 16;
        hb = (kvcu ? OOB : (unsigned)((R * 4 + 2 * rt) * 112 + cb * 8 + fh * 4) * 1024u) + lane * 16; hb1 = hb + 112 * 1024;
        o_own = (unsigned)(((row64 >> 5) + rt) * 32 + hg * 4 + hq) * 1024u + lane * 16;
        omark = (u32x4){~0u, ~0u, ~0u, ~0u};
        PIN(oacc); PIN(spidx); PIN(cst); PIN(cst_wr); PIN(hb); PIN(hb1); PIN(zf); PIN(o_own); PIN(omark);
        PIN(kp); PIN(vp0); PIN(vp1); PIN(wrA0); PIN(wrA1); PIN(wrB0); PIN(wrB1); PIN(q_rd); PIN(qn_rd); PIN(km_rd);
        PIN(h_voff); PIN(hf_voff); PIN(g_voff0); PIN(g_voff1);
        SBAR();
    };
    u32x4 hs[8];
#pragma unroll
    for (int i = 0; i < 8; ++i) asm volatile("" : "=v"(hs[i]));
    f32x16 a00 = {}, a01 = {}, a10 = {}, a11 = {};
    auto kv_wait = [&](u32x4& fv) {
        const unsigned ep = get_epoch();
        if (__builtin_expect(!__all(fv[1] == ep && fv[3] == ep), 0)) {
            unsigned spins = 0;
            do { __builtin_amdgcn_s_sleep(6); CFENCE(); fv = bload_sc1(rs_kf, r * 128 + h * 16, 0); } while (!__all(fv[1] == ep && fv[3] == ep) && ++spins < SPIN_CAP);
        }
        CFENCE();
    };
    auto kv_bound = [&](const u32x4& fv) {
        const float km = row32_max(fmaxf(__uint_as_float(fv[0]), __uint_as_float(fv[2])));
        if (tid == 256) *(LAS float*)(lds + L_KM) = __builtin_amdgcn_sqrtf(km) * 1.0001f;
    };
    auto kv_tile = [&](int t) { glds16x2_sc1(ws + WS_KV + ((t + 8 * bi) & 63) * 2048, lane * 16, lds0 + t * KVT); };
    if (!kvcu) {
        unsigned cur = 0, nxt = L_STAGE;
        if (big) {
            const unsigned xb = rt * 8192, w0o = 16384 + (8 * h + q4) * 256 + (((2 * fh) ^ q4) << 6) + tr_in, w1o = w0o ^ 64;
            BigFrag F[4];
            auto rd = [&](BigFrag& n, lptr sb, int ks) {
                n.x0 = lds_frag(sb + xb + xo[ks]); n.x1 = lds_frag(sb + xb + 4096 + xo[ks]); n.w0 = tr_frag(sb + w0o + ks * 4096, 1024); n.w1 = tr_frag(sb + w1o + ks * 4096, 1024); };
            auto step = [&](const BigFrag& c, BigFrag& n, lptr sb, int ks) {
                a00 = MFMA(c.w0, c.x0, a00); SBAR(); n.x0 = lds_frag(sb + xb + xo[ks]); n.x1 = lds_frag(sb + xb + 4096 + xo[ks]); SBAR();
                a10 = MFMA(c.w0, c.x1, a10); SBAR(); n.w0 = tr_frag(sb + w0o + ks * 4096, 1024); SBAR();
                a01 = MFMA(c.w1, c.x0, a01); SBAR(); n.w1 = tr_frag(sb + w1o + ks * 4096, 1024); SBAR();
                a11 = MFMA(c.w1, c.x1, a11); SBAR(); };
            auto stage = [&](auto bar) {
                step(F[0], F[3], lds + cur, 3);
                bar();
                step(F[1], F[0], lds + nxt, 0); step(F[2], F[1], lds + nxt, 1); step(F[3], F[2], lds + nxt, 2);
                cur = nxt; nxt = (nxt == 2 * L_STAGE) ? 0 : nxt + L_STAGE; };
            consts();
            BAR_VL(6);
            rd(F[0], lds, 0); rd(F[1], lds, 1); rd(F[2], lds, 2);
#pragma unroll 1
            for (int s = 0; s < 7; ++s) stage([&] {
                BAR_VL(1);
                if (s == 3 && w < 2) touch8k(w == 0 ? (const char*)w2 + (R * 14 + cb) * 8192 : (const char*)wo + (b & 63) * 8192, lane, lds0 + L_TOUCH); });
            rd(F[3], lds + cur, 3);
#define LAST4(acc, wm, xm) do { _Pragma("unroll") for (int ks = 0; ks < 4; ++ks) acc = MFMA(F[ks].wm, F[ks].xm, acc); } while (0)
#define LAST4P(acc, wm, xm, prev, i) do { acc = MFMA(F[0].wm, F[0].xm, acc); acc = MFMA(F[1].wm, F[1].xm, acc); SBAR(); hs[i] = pack8_relu<0>(prev); PIN(hs[i]); SBAR(); \
            acc = MFMA(F[2].wm, F[2].xm, acc); SBAR(); hs[(i) + 1] = pack8_relu<1>(prev); PIN(hs[(i) + 1]); SBAR(); acc = MFMA(F[3].wm, F[3].xm, acc); SBAR(); } while (0)
            LAST4(a00, w0, x0); SBAR();
            LAST4P(a01, w1, x0, a00, 0); LAST4P(a10, w0, x1, a01, 2); LAST4P(a11, w1, x1, a10, 4);
            hs[6] = pack8_relu<0>(a11); hs[7] = pack8_relu<1>(a11);
        } else {
            const int j = w - 4;
            __builtin_amdgcn_s_setprio(2);
            const unsigned xb = (2 * rh64 + rt) * 4096, qo = 32768 + wq_rd;
            SmallFrag F[4];
            auto rd = [&](SmallFrag& n, lptr sb, int ks) { n.x = lds_frag(sb + xb + xo[ks]); n.q = tr_frag(sb + qo + ks * 2048, 512); SBAR(); };
            auto mma = [&](const SmallFrag& c) { a00 = MFMA(c.q, c.x, a00); SBAR(); };
            const unsigned xv2 = xv + 2 * (8192 - 1024), xv3 = xv1 + 2 * (8192 - 1024), wv2 = wv0 + 2 * (4 * DFF * 2 - 1024), wv3 = wv1 + 2 * (4 * DFF * 2 - 1024);
            const char* xs = (const char*)x + R * 131072 + j * 32768;
            const char* w1s = (const char*)w1 + (size_t)(j * 16) * (DFF * 2);
            const char* wqs = (const char*)wq + j * 16384;
            auto fill0 = [&](int sr, unsigned slot) { glds16x4(xs + sr * 128, xv, xv1, xv2, xv3, lds0 + slot + j * 4096); };
            auto fill1 = [&](int sr, unsigned slot) { glds16x4(w1s + sr * (64 * DFF * 2), wv0, wv1, wv2, wv3, lds0 + slot + 16384 + j * 4096); };
            auto fill2 = [&](int sr, unsigned slot) { glds16x2(wqs + sr * 65536, w64_src, w64_src1, lds0 + slot + 32768 + j * 2048); };
            fill0((rot + 2) & 7, 2 * L_STAGE); fill1((rot + 2) & 7, 2 * L_STAGE); fill2((rot + 2) & 7, 2 * L_STAGE);
            consts();
            BAR_VL(16);
            rd(F[0], lds, 0); rd(F[1], lds, 1); rd(F[2], lds, 2);
#pragma unroll 1
            for (int s = 0; s < 6; ++s) {
                const bool more = s < 5;
                const int sr = (rot + s + 3) & 7;
                rd(F[3], lds + cur, 3); mma(F[0]);
                BAR_VL(10);
                if (more) fill0(sr, cur);
                rd(F[0], lds + nxt, 0); mma(F[1]);
                if (more) fill1(sr, cur);
                rd(F[1], lds + nxt, 1); mma(F[2]);
                if (more) fill2(sr, cur);
                rd(F[2], lds + nxt, 2); mma(F[3]);
                cur = nxt; nxt = (nxt == 2 * L_STAGE) ? 0 : nxt + L_STAGE;
            }
            u32x4 fv = bload_sc1(rs_kf, r * 128 + h * 16, 0);
            rd(F[3], lds, 3); mma(F[0]);
            BAR_VL(0);
            rd(F[0], lds + L_STAGE, 0); mma(F[1]);
            kv_wait(fv);
            kv_tile(j); kv_tile(4 + j);
            rd(F[1], lds + L_STAGE, 1); mma(F[2]);
            kv_tile(8 + j); kv_tile(12 + j);
            rd(F[2], lds + L_STAGE, 2); mma(F[3]); rd(F[3], lds + L_STAGE, 3);
            mma(F[0]); kv_bound(fv); mma(F[1]); mma(F[2]); mma(F[3]);
        }
    } else {
        const unsigned xb = rt * 4096, w64_src2 = w64_src + 2 * (8192 - 1024), w64_src3 = w64_src + 3 * (8192 - 1024);
        {
            const unsigned wkv_rd = 8192 + g1 * 2048 + (((8 * h + q4) * 32 + 8 * p4) ^ (g1 << 7));
#pragma unroll
            for (int g = 0; g < 2; ++g) {
                if (g == 0) { if (big) BAR_VL(5); else BAR_VL(9); } else BAR_VL(1);
                const lptr st = lds + (g * 4 + hq) * L_KSTAGE;
#pragma unroll
                for (int k = 0; k < 4; ++k) a00 = MFMA(tr_frag2(st + wkv_rd + k * 512, st + (wkv_rd ^ 128) + k * 512), lds_frag(st + xb + xo[k]), a00);
            }
        }
        BAR_L();
        {
            const lptr pw = lds + w * L_KSTAGE + 8192 + r * 128;
            const unsigned sz = (r & 7) ^ ((r >> 1) & 4);
#pragma unroll
            for (int i = 0; i < 4; ++i) *(LAS f32x4*)(pw + (((2 * i + h) ^ sz) << 4)) = (f32x4){a00[4 * i], a00[4 * i + 1], a00[4 * i + 2], a00[4 * i + 3]};
            a00 = (f32x16){};
        }
        BAR_L();
        if (big) {
            const unsigned kvo = (unsigned)((row64 >> 5) + rt) * 2048u + lane * 16;
            const lptr pr = lds + rt * L_KSTAGE + 8192;
            float n2 = 0.f;
            if (fh == 0) {
                const unsigned sz = (r & 7) ^ ((r >> 1) & 4);
                f32x4 lo = {}, hi = {};
#pragma unroll
                for (int i = 0; i < 4; ++i) { lo += *(const LAS f32x4*)(pr + i * 2 * L_KSTAGE + r * 128 + (((4 + h) ^ sz) << 4)); hi += *(const LAS f32x4*)(pr + i * 2 * L_KSTAGE + r * 128 + (((6 + h) ^ sz) << 4)); }
                const u32x4 v = {cvtpk(lo[0], lo[1]), cvtpk(lo[2], lo[3]), cvtpk(hi[0], hi[1]), cvtpk(hi[2], hi[3])};
                bstore_sc1(v, rs_kv, kvo);
                n2 = row32_max(swap_add(dot2(v[0], v[0], dot2(v[1], v[1], 0.f)) + dot2(v[2], v[2], dot2(v[3], v[3], 0.f))));
            } else {
                float t[8];
#pragma unroll
                for (int j = 0; j < 8; ++j) {
                    const lptr q = pr + ((lane >> 4) * 8 + j) * 128 + ((q4 ^ j ^ (g1 << 2)) << 4) + p4 * 4;
                    t[j] = (*(const LAS float*)q + *(const LAS float*)(q + 2 * L_KSTAGE)) + (*(const LAS float*)(q + 4 * L_KSTAGE) + *(const LAS float*)(q + 6 * L_KSTAGE));
                }
                bstore_sc1((u32x4){cvtpk(t[0], t[1]), cvtpk(t[2], t[3]), cvtpk(t[4], t[5]), cvtpk(t[6], t[7])}, rs_kv, kvo + 1024);
            }
            WAIT_V(0);
            const unsigned ep = get_epoch();
            if (lane == 0) __hip_atomic_store((gu64*)(ws + WS_KVFLAG) + (row64 >> 6) * 16 + w, ((unsigned long long)ep << 32) | __float_as_uint(n2), RLX_AGENT);
            u32x4 gf[16];
#pragma unroll
            for (int i = 0; i < 16; ++i) gf[i] = *(const u32x4*)(gw + i * 32 + (lane >> 4) * 8);
            consts();
            BAR_L();
            f32x4 z = {};
            const int zr = 16 * w + (lane & 15);
#pragma unroll
            for (int i = 0; i < 16; ++i)
                z = __builtin_amdgcn_mfma_f32_16x16x32_bf16(asfrag(gf[i]), lds_frag(lds + (i >> 1) * L_KSTAGE + zr * 128 + (((4 * (i & 1) + (lane >> 4)) ^ ((zr >> 1) & 7)) << 4)), z, 0, 0, 0);
            const float gt = rbf(__builtin_amdgcn_rcpf(1.f + __expf(-rbf(z[0])))), og = fmaxf(rbf(1.f - gt), 0x1p-40f);
            if (lane < 16) __hip_atomic_store((gu64*)(ws + WS_GZ) + row64 + zr, ((unsigned long long)__float_as_uint(og) << 32) | __float_as_uint(gt / og), RLX_AGENT);
            BAR_L();
        } else {
#pragma unroll
            for (int i = 0; i < 4; ++i) { const int p = i * 16 + (w - 4) * 4; glds16x4((const char*)wq + p * 8192, w64_src, w64_src1, w64_src2, w64_src3, lds0 + L_KWQ + p * 1024); }
            consts();
            BAR_VL(0);
            SmallFrag F[4];
            auto rd = [&](SmallFrag& n, int s, int k) { n.x = lds_frag(lds + s * L_KSTAGE + xb + xo[k]); n.q = tr_frag(lds + L_KWQ + s * 8192 + wq_rd + k * 2048, 512); SBAR(); };
#pragma unroll
            for (int k = 0; k < 4; ++k) rd(F[k], 0, k);
#pragma unroll 1
            for (int s = 1; s < 8; ++s) {
#pragma unroll
                for (int k = 0; k < 4; ++k) { a00 = MFMA(F[k].q, F[k].x, a00); SBAR(); rd(F[k], s, k); }
            }
            u32x4 fv = bload_sc1(rs_kf, r * 128 + h * 16, 0);
#pragma unroll
            for (int k = 0; k < 4; ++k) a00 = MFMA(F[k].q, F[k].x, a00);
            BAR_L();
            kv_wait(fv);
            kv_tile(w - 4); kv_tile(w); kv_tile(4 + w); kv_tile(8 + w);
            kv_bound(fv);
        }
    }
    if (big) {
        if (tid < 32) lds_put(cst_wr, cst);
    } else {
#pragma unroll
        for (int s = 0; s < 2; ++s) {
            const u32x4 v = s ? pack8<1>(a00, QSCALE) : pack8<0>(a00, QSCALE);
            lds_put(lds + L_QX + (4 * fh + 2 * s + rt) * 1024 + lane * 16, v);
            const float n2 = swap_add(dot2(v[0], v[0], dot2(v[1], v[1], 0.f)) + dot2(v[2], v[2], dot2(v[3], v[3], 0.f)));
            *(LAS float*)(lds + L_QN + (4 * fh + 2 * s + rt) * 128 + r * 4) = __builtin_amdgcn_sqrtf(n2);
        }
    }
    __builtin_amdgcn_s_setprio(3);
    BAR_VL(0);
    const s16x8 qf = lds_frag(q_rd);
    s16x8 kf = lds_frag(kp), vf0, vf1;
    float mshift = *(const LAS float*)qn_rd * *(const LAS float*)km_rd;
    vf0 = lds_frag(vp0);
    if (__builtin_expect(!__all(mshift <= 0.5f * SAFE_RANGE), 0)) {
        float m = -__builtin_inff();
#pragma unroll 1
        for (int t = 0; t < 64; t += 16) {
            u32x4 kk[16];
#pragma unroll
            for (int u = 0; u < 16; ++u) kk[u] = bload_sc1(rs_kv, lane * 16, (t + u) * 2048);
#pragma unroll
            for (int u = 0; u < 16; ++u) {
                const f32x16 s = MFMA(asfrag(kk[u]), qf, (f32x16){});
#pragma unroll
                for (int i = 0; i < 16; ++i) m = fmaxf(m, s[i]);
            }
        }
        mshift = swap_max(m);
    }
    f32x16 cinit;
#pragma unroll
    for (int i = 0; i < 16; ++i) cinit[i] = -mshift;
    PIN(cinit);
    f32x16 sA = MFMA(kf, qf, cinit), sB;
    kf = lds_frag(kp + KVT);
    if (tid >= 32 && tid < 128) lds_put(cst_wr, cst);

#define S1(S_, i) S_[i] = __builtin_amdgcn_exp2f(S_[i])
#define EX2(S_, i) do { S1(S_, i); S1(S_, (i) + 1); } while (0)
#define EX3(S_, i) do { S1(S_, i); S1(S_, (i) + 1); S1(S_, (i) + 2); } while (0)
#define EX4(S_, i) do { EX2(S_, i); EX2(S_, (i) + 2); } while (0)
#define PV1(SN, T) do { if ((T) >= 0) oacc = pv_mma<1>(vf1, SN, oacc, spidx); vf1 = lds_frag(vp1 + ((T) < 0 ? 0 : (T)) * KVT); SBAR(); } while (0)
#define QK(SN, T) do { SN = MFMA(kf, qf, cinit); kf = lds_frag(kp + ((T) + 2) * KVT); SBAR(); } while (0)
#define PV0(SC, T) do { oacc = pv_mma<0>(vf0, SC, oacc, spidx); vf0 = lds_frag(vp0 + ((T) + 1) * KVT); SBAR(); } while (0)
#define CUT(S_) do { PIN(S_); SBAR(); } while (0)
#define ITER0(SC, SN, T, T1, PR, MEM) do { PR; \
    EX2(SC, 0); CUT(SC); PV1(SN, T1); EX4(SC, 2); CUT(SC); QK(SN, T); EX4(SC, 6); S1(SC, 10); CUT(SC); PV0(SC, T); MEM; S1(SC, 11); EX4(SC, 12); CUT(SC); } while (0)
#define ITER1(SC, SN, T, PR, PRE, MEM, G1) do { PR; PRE; \
    EX2(SC, 0); CUT(SC); PV1(SN, T); EX4(SC, 2); CUT(SC); QK(SN, T); EX4(SC, 6); CUT(SC); PV0(SC, T); MEM; EX4(SC, 10); CUT(SC); G1; SBAR(); EX2(SC, 14); CUT(SC); } while (0)
#define ITERG(SC, SN, T, PR, MEM, G0, G1) do { PR; \
    EX2(SC, 0); CUT(SC); PV1(SN, T); EX3(SC, 2); CUT(SC); QK(SN, T); EX3(SC, 5); CUT(SC); G0; SBAR(); EX3(SC, 8); CUT(SC); PV0(SC, T); MEM; EX3(SC, 11); CUT(SC); G1; SBAR(); EX2(SC, 14); CUT(SC); } while (0)
#define NOP (void)0
#define PRIO(p) __builtin_amdgcn_s_setprio(p)
#define ITER2(T, P0, P1, M0, M1) do { ITER0(sA, sB, T, T, PRIO(P0), M0); ITER0(sB, sA, (T) + 1, (T) + 1, PRIO(P1), M1); } while (0)
#define ADV(n) do { kp += (n) * KVT; vp0 += (n) * KVT; vp1 += (n) * KVT; PIN(kp); PIN(vp0); PIN(vp1); } while (0)
    const char* const w2w = (const char*)w2 + w * (14 * 16384);
    const char* const wow = (const char*)wo + w * 65536;
    int pn = (row64 >> 6) % 7;
    const unsigned wbA = lds0 + L_WB + w * 2048, wbB = lds0 + w * 2048;
    const __amdgpu_buffer_rsrc_t rs_f = mk_rsrc(ws + WS_HFLAG, 32768 + 16384);
    u32x4 hA0, hA1, hB0, hB1;
    s16x8 wf0, wf1;
#define W_LD(src, buf) glds16x2(src, w64_src, w64_src1, buf)
#define H_ST(i) do { if (big) bstore_sc1(hs[i], rs_h, ((i) < 4 ? hb : hb1) + ((i) & 3) * 1024); } while (0)
    ITER0(sA, sB, 0, -1, PRIO(3), H_ST(0)); ITER0(sB, sA, 1, 1, NOP, H_ST(1); kv_tile(16 + w));
    ITER2(2, 3, 3, H_ST(2); kv_tile(24 + w), H_ST(3); kv_tile(32 + w));
    ITER2(4, 2, 2, H_ST(4); kv_tile(40 + w), H_ST(5); kv_tile(48 + w));
    ITER2(6, 2, 2, H_ST(6); kv_tile(56 + w), H_ST(7); W_LD(w2w + pn * 32768, wbA));
    ITER2(8, 1, 1, bstore_sc1(omark, rs_o, o_own), NOP);
    ITER2(10, 1, 1, NOP, NOP);
    ITER2(12, 0, 0, NOP, NOP);
    BAR_VL(0);
    const unsigned epoch = get_epoch();
    if (tid == 0) __hip_atomic_store((gu32*)(ws + WS_HFLAG) + (R * 16 + cb) * 32 + 1, epoch, RLX_AGENT);
    unsigned hfv;
    ITER2(14, 1, 3, W_LD(w2w + pn * 32768 + 16384, wbB); a00 = MFMA(zf, zf, (f32x16){}); PIN(zf); a01 = MFMA(zf, zf, (f32x16){}), PIN(zf); a10 = MFMA(zf, zf, (f32x16){}); PIN(zf); a11 = MFMA(zf, zf, (f32x16){}));
    ITER2(16, 2, 1, NOP, hfv = __builtin_amdgcn_raw_buffer_load_b32(rs_f, hf_voff, 0, 16));
    ITER0(sA, sB, 18, 18, PRIO(3), NOP);
    if (__builtin_expect(!__all(hfv == epoch), 0)) {
        unsigned spins = 0;
        do { __builtin_amdgcn_s_sleep(6); CFENCE(); hfv = __builtin_amdgcn_raw_buffer_load_b32(rs_f, hf_voff, 0, 16); } while (!__all(hfv == epoch) && ++spins < SPIN_CAP);
    }
    CFENCE();
    unsigned gr_u, og_u;
    ITER0(sB, sA, 19, 19, PRIO(2), gr_u = __builtin_amdgcn_raw_buffer_load_b32(rs_f, g_voff0, 0, 16); og_u = __builtin_amdgcn_raw_buffer_load_b32(rs_f, g_voff1, 0, 16);
          hA0 = bload_sc1(rs_h, h_voff, pn * 2048); hA1 = bload_sc1(rs_h, h_voff, pn * 2048 + 112 * 1024));
    ITER2(20, 1, 3, hB0 = bload_sc1(rs_h, h_voff, pn * 2048 + 1024), hB1 = bload_sc1(rs_h, h_voff, pn * 2048 + 113 * 1024));
    ADV(22);
    PIN(a00); PIN(a01); PIN(a10); PIN(a11);
#define GM(acc, wf, H) acc = MFMA(asfrag(H), wf, acc); SBAR()
#define GSTEP(SX, SY, T, R0, R1, H0, H1, LW, LH0, LH1) do { \
    ITER1(SX, SY, T, PRIO(3), WAIT_V(5); wf0 = tr_frag(R0, 1024); wf1 = tr_frag(R1, 1024), NOP, GM(a00, wf0, H0)); \
    ITERG(SY, SX, (T) + 1, PRIO(2), LW, GM(a01, wf1, H0); LH0, GM(a10, wf0, H1)); \
    ITER1(SX, SY, (T) + 2, PRIO(1), NOP, NOP, GM(a11, wf1, H1); LH1); } while (0)
#pragma unroll 1
    for (int n = 0; n < 4; ++n) {
        pn = pn == 6 ? 0 : pn + 1;
        const char* wsrc = w2w + pn * 32768;
        GSTEP(sA, sB, 0, wrA0, wrA1, hA0, hA1, W_LD(wsrc, wbA), hA0 = bload_sc1(rs_h, h_voff, pn * 2048), hA1 = bload_sc1(rs_h, h_voff, pn * 2048 + 112 * 1024));
        GSTEP(sB, sA, 3, wrB0, wrB1, hB0, hB1, W_LD(wsrc + 16384, wbB), hB0 = bload_sc1(rs_h, h_voff, pn * 2048 + 1024), hB1 = bload_sc1(rs_h, h_voff, pn * 2048 + 113 * 1024));
        ADV(6);
    }
    BAR_L();
    pn = pn == 6 ? 0 : pn + 1;
    const int pm = pn == 6 ? 0 : pn + 1;
    u32x4 hC0, hC1, hD0, hD1, hE0, hE1, hF0, hF1;
    ITER2(0, 3, 2, W_LD(w2w + pn * 32768, wbB + 16384); hC0 = bload_sc1(rs_h, h_voff, pn * 2048), hC1 = bload_sc1(rs_h, h_voff, pn * 2048 + 112 * 1024));
    ITER2(2, 1, 3, W_LD(w2w + pn * 32768 + 16384, wbB + 32768); hD0 = bload_sc1(rs_h, h_voff, pn * 2048 + 1024), hD1 = bload_sc1(rs_h, h_voff, pn * 2048 + 113 * 1024));
    ITER2(4, 2, 1, W_LD(w2w + pm * 32768, wbB + 49152); hE0 = bload_sc1(rs_h, h_voff, pm * 2048), hE1 = bload_sc1(rs_h, h_voff, pm * 2048 + 112 * 1024));
    ITER2(6, 3, 2, W_LD(w2w + pm * 32768 + 16384, wbB + 65536); hF0 = bload_sc1(rs_h, h_voff, pm * 2048 + 1024), hF1 = bload_sc1(rs_h, h_voff, pm * 2048 + 113 * 1024));
    const bool closer = b == 112 && w == 7;
    unsigned ef[4];
    auto ef_load = [&]() {
#pragma unroll
        for (int i = 0; i < 4; ++i) ef[i] = __builtin_amdgcn_raw_buffer_load_b32(mk_rsrc(ws + WS_HFLAG + i * 8192 + 4, closer ? 8188 : 0), lane * 128, 0, 16);
    };
    const unsigned xtra = w < 3 ? 5 * 16384 : L_QX - 3 * 2048;
    s16x8 wf2, wf3, wf4, wf5;
    ITER2(8, 1, 3, WAIT_V(14); wf0 = tr_frag(wrA0, 1024); wf1 = tr_frag(wrA1, 1024); W_LD(wow + 49152, wbB + xtra), wf2 = tr_frag(wrB0, 1024); wf3 = tr_frag(wrB1, 1024); ef_load());
    ITER2(10, 2, 1, wf4 = tr_frag(wrB0 + 16384, 1024); wf5 = tr_frag(wrB1 + 16384, 1024); PIN(wf0); PIN(wf1); W_LD(wow, wbA), PIN(wf2); PIN(wf3); W_LD(wow + 16384, wbB));
    ITER2(12, 3, 3, PIN(wf4); PIN(wf5); W_LD(wow + 32768, wbB + 16384), NOP);
    ITER2(14, 2, 2, NOP, NOP);
    ITER2(16, 1, 0, NOP, NOP);
#define GMA(acc, wf, H) asm volatile("v_mfma_f32_32x32x16_bf16 %0, %1, %2, %0" : "+v"(acc) : "v"(H), "v"(wf))
    PRIO(3);
    oacc = pv_mma<1>(vf1, sB, oacc, spidx);
    {
        u32x4 v = pack8<0>(oacc, __builtin_amdgcn_rcpf(oacc[8]) * __uint_as_float(gr_u));
#pragma unroll
        for (int e = 0; e < 4; ++e) v[e] = v[e] < 0xfffffffeu ? v[e] : 0xfffffffeu;
        bstore_sc1(v, rs_o, o_own);
        asm volatile("s_nop 1");
    }
    PRIO(0);
    GMA(a00, wf0, hA0); GMA(a01, wf1, hA0);
    WAIT_V(1);
    lptr wrE0 = wrB0 + 32768, wrE1 = wrB1 + 32768; PIN(wrE0); PIN(wrE1);
    const s16x8 wf6 = tr_frag(wrE0, 1024), wf7 = tr_frag(wrE1, 1024), wf8 = tr_frag(wrE0 + 16384, 1024), wf9 = tr_frag(wrE1 + 16384, 1024);
    SBAR();
    GMA(a10, wf0, hA1); GMA(a11, wf1, hA1);
    const s16x8 wfa = tr_frag(wrE0 + 32768, 1024), wfb = tr_frag(wrE1 + 32768, 1024);
    s16x8 wof[8];
    wof[0] = tr_frag(wrA0, 1024); wof[1] = tr_frag(wrA1, 1024);
    SBAR();
    GMA(a00, wf2, hB0); GMA(a01, wf3, hB0);
    wof[2] = tr_frag(wrB0, 1024); wof[3] = tr_frag(wrB1, 1024); wof[4] = tr_frag(wrB0 + 16384, 1024); wof[5] = tr_frag(wrB1 + 16384, 1024);
    SBAR();
    GMA(a10, wf2, hB1); GMA(a11, wf3, hB1);
    wof[6] = tr_frag(wrB0 + xtra, 1024); wof[7] = tr_frag(wrB1 + xtra, 1024);
    SBAR();
    GMA(a00, wf4, hC0); GMA(a01, wf5, hC0); GMA(a10, wf4, hC1); GMA(a11, wf5, hC1);
    const unsigned ob = (unsigned)((row64 >> 5) * 32 + 4 * w) * 1024u;
    u32x4 of[8];
    auto o_load = [&]() {
#pragma unroll
        for (int i = 0; i < 8; ++i) of[i] = bload_sc1(rs_o, lane * 16 + (i & 3) * 1024, ob + (i >> 2) * 32768);
    };
    auto o_ready = [&]() {
        unsigned m = 0;
#pragma unroll
        for (int i = 0; i < 8; ++i) { m = max(m, max(of[i][0], of[i][1])); m = max(m, max(of[i][2], of[i][3])); }
        return __all(m != ~0u);
    };
    BAR_VL(0);
#pragma unroll
    for (int i = 0; i < 8; ++i) PIN(wof[i]);
    o_load();
    SBAR();
    GMA(a00, wf6, hD0); GMA(a01, wf7, hD0); GMA(a10, wf6, hD1); GMA(a11, wf7, hD1);
    GMA(a00, wf8, hE0); GMA(a01, wf9, hE0); GMA(a10, wf8, hE1); GMA(a11, wf9, hE1);
    GMA(a00, wfa, hF0); GMA(a01, wfb, hF0); GMA(a10, wfa, hF1); GMA(a11, wfb, hF1);
    SBAR();
    const lptr prb = lds + (((w >> 2) * 2 + ((lane >> 2) & 1)) * 16 + 4 * (w & 3) + (lane >> 4)) * 2048 + ((lane >> 3) & 1) * 128 + p4 * 32;
    lptr pr0 = prb + g1 * 16, pr1 = prb + (g1 ^ 1) * 16;
    PIN(pr0); PIN(pr1);
    uint16_t* outp = out + (row64 + 8 * w + ((lane >> 1) & 4) + (lane >> 4)) * 512 + hg * 64 + (lane & 7) * 8;
    PIN(outp);
    SBAR();
    if (!o_ready()) {
        unsigned spins = 0;
        do { CFENCE(); o_load(); } while (!o_ready() && ++spins < SPIN_CAP);
    }
#define AMM(acc, wi, oi) GMA(acc, wof[wi], of[oi])
#define PUT(a, i, off) asm volatile("ds_write_addtid_b32 %0 offset:%4\n\tds_write_addtid_b32 %1 offset:%5\n\tds_write_addtid_b32 %2 offset:%6\n\tds_write_addtid_b32 %3 offset:%7" \
    :: "v"(a[4 * (i)]), "v"(a[4 * (i) + 1]), "v"(a[4 * (i) + 2]), "v"(a[4 * (i) + 3]), "n"((off) + (i) * 8192), "n"((off) + (i) * 8192 + 2048), "n"((off) + (i) * 8192 + 4096), "n"((off) + (i) * 8192 + 6144) : "memory")
#define SETM0(v, prev) asm volatile("s_mov_b32 m0, %1\n\ts_nop 10" : "+v"(prev) : "s"(v))
#define MM_PUT(acc, c, o, prev, m0v, off) do { AMM(acc, c, o); SETM0(m0v, prev); PUT(prev, 0, off); AMM(acc, 2 + (c), (o) + 1); PUT(prev, 1, off); \
    AMM(acc, 4 + (c), (o) + 2); PUT(prev, 2, off); AMM(acc, 6 + (c), (o) + 3); PUT(prev, 3, off); } while (0)
    const unsigned m0a = lds0 + w * 256, m0b = m0a + 63616;
    asm volatile("s_nop 1");
    AMM(a00, 0, 0); AMM(a00, 2, 1); AMM(a00, 4, 2); AMM(a00, 6, 3);
    MM_PUT(a01, 1, 0, a00, m0a, 0);
    MM_PUT(a10, 0, 4, a01, m0a, 32768);
    MM_PUT(a11, 1, 4, a10, m0b, 65536 - 63616);
    asm volatile("s_nop 7" : "+v"(a11));
    PUT(a11, 0, 98304 - 63616); PUT(a11, 1, 98304 - 63616); PUT(a11, 2, 98304 - 63616); PUT(a11, 3, 98304 - 63616);
    BAR_L();
    {
        f32x4 t[16];
#pragma unroll
        for (int k = 0; k < 8; ++k) { t[2 * k] = *(const LAS f32x4*)(pr0 + k * 256); t[2 * k + 1] = *(const LAS f32x4*)(pr1 + k * 256); }
        SBAR();
        f32x4 lo = t[0], hi = t[1];
#pragma unroll
        for (int k = 1; k < 8; ++k) { lo += t[2 * k]; hi += t[2 * k + 1]; }
        lo *= __uint_as_float(og_u); hi *= __uint_as_float(og_u);
        const unsigned l0 = cvtpk(lo[0], lo[1]), l1 = cvtpk(lo[2], lo[3]), h0 = cvtpk(hi[0], hi[1]), h1 = cvtpk(hi[2], hi[3]);
        const u32x4 v = g1 ? (u32x4){h0, h1, l0, l1} : (u32x4){l0, l1, h0, h1};
        __builtin_nontemporal_store(v, (GAS u32x4*)outp);
    }
    if (closer) {
        unsigned spins = 0;
        while (!__all(ef[0] == epoch && ef[1] == epoch && ef[2] == epoch && ef[3] == epoch) && ++spins < SPIN_CAP) { CFENCE(); ef_load(); }
        if (lane == 0) __hip_atomic_store((gu32*)(ws + WS_EPOCH), epoch, RLX_AGENT);
    }
}

extern "C" void kernel_launch(void* const* d_in, const int* in_sizes, int n_in, void* d_out, int out_size, void* d_ws, size_t ws_size,
                              hipStream_t stream) {
    fused_kernel<<<256, 512, 0, stream>>>((const uint16_t*)d_in[0], (const uint16_t*)d_in[1], (const uint16_t*)d_in[2], (const uint16_t*)d_in[3],
                                          (const uint16_t*)d_in[4], (const uint16_t*)d_in[6], (const uint16_t*)d_in[7], (const uint16_t*)d_in[8],
                                          (uint16_t*)d_out, (char*)d_ws);
}
```
